```python
import torch

def _softmax(x, axis=-1):
    return torch.softmax(x, dim=axis)
def _gelu(x):
    return torch.nn.functional.gelu(x, approximate="tanh")
def _silu(x):
    return torch.nn.functional.silu(x)
def _sigmoid(x):
    return torch.sigmoid(x)
def _layernorm(x, axis=-1, eps=1e-5):
    m = x.mean(dim=axis, keepdim=True)
    v = x.var(dim=axis, keepdim=True, unbiased=False)
    return (x - m) / torch.sqrt(v + eps)
def _scatter_add(tgt, idx, src, axis=0):
    return tgt.clone().index_add_(axis, idx, src)
def _segment_sum(src, seg, n):
    out = torch.zeros((n,) + src.shape[1:], dtype=src.dtype)
    return out.index_add_(0, seg, src)
def _pad(x, pw):
    flat = tuple(v for pair in reversed(pw) for v in pair)
    return torch.nn.functional.pad(x, flat)

kernel_name = 'recurrent_scan__mha_block_cached__geglu_ffn__decode'

def setup_inputs():

    _w = (lambda a: (lambda t: t.to(torch.bfloat16) if t.is_floating_point() else t)(torch.as_tensor(a)))
    def _A_setup():
        B, Sq, D = 2, 1, 640
        H, Sk, Dh = 32, 4096, 192
        return {
            "x": _w((lambda *s: torch.randn(*s, dtype=torch.bfloat16) * 0.05)(B, Sq, D)),
            "k_cache": _w((lambda *s: torch.randn(*s, dtype=torch.bfloat16) * 1.6)(B, H, Sk, Dh)),
            "v_cache": _w((lambda *s: torch.randn(*s, dtype=torch.bfloat16) * 0.2)(B, H, Sk, Dh)),
            "wq": _w((lambda *s: torch.randn(*s, dtype=torch.bfloat16) * 1.6)(D, H * Dh) * (D ** -0.5)),
            "wo": _w((lambda *s: torch.randn(*s, dtype=torch.bfloat16) * 0.2)(H * Dh, D) * ((H * Dh) ** -0.5)),
        }
    _A_in = _A_setup()

    def _B_setup():
        B, S, D, F = 2, 1, 640, 1536
        return {
            "x": _w((lambda *s: torch.randn(*s, dtype=torch.bfloat16) * 0.05)(B, S, D)),
            "w_gate": _w((lambda *s: torch.randn(*s, dtype=torch.bfloat16) * 0.1)(D, F)),
            "w_up": _w((lambda *s: torch.randn(*s, dtype=torch.bfloat16) * 0.1)(D, F)),
            "w_down": _w((lambda *s: torch.randn(*s, dtype=torch.bfloat16) * 0.1)(F, D)),
        }
    _B_in = _B_setup()

    return {
        "A_x": _A_in["x"],
        "A_k_cache": _A_in["k_cache"],
        "A_v_cache": _A_in["v_cache"],
        "A_wq": _A_in["wq"],
        "A_wo": _A_in["wo"],
        "B_x": _B_in["x"],
        "B_w_gate": _B_in["w_gate"],
        "B_w_up": _B_in["w_up"],
        "B_w_down": _B_in["w_down"],
    }

def reference(A_x, A_k_cache, A_v_cache, A_wq, A_wo, B_x, B_w_gate, B_w_up, B_w_down):
    def _A_ref(x, k_cache, v_cache, wq, wo):
        B, Sq, D = x.shape
        H, Dh = 32, 192
        q = (lambda x, *d: x.permute(*d))((x @ wq).reshape(B, Sq, H, Dh), 0, 2, 1, 3)
        scores = torch.einsum("bhqd,bhkd->bhqk", q, k_cache) * (Dh ** -0.5)
        p = _softmax(scores, axis=-1)
        o = torch.einsum("bhqk,bhkd->bhqd", p, v_cache)
        return (lambda x, *d: x.permute(*d))(o, 0, 2, 1, 3).reshape(B, Sq, H * Dh) @ wo

    def _B_ref(x, w_gate, w_up, w_down):
        return (_gelu(x @ w_gate) * (x @ w_up)) @ w_down

    x = A_x
    xn = _layernorm(x, axis=-1)
    s = _A_ref(xn, A_k_cache, A_v_cache, A_wq, A_wo)
    g = _silu(_B_ref(xn, B_w_gate, B_w_up, B_w_down))
    return x + s * g
```

```cpp
#include <hip/hip_runtime.h>
#include <hip/hip_bf16.h>
#include <stdint.h>

#define KV_NT 1
#define ROUND_SCORES 0

typedef unsigned v4u __attribute__((ext_vector_type(4)));
typedef __bf16 bf16x2 __attribute__((ext_vector_type(2)));
typedef unsigned short u16;

constexpr int B = 2, D = 640, H = 32, SK = 4096, DH = 192, F = 1536;
constexpr int NSPLIT = 4, KPS = SK / NSPLIT;

__device__ __forceinline__ float bf2f(u16 v) { return __uint_as_float((unsigned)v << 16); }
__device__ __forceinline__ float rbf(float f) { return (float)(__bf16)f; }
__device__ __forceinline__ u16 f2bf(float f) { return __builtin_bit_cast(u16, (__bf16)f); }
__device__ __forceinline__ float dot2(unsigned a, unsigned b, float acc) {
  return __builtin_amdgcn_fdot2_f32_bf16(__builtin_bit_cast(bf16x2, a), __builtin_bit_cast(bf16x2, b), acc, false);
}
template <int CTRL> __device__ __forceinline__ float dpp(float x) {
  return __builtin_bit_cast(float, __builtin_amdgcn_mov_dpp(__builtin_bit_cast(int, x), CTRL, 0xf, 0xf, true));
}
constexpr int XOR1 = 0xB1, XOR2 = 0x4E, XOR7 = 0x141;

__device__ void layernorm_to_lds(const u16* x, float* xn  , int tid, int nthr) {
  int wave = tid >> 6, lane = tid & 63;
  if (wave < 2) {
    float v[10];
    float s = 0.f;
    for (int i = 0; i < 10; ++i) { v[i] = bf2f(x[wave * D + lane + 64 * i]); s += v[i]; }
    for (int m = 32; m >= 1; m >>= 1) s += __shfl_xor(s, m);
    float mean = s * (1.f / D);
    float q = 0.f;
    for (int i = 0; i < 10; ++i) { float d = v[i] - mean; q += d * d; }
    for (int m = 32; m >= 1; m >>= 1) q += __shfl_xor(q, m);
    float var = rbf(q * (1.f / D));
    float mb = rbf(mean);
    float den = rbf(sqrtf(rbf(var + 1e-5f)));
    for (int i = 0; i < 10; ++i) xn[wave * D + lane + 64 * i] = rbf(rbf(v[i] - mb) / den);
  }
  __syncthreads();
}

__global__ void __launch_bounds__(512) k1(const u16* x, const u16* wq, const u16* wg, const u16* wu, u16* q, float* hbuf) {
  __shared__ float xn[2 * D];
  __shared__ float red[8][4][128];
  int tid = threadIdx.x, wave = tid >> 6, lane = tid & 63;
  layernorm_to_lds(x, xn, tid, 512);
  int blk = blockIdx.x;
  if (blk < 48) {
    int n0 = blk * 128 + lane * 2;
    float a00 = 0, a01 = 0, a10 = 0, a11 = 0;
    for (int k = wave * 80; k < wave * 80 + 80; ++k) {
      unsigned w = *(const unsigned*)(wq + (size_t)k * (H * DH) + n0);
      float w0 = __uint_as_float(w << 16), w1 = __uint_as_float(w & 0xffff0000u);
      a00 += xn[k] * w0; a01 += xn[k] * w1; a10 += xn[D + k] * w0; a11 += xn[D + k] * w1;
    }
    red[wave][0][lane * 2] = a00; red[wave][0][lane * 2 + 1] = a01; red[wave][1][lane * 2] = a10; red[wave][1][lane * 2 + 1] = a11;
    __syncthreads();
    if (tid < 256) {
      int b = tid >> 7, c = tid & 127; float s = 0;
      for (int w = 0; w < 8; ++w) s += red[w][b][c];
      q[b * (H * DH) + blk * 128 + c] = f2bf(s);
    }
  } else {
    int fb = blk - 48;
    int n0 = fb * 128 + lane * 2;
    float g00 = 0, g01 = 0, g10 = 0, g11 = 0, u00 = 0, u01 = 0, u10 = 0, u11 = 0;
    for (int k = wave * 80; k < wave * 80 + 80; ++k) {
      unsigned w = *(const unsigned*)(wg + (size_t)k * F + n0);
      unsigned u = *(const unsigned*)(wu + (size_t)k * F + n0);
      float w0 = __uint_as_float(w << 16), w1 = __uint_as_float(w & 0xffff0000u);
      float v0 = __uint_as_float(u << 16), v1 = __uint_as_float(u & 0xffff0000u);
      g00 += xn[k] * w0; g01 += xn[k] * w1; g10 += xn[D + k] * w0; g11 += xn[D + k] * w1;
      u00 += xn[k] * v0; u01 += xn[k] * v1; u10 += xn[D + k] * v0; u11 += xn[D + k] * v1;
    }
    red[wave][0][lane * 2] = g00; red[wave][0][lane * 2 + 1] = g01; red[wave][1][lane * 2] = g10; red[wave][1][lane * 2 + 1] = g11;
    red[wave][2][lane * 2] = u00; red[wave][2][lane * 2 + 1] = u01; red[wave][3][lane * 2] = u10; red[wave][3][lane * 2 + 1] = u11;
    __syncthreads();
    if (tid < 256) {
      int b = tid >> 7, c = tid & 127; float g = 0, u = 0;
      for (int w = 0; w < 8; ++w) { g += red[w][b][c]; u += red[w][2 + b][c]; }
      g = rbf(g); u = rbf(u);
      float t = 0.7978845608028654f * (g + 0.044715f * g * g * g);
      float ge = rbf(0.5f * g * (1.f + tanhf(t)));
      hbuf[b * F + fb * 128 + c] = rbf(ge * u);
    }
  }
}

__device__ __forceinline__ v4u ldkv(const u16* p) {
  return __builtin_nontemporal_load((const v4u*)p);
}

constexpr int NW = 8, U = 2;
__global__ void __launch_bounds__(NW * 64) k2(const u16* q, const u16* kc, const u16* vc, float* part  ) {
  __shared__ float sm[NW * 8][DH + 4];
  int tid = threadIdx.x, wave = tid >> 6, lane = tid & 63, g = lane >> 3, c = lane & 7;
  int blk = blockIdx.x; int bh = blk >> 2, sp = blk & 3;
  v4u qf[3];
  for (int j = 0; j < 3; ++j) qf[j] = *(const v4u*)(q + (size_t)bh * DH + j * 64 + c * 8);
  const size_t base = ((size_t)bh * SK + sp * KPS) * DH;
  float m = -INFINITY, l = 0.f, acc[24];
  for (int i = 0; i < 24; ++i) acc[i] = 0.f;
  constexpr int NIT = KPS / (NW * 8 * U);
  v4u kn[U][3], vn[U][3];
  auto issue = [&](int it) {
#pragma unroll
    for (int u = 0; u < U; ++u) {
      int key = (it * NW + wave) * 8 * U + u * 8 + g;
#pragma unroll
      for (int j = 0; j < 3; ++j) kn[u][j] = ldkv(kc + base + (size_t)key * DH + j * 64 + c * 8);
    }
#pragma unroll
    for (int u = 0; u < U; ++u) {
      int key = (it * NW + wave) * 8 * U + u * 8 + g;
#pragma unroll
      for (int j = 0; j < 3; ++j) vn[u][j] = ldkv(vc + base + (size_t)key * DH + j * 64 + c * 8);
    }
  };
  issue(0);
  const float SC = 0.07216878364870322f;
  const float L2E = 1.4426950408889634f;
  for (int it = 0; it < NIT; ++it) {
    v4u kk[U][3], vv[U][3];
#pragma unroll
    for (int u = 0; u < U; ++u)
#pragma unroll
      for (int j = 0; j < 3; ++j) { kk[u][j] = kn[u][j]; vv[u][j] = vn[u][j]; }
    if (it + 1 < NIT) issue(it + 1);
    float s[U];
#pragma unroll
    for (int u = 0; u < U; ++u) {
      float d = 0.f;
#pragma unroll
      for (int j = 0; j < 3; ++j)
#pragma unroll
        for (int e = 0; e < 4; ++e) d = dot2(kk[u][j][e], qf[j][e], d);
      d += dpp<XOR1>(d); d += dpp<XOR2>(d); d += dpp<XOR7>(d);
      s[u] = d * (SC * L2E);
    }
    float mn = m;
#pragma unroll
    for (int u = 0; u < U; ++u) mn = fmaxf(mn, s[u]);
    float alpha = __builtin_amdgcn_exp2f(m - mn);
    m = mn;
    l *= alpha;
#pragma unroll
    for (int i = 0; i < 24; ++i) acc[i] *= alpha;
#pragma unroll
    for (int u = 0; u < U; ++u) {
      float p = __builtin_amdgcn_exp2f(s[u] - mn);
      l += p;
#pragma unroll
      for (int j = 0; j < 3; ++j)
#pragma unroll
        for (int e = 0; e < 4; ++e) {
          acc[j * 8 + 2 * e] += p * __uint_as_float(vv[u][j][e] << 16);
          acc[j * 8 + 2 * e + 1] += p * __uint_as_float(vv[u][j][e] & 0xffff0000u);
        }
    }
  }
  float* rec = sm[wave * 8 + g];
#pragma unroll
  for (int j = 0; j < 3; ++j)
#pragma unroll
    for (int e = 0; e < 8; ++e) rec[j * 64 + c * 8 + e] = acc[j * 8 + e];
  if (c == 0) { rec[DH] = m; rec[DH + 1] = l; }
  __syncthreads();
  if (tid < DH) {
    float M = -INFINITY;
    for (int r = 0; r < NW * 8; ++r) M = fmaxf(M, sm[r][DH]);
    float L = 0.f, o = 0.f;
    for (int r = 0; r < NW * 8; ++r) { float w = __builtin_amdgcn_exp2f(sm[r][DH] - M); L += w * sm[r][DH + 1]; o += w * sm[r][tid]; }
    part[blk * 200 + tid] = o;
    if (tid == 0) { part[blk * 200 + DH] = M; part[blk * 200 + DH + 1] = L; }
  }
}

__global__ void __launch_bounds__(320) k3(const float* part, const u16* wo, const float* hbuf, const u16* wd, float* ypart) {
  __shared__ float o[2][DH];
  int tid = threadIdx.x, blk = blockIdx.x;
  const u16* w;
  if (blk < 32) {
    for (int i = tid; i < 2 * DH; i += 320) {
      int b = i / DH, d = i % DH; const float* p = part + (size_t)((b * H + blk) * 4) * 200;
      float M = -INFINITY;
      for (int s = 0; s < 4; ++s) M = fmaxf(M, p[s * 200 + DH]);
      float L = 0, a = 0;
      for (int s = 0; s < 4; ++s) { float ww = __builtin_amdgcn_exp2f(p[s * 200 + DH] - M); L += ww * p[s * 200 + DH + 1]; a += ww * p[s * 200 + d]; }
      o[b][d] = rbf(a / L);
    }
    w = wo + (size_t)blk * DH * D;
  } else {
    for (int i = tid; i < 2 * DH; i += 320) { int b = i / DH, d = i % DH; o[b][d] = hbuf[b * F + (blk - 32) * DH + d]; }
    w = wd + (size_t)(blk - 32) * DH * D;
  }
  __syncthreads();
  float a00 = 0, a01 = 0, a10 = 0, a11 = 0;
#pragma unroll 16
  for (int k = 0; k < DH; ++k) {
    unsigned ww = *(const unsigned*)(w + (size_t)k * D + tid * 2);
    float w0 = __uint_as_float(ww << 16), w1 = __uint_as_float(ww & 0xffff0000u);
    a00 += o[0][k] * w0; a01 += o[0][k] * w1; a10 += o[1][k] * w0; a11 += o[1][k] * w1;
  }
  float* y = ypart + (size_t)blk * 2 * D;
  y[tid * 2] = a00; y[tid * 2 + 1] = a01; y[D + tid * 2] = a10; y[D + tid * 2 + 1] = a11;
}

__global__ void __launch_bounds__(256) k4(const float* ypart, const u16* x, u16* out) {
  int i = blockIdx.x * 256 + threadIdx.x;
  if (i >= 2 * D) return;
  float s = 0, gp = 0;
  for (int h = 0; h < 32; ++h) s += ypart[(size_t)h * 2 * D + i];
  for (int h = 32; h < 40; ++h) gp += ypart[(size_t)h * 2 * D + i];
  s = rbf(s); gp = rbf(gp);
  float g = rbf(gp / (1.f + __expf(-gp)));
  out[i] = f2bf(bf2f(x[i]) + rbf(s * g));
}

extern "C" void kernel_launch(void* const* d_in, const int* in_sizes, int n_in, void* d_out, int out_size, void* d_ws, size_t ws_size,
                              hipStream_t stream) {
  const u16* x = (const u16*)d_in[0];
  const u16* kc = (const u16*)d_in[1];
  const u16* vc = (const u16*)d_in[2];
  const u16* wq = (const u16*)d_in[3];
  const u16* wo = (const u16*)d_in[4];
  const u16* wg = (const u16*)d_in[6];
  const u16* wu = (const u16*)d_in[7];
  const u16* wd = (const u16*)d_in[8];
  char* ws = (char*)d_ws;
  u16* q = (u16*)ws;
  float* hbuf = (float*)(ws + 32768);
  float* part = (float*)(ws + 65536);
  float* ypart = (float*)(ws + 65536 + 262144);
  k1<<<60, 512, 0, stream>>>(x, wq, wg, wu, q, hbuf);
  k2<<<256, NW * 64, 0, stream>>>(q, kc, vc, part);
  k3<<<40, 320, 0, stream>>>(part, wo, hbuf, wd, ypart);
  k4<<<5, 256, 0, stream>>>(ypart, x, (u16*)d_out);
}
```

```cpp
#define METRIC 0
#include <hip/hip_runtime.h>
#include <hip/hip_bf16.h>
#include <stdint.h>

#define KV_NT 1
#define ROUND_SCORES 1

typedef unsigned v4u __attribute__((ext_vector_type(4)));
typedef __bf16 bf16x2 __attribute__((ext_vector_type(2)));
typedef unsigned short u16;

constexpr int B = 2, D = 640, H = 32, SK = 4096, DH = 192, F = 1536;
constexpr int NSPLIT = 4, KPS = SK / NSPLIT;

__device__ __forceinline__ float bf2f(u16 v) { return __uint_as_float((unsigned)v << 16); }
__device__ __forceinline__ float rbf(float f) { return (float)(__bf16)f; }
__device__ __forceinline__ u16 f2bf(float f) { return __builtin_bit_cast(u16, (__bf16)f); }
__device__ __forceinline__ float dot2(unsigned a, unsigned b, float acc) {
  return __builtin_amdgcn_fdot2_f32_bf16(__builtin_bit_cast(bf16x2, a), __builtin_bit_cast(bf16x2, b), acc, false);
}
template <int CTRL> __device__ __forceinline__ float dpp(float x) {
  return __builtin_bit_cast(float, __builtin_amdgcn_mov_dpp(__builtin_bit_cast(int, x), CTRL, 0xf, 0xf, true));
}
constexpr int XOR1 = 0xB1, XOR2 = 0x4E, XOR7 = 0x141;

__device__ void layernorm_to_lds(const u16* x, float* xn  , int tid, int nthr) {
  int wave = tid >> 6, lane = tid & 63;
  if (wave < 2) {
    float v[10];
    float s = 0.f;
    for (int i = 0; i < 10; ++i) { v[i] = bf2f(x[wave * D + lane + 64 * i]); s += v[i]; }
    for (int m = 32; m >= 1; m >>= 1) s += __shfl_xor(s, m);
    float mean = s * (1.f / D);
    float q = 0.f;
    for (int i = 0; i < 10; ++i) { float d = v[i] - mean; q += d * d; }
    for (int m = 32; m >= 1; m >>= 1) q += __shfl_xor(q, m);
    float var = rbf(q * (1.f / D));
    float mb = rbf(mean);
    float den = rbf(sqrtf(rbf(var + 1e-5f)));
    for (int i = 0; i < 10; ++i) xn[wave * D + lane + 64 * i] = rbf(rbf(v[i] - mb) / den);
  }
  __syncthreads();
}

__global__ void __launch_bounds__(512) k1(const u16* x, const u16* wq, const u16* wg, const u16* wu, u16* q, float* hbuf) {
  __shared__ float xn[2 * D];
  __shared__ float red[8][4][128];
  int tid = threadIdx.x, wave = tid >> 6, lane = tid & 63;
  layernorm_to_lds(x, xn, tid, 512);
  int blk = blockIdx.x;
  if (blk < 48) {
    int n0 = blk * 128 + lane * 2;
    float a00 = 0, a01 = 0, a10 = 0, a11 = 0;
    for (int k = wave * 80; k < wave * 80 + 80; ++k) {
      unsigned w = *(const unsigned*)(wq + (size_t)k * (H * DH) + n0);
      float w0 = __uint_as_float(w << 16), w1 = __uint_as_float(w & 0xffff0000u);
      a00 += xn[k] * w0; a01 += xn[k] * w1; a10 += xn[D + k] * w0; a11 += xn[D + k] * w1;
    }
    red[wave][0][lane * 2] = a00; red[wave][0][lane * 2 + 1] = a01; red[wave][1][lane * 2] = a10; red[wave][1][lane * 2 + 1] = a11;
    __syncthreads();
    if (tid < 256) {
      int b = tid >> 7, c = tid & 127; float s = 0;
      for (int w = 0; w < 8; ++w) s += red[w][b][c];
      q[b * (H * DH) + blk * 128 + c] = f2bf(s);
    }
  } else {
    int fb = blk - 48;
    int n0 = fb * 128 + lane * 2;
    float g00 = 0, g01 = 0, g10 = 0, g11 = 0, u00 = 0, u01 = 0, u10 = 0, u11 = 0;
    for (int k = wave * 80; k < wave * 80 + 80; ++k) {
      unsigned w = *(const unsigned*)(wg + (size_t)k * F + n0);
      unsigned u = *(const unsigned*)(wu + (size_t)k * F + n0);
      float w0 = __uint_as_float(w << 16), w1 = __uint_as_float(w & 0xffff0000u);
      float v0 = __uint_as_float(u << 16), v1 = __uint_as_float(u & 0xffff0000u);
      g00 += xn[k] * w0; g01 += xn[k] * w1; g10 += xn[D + k] * w0; g11 += xn[D + k] * w1;
      u00 += xn[k] * v0; u01 += xn[k] * v1; u10 += xn[D + k] * v0; u11 += xn[D + k] * v1;
    }
    red[wave][0][lane * 2] = g00; red[wave][0][lane * 2 + 1] = g01; red[wave][1][lane * 2] = g10; red[wave][1][lane * 2 + 1] = g11;
    red[wave][2][lane * 2] = u00; red[wave][2][lane * 2 + 1] = u01; red[wave][3][lane * 2] = u10; red[wave][3][lane * 2 + 1] = u11;
    __syncthreads();
    if (tid < 256) {
      int b = tid >> 7, c = tid & 127; float g = 0, u = 0;
      for (int w = 0; w < 8; ++w) { g += red[w][b][c]; u += red[w][2 + b][c]; }
      g = rbf(g); u = rbf(u);
      float t = 0.7978845608028654f * (g + 0.044715f * g * g * g);
      float ge = rbf(0.5f * g * (1.f + tanhf(t)));
      hbuf[b * F + fb * 128 + c] = rbf(ge * u);
    }
  }
}

__device__ __forceinline__ v4u ldkv(const u16* p) {
  return __builtin_nontemporal_load((const v4u*)p);
}

constexpr int NW = 8, U = 2;
__global__ void __launch_bounds__(NW * 64) k2(const u16* q, const u16* kc, const u16* vc, float* part  ) {
  __shared__ float sm[NW * 8][DH + 4];
  int tid = threadIdx.x, wave = tid >> 6, lane = tid & 63, g = lane >> 3, c = lane & 7;
  int blk = blockIdx.x; int bh = blk >> 2, sp = blk & 3;
  v4u qf[3];
  for (int j = 0; j < 3; ++j) qf[j] = *(const v4u*)(q + (size_t)bh * DH + j * 64 + c * 8);
  const size_t base = ((size_t)bh * SK + sp * KPS) * DH;
  float m = -INFINITY, l = 0.f, acc[24];
  for (int i = 0; i < 24; ++i) acc[i] = 0.f;
  constexpr int NIT = KPS / (NW * 8 * U);
  v4u kn[U][3], vn[U][3];
  auto issue = [&](int it) {
#pragma unroll
    for (int u = 0; u < U; ++u) {
      int key = (it * NW + wave) * 8 * U + u * 8 + g;
#pragma unroll
      for (int j = 0; j < 3; ++j) kn[u][j] = ldkv(kc + base + (size_t)key * DH + j * 64 + c * 8);
    }
#pragma unroll
    for (int u = 0; u < U; ++u) {
      int key = (it * NW + wave) * 8 * U + u * 8 + g;
#pragma unroll
      for (int j = 0; j < 3; ++j) vn[u][j] = ldkv(vc + base + (size_t)key * DH + j * 64 + c * 8);
    }
  };
  issue(0);
  const float SC = 0.07216878364870322f;
  const float L2E = 1.4426950408889634f;
  for (int it = 0; it < NIT; ++it) {
    v4u kk[U][3], vv[U][3];
#pragma unroll
    for (int u = 0; u < U; ++u)
#pragma unroll
      for (int j = 0; j < 3; ++j) { kk[u][j] = kn[u][j]; vv[u][j] = vn[u][j]; }
    if (it + 1 < NIT) issue(it + 1);
    float s[U];
#pragma unroll
    for (int u = 0; u < U; ++u) {
      float d = 0.f;
#pragma unroll
      for (int j = 0; j < 3; ++j)
#pragma unroll
        for (int e = 0; e < 4; ++e) d = dot2(kk[u][j][e], qf[j][e], d);
      d += dpp<XOR1>(d); d += dpp<XOR2>(d); d += dpp<XOR7>(d);
      s[u] = rbf(rbf(d) * SC) * L2E;
    }
    float mn = m;
#pragma unroll
    for (int u = 0; u < U; ++u) mn = fmaxf(mn, s[u]);
    float alpha = __builtin_amdgcn_exp2f(m - mn);
    m = mn;
    l *= alpha;
#pragma unroll
    for (int i = 0; i < 24; ++i) acc[i] *= alpha;
#pragma unroll
    for (int u = 0; u < U; ++u) {
      float p = __builtin_amdgcn_exp2f(s[u] - mn);
      l += p;
#pragma unroll
      for (int j = 0; j < 3; ++j)
#pragma unroll
        for (int e = 0; e < 4; ++e) {
          acc[j * 8 + 2 * e] += p * __uint_as_float(vv[u][j][e] << 16);
          acc[j * 8 + 2 * e + 1] += p * __uint_as_float(vv[u][j][e] & 0xffff0000u);
        }
    }
  }
  float* rec = sm[wave * 8 + g];
#pragma unroll
  for (int j = 0; j < 3; ++j)
#pragma unroll
    for (int e = 0; e < 8; ++e) rec[j * 64 + c * 8 + e] = acc[j * 8 + e];
  if (c == 0) { rec[DH] = m; rec[DH + 1] = l; }
  __syncthreads();
  if (tid < DH) {
    float M = -INFINITY;
    for (int r = 0; r < NW * 8; ++r) M = fmaxf(M, sm[r][DH]);
    float L = 0.f, o = 0.f;
    for (int r = 0; r < NW * 8; ++r) { float w = __builtin_amdgcn_exp2f(sm[r][DH] - M); L += w * sm[r][DH + 1]; o += w * sm[r][tid]; }
    part[blk * 200 + tid] = o;
    if (tid == 0) { part[blk * 200 + DH] = M; part[blk * 200 + DH + 1] = L; }
  }
}

__global__ void __launch_bounds__(320) k3(const float* part, const u16* wo, const float* hbuf, const u16* wd, float* ypart) {
  __shared__ float o[2][DH];
  int tid = threadIdx.x, blk = blockIdx.x;
  const u16* w;
  if (blk < 32) {
    for (int i = tid; i < 2 * DH; i += 320) {
      int b = i / DH, d = i % DH; const float* p = part + (size_t)((b * H + blk) * 4) * 200;
      float M = -INFINITY;
      for (int s = 0; s < 4; ++s) M = fmaxf(M, p[s * 200 + DH]);
      float L = 0, a = 0;
      for (int s = 0; s < 4; ++s) { float ww = __builtin_amdgcn_exp2f(p[s * 200 + DH] - M); L += ww * p[s * 200 + DH + 1]; a += ww * p[s * 200 + d]; }
      o[b][d] = rbf(a / L);
    }
    w = wo + (size_t)blk * DH * D;
  } else {
    for (int i = tid; i < 2 * DH; i += 320) { int b = i / DH, d = i % DH; o[b][d] = hbuf[b * F + (blk - 32) * DH + d]; }
    w = wd + (size_t)(blk - 32) * DH * D;
  }
  __syncthreads();
  float a00 = 0, a01 = 0, a10 = 0, a11 = 0;
#pragma unroll 16
  for (int k = 0; k < DH; ++k) {
    unsigned ww = *(const unsigned*)(w + (size_t)k * D + tid * 2);
    float w0 = __uint_as_float(ww << 16), w1 = __uint_as_float(ww & 0xffff0000u);
    a00 += o[0][k] * w0; a01 += o[0][k] * w1; a10 += o[1][k] * w0; a11 += o[1][k] * w1;
  }
  float* y = ypart + (size_t)blk * 2 * D;
  y[tid * 2] = a00; y[tid * 2 + 1] = a01; y[D + tid * 2] = a10; y[D + tid * 2 + 1] = a11;
}

__global__ void __launch_bounds__(256) k4(const float* ypart, const u16* x, u16* out) {
  int i = blockIdx.x * 256 + threadIdx.x;
  if (i >= 2 * D) return;
  float s = 0, gp = 0;
  for (int h = 0; h < 32; ++h) s += ypart[(size_t)h * 2 * D + i];
  for (int h = 32; h < 40; ++h) gp += ypart[(size_t)h * 2 * D + i];
  s = rbf(s); gp = rbf(gp);
  float g = rbf(gp / (1.f + __expf(-gp)));
  out[i] = f2bf(bf2f(x[i]) + rbf(s * g));
}

__global__ void __launch_bounds__(512) kdiag(unsigned long long* st) {
  unsigned long long t0 = __builtin_amdgcn_s_memrealtime();
  if (threadIdx.x == 0) st[blockIdx.x] = t0;
}
__global__ void __launch_bounds__(64) kspin(const unsigned long long* st) {
  unsigned long long mn = ~0ull, mx = 0, sum = 0;
  for (int i = 0; i < 256; ++i) { unsigned long long v = st[i]; mn = v < mn ? v : mn; mx = v > mx ? v : mx; }
  for (int i = 0; i < 256; ++i) sum += st[i] - mn;
  unsigned long long metric = METRIC == 0 ? 0 : METRIC == 1 ? (mx - mn) : sum / 256;
  if (metric > 2000) metric = 2000;
  unsigned long long t0 = __builtin_amdgcn_s_memrealtime();
  while (__builtin_amdgcn_s_memrealtime() - t0 < metric * 20) __builtin_amdgcn_s_sleep(1);
}
extern "C" void kernel_launch(void* const* d_in, const int* in_sizes, int n_in, void* d_out, int out_size, void* d_ws, size_t ws_size,
                              hipStream_t stream) {
  const u16* x = (const u16*)d_in[0];
  const u16* kc = (const u16*)d_in[1];
  const u16* vc = (const u16*)d_in[2];
  const u16* wq = (const u16*)d_in[3];
  const u16* wo = (const u16*)d_in[4];
  const u16* wg = (const u16*)d_in[6];
  const u16* wu = (const u16*)d_in[7];
  const u16* wd = (const u16*)d_in[8];
  char* ws = (char*)d_ws;
  u16* q = (u16*)ws;
  float* hbuf = (float*)(ws + 32768);
  float* part = (float*)(ws + 65536);
  float* ypart = (float*)(ws + 65536 + 262144);
  unsigned long long* st = (unsigned long long*)(ws + (1 << 20));
  kdiag<<<256, 512, 0, stream>>>(st);
  k1<<<60, 512, 0, stream>>>(x, wq, wg, wu, q, hbuf);
  k2<<<256, NW * 64, 0, stream>>>(q, kc, vc, part);
  k3<<<40, 320, 0, stream>>>(part, wo, hbuf, wd, ypart);
  k4<<<5, 256, 0, stream>>>(ypart, x, (u16*)d_out);
  kspin<<<1, 64, 0, stream>>>(st);
}
```

```cpp
#define DLD(x) __builtin_nontemporal_load((const v4u*)(x))
#define DSRC d_in[1]
#define NBYTES 0
#include <hip/hip_runtime.h>
#include <hip/hip_bf16.h>
#include <stdint.h>

#define KV_NT 1
#define ROUND_SCORES 1

typedef unsigned v4u __attribute__((ext_vector_type(4)));
typedef __bf16 bf16x2 __attribute__((ext_vector_type(2)));
typedef unsigned short u16;

constexpr int B = 2, D = 640, H = 32, SK = 4096, DH = 192, F = 1536;
constexpr int NSPLIT = 4, KPS = SK / NSPLIT;

__device__ __forceinline__ float bf2f(u16 v) { return __uint_as_float((unsigned)v << 16); }
__device__ __forceinline__ float rbf(float f) { return (float)(__bf16)f; }
__device__ __forceinline__ u16 f2bf(float f) { return __builtin_bit_cast(u16, (__bf16)f); }
__device__ __forceinline__ float dot2(unsigned a, unsigned b, float acc) {
  return __builtin_amdgcn_fdot2_f32_bf16(__builtin_bit_cast(bf16x2, a), __builtin_bit_cast(bf16x2, b), acc, false);
}
template <int CTRL> __device__ __forceinline__ float dpp(float x) {
  return __builtin_bit_cast(float, __builtin_amdgcn_mov_dpp(__builtin_bit_cast(int, x), CTRL, 0xf, 0xf, true));
}
constexpr int XOR1 = 0xB1, XOR2 = 0x4E, XOR7 = 0x141;

__device__ void layernorm_to_lds(const u16* x, float* xn  , int tid, int nthr) {
  int wave = tid >> 6, lane = tid & 63;
  if (wave < 2) {
    float v[10];
    float s = 0.f;
    for (int i = 0; i < 10; ++i) { v[i] = bf2f(x[wave * D + lane + 64 * i]); s += v[i]; }
    for (int m = 32; m >= 1; m >>= 1) s += __shfl_xor(s, m);
    float mean = s * (1.f / D);
    float q = 0.f;
    for (int i = 0; i < 10; ++i) { float d = v[i] - mean; q += d * d; }
    for (int m = 32; m >= 1; m >>= 1) q += __shfl_xor(q, m);
    float var = rbf(q * (1.f / D));
    float mb = rbf(mean);
    float den = rbf(sqrtf(rbf(var + 1e-5f)));
    for (int i = 0; i < 10; ++i) xn[wave * D + lane + 64 * i] = rbf(rbf(v[i] - mb) / den);
  }
  __syncthreads();
}

__global__ void __launch_bounds__(512) k1(const u16* x, const u16* wq, const u16* wg, const u16* wu, u16* q, float* hbuf) {
  __shared__ float xn[2 * D];
  __shared__ float red[8][4][128];
  int tid = threadIdx.x, wave = tid >> 6, lane = tid & 63;
  layernorm_to_lds(x, xn, tid, 512);
  int blk = blockIdx.x;
  if (blk < 48) {
    int n0 = blk * 128 + lane * 2;
    float a00 = 0, a01 = 0, a10 = 0, a11 = 0;
    for (int k = wave * 80; k < wave * 80 + 80; ++k) {
      unsigned w = *(const unsigned*)(wq + (size_t)k * (H * DH) + n0);
      float w0 = __uint_as_float(w << 16), w1 = __uint_as_float(w & 0xffff0000u);
      a00 += xn[k] * w0; a01 += xn[k] * w1; a10 += xn[D + k] * w0; a11 += xn[D + k] * w1;
    }
    red[wave][0][lane * 2] = a00; red[wave][0][lane * 2 + 1] = a01; red[wave][1][lane * 2] = a10; red[wave][1][lane * 2 + 1] = a11;
    __syncthreads();
    if (tid < 256) {
      int b = tid >> 7, c = tid & 127; float s = 0;
      for (int w = 0; w < 8; ++w) s += red[w][b][c];
      q[b * (H * DH) + blk * 128 + c] = f2bf(s);
    }
  } else {
    int fb = blk - 48;
    int n0 = fb * 128 + lane * 2;
    float g00 = 0, g01 = 0, g10 = 0, g11 = 0, u00 = 0, u01 = 0, u10 = 0, u11 = 0;
    for (int k = wave * 80; k < wave * 80 + 80; ++k) {
      unsigned w = *(const unsigned*)(wg + (size_t)k * F + n0);
      unsigned u = *(const unsigned*)(wu + (size_t)k * F + n0);
      float w0 = __uint_as_float(w << 16), w1 = __uint_as_float(w & 0xffff0000u);
      float v0 = __uint_as_float(u << 16), v1 = __uint_as_float(u & 0xffff0000u);
      g00 += xn[k] * w0; g01 += xn[k] * w1; g10 += xn[D + k] * w0; g11 += xn[D + k] * w1;
      u00 += xn[k] * v0; u01 += xn[k] * v1; u10 += xn[D + k] * v0; u11 += xn[D + k] * v1;
    }
    red[wave][0][lane * 2] = g00; red[wave][0][lane * 2 + 1] = g01; red[wave][1][lane * 2] = g10; red[wave][1][lane * 2 + 1] = g11;
    red[wave][2][lane * 2] = u00; red[wave][2][lane * 2 + 1] = u01; red[wave][3][lane * 2] = u10; red[wave][3][lane * 2 + 1] = u11;
    __syncthreads();
    if (tid < 256) {
      int b = tid >> 7, c = tid & 127; float g = 0, u = 0;
      for (int w = 0; w < 8; ++w) { g += red[w][b][c]; u += red[w][2 + b][c]; }
      g = rbf(g); u = rbf(u);
      float t = 0.7978845608028654f * (g + 0.044715f * g * g * g);
      float ge = rbf(0.5f * g * (1.f + tanhf(t)));
      hbuf[b * F + fb * 128 + c] = rbf(ge * u);
    }
  }
}

__device__ __forceinline__ v4u ldkv(const u16* p) {
  return __builtin_nontemporal_load((const v4u*)p);
}

constexpr int NW = 8, U = 2;
__global__ void __launch_bounds__(NW * 64) k2(const u16* q, const u16* kc, const u16* vc, float* part  ) {
  __shared__ float sm[NW * 8][DH + 4];
  int tid = threadIdx.x, wave = tid >> 6, lane = tid & 63, g = lane >> 3, c = lane & 7;
  int blk = blockIdx.x; int bh = blk >> 2, sp = blk & 3;
  v4u qf[3];
  for (int j = 0; j < 3; ++j) qf[j] = *(const v4u*)(q + (size_t)bh * DH + j * 64 + c * 8);
  const size_t base = ((size_t)bh * SK + sp * KPS) * DH;
  float m = -INFINITY, l = 0.f, acc[24];
  for (int i = 0; i < 24; ++i) acc[i] = 0.f;
  constexpr int NIT = KPS / (NW * 8 * U);
  v4u kn[U][3], vn[U][3];
  auto issue = [&](int it) {
#pragma unroll
    for (int u = 0; u < U; ++u) {
      int key = (it * NW + wave) * 8 * U + u * 8 + g;
#pragma unroll
      for (int j = 0; j < 3; ++j) kn[u][j] = ldkv(kc + base + (size_t)key * DH + j * 64 + c * 8);
    }
#pragma unroll
    for (int u = 0; u < U; ++u) {
      int key = (it * NW + wave) * 8 * U + u * 8 + g;
#pragma unroll
      for (int j = 0; j < 3; ++j) vn[u][j] = ldkv(vc + base + (size_t)key * DH + j * 64 + c * 8);
    }
  };
  issue(0);
  const float SC = 0.07216878364870322f;
  const float L2E = 1.4426950408889634f;
  for (int it = 0; it < NIT; ++it) {
    v4u kk[U][3], vv[U][3];
#pragma unroll
    for (int u = 0; u < U; ++u)
#pragma unroll
      for (int j = 0; j < 3; ++j) { kk[u][j] = kn[u][j]; vv[u][j] = vn[u][j]; }
    if (it + 1 < NIT) issue(it + 1);
    float s[U];
#pragma unroll
    for (int u = 0; u < U; ++u) {
      float d = 0.f;
#pragma unroll
      for (int j = 0; j < 3; ++j)
#pragma unroll
        for (int e = 0; e < 4; ++e) d = dot2(kk[u][j][e], qf[j][e], d);
      d += dpp<XOR1>(d); d += dpp<XOR2>(d); d += dpp<XOR7>(d);
      s[u] = rbf(rbf(d) * SC) * L2E;
    }
    float mn = m;
#pragma unroll
    for (int u = 0; u < U; ++u) mn = fmaxf(mn, s[u]);
    float alpha = __builtin_amdgcn_exp2f(m - mn);
    m = mn;
    l *= alpha;
#pragma unroll
    for (int i = 0; i < 24; ++i) acc[i] *= alpha;
#pragma unroll
    for (int u = 0; u < U; ++u) {
      float p = __builtin_amdgcn_exp2f(s[u] - mn);
      l += p;
#pragma unroll
      for (int j = 0; j < 3; ++j)
#pragma unroll
        for (int e = 0; e < 4; ++e) {
          acc[j * 8 + 2 * e] += p * __uint_as_float(vv[u][j][e] << 16);
          acc[j * 8 + 2 * e + 1] += p * __uint_as_float(vv[u][j][e] & 0xffff0000u);
        }
    }
  }
  float* rec = sm[wave * 8 + g];
#pragma unroll
  for (int j = 0; j < 3; ++j)
#pragma unroll
    for (int e = 0; e < 8; ++e) rec[j * 64 + c * 8 + e] = acc[j * 8 + e];
  if (c == 0) { rec[DH] = m; rec[DH + 1] = l; }
  __syncthreads();
  if (tid < DH) {
    float M = -INFINITY;
    for (int r = 0; r < NW * 8; ++r) M = fmaxf(M, sm[r][DH]);
    float L = 0.f, o = 0.f;
    for (int r = 0; r < NW * 8; ++r) { float w = __builtin_amdgcn_exp2f(sm[r][DH] - M); L += w * sm[r][DH + 1]; o += w * sm[r][tid]; }
    part[blk * 200 + tid] = o;
    if (tid == 0) { part[blk * 200 + DH] = M; part[blk * 200 + DH + 1] = L; }
  }
}

__global__ void __launch_bounds__(320) k3(const float* part, const u16* wo, const float* hbuf, const u16* wd, float* ypart) {
  __shared__ float o[2][DH];
  int tid = threadIdx.x, blk = blockIdx.x;
  const u16* w;
  if (blk < 32) {
    for (int i = tid; i < 2 * DH; i += 320) {
      int b = i / DH, d = i % DH; const float* p = part + (size_t)((b * H + blk) * 4) * 200;
      float M = -INFINITY;
      for (int s = 0; s < 4; ++s) M = fmaxf(M, p[s * 200 + DH]);
      float L = 0, a = 0;
      for (int s = 0; s < 4; ++s) { float ww = __builtin_amdgcn_exp2f(p[s * 200 + DH] - M); L += ww * p[s * 200 + DH + 1]; a += ww * p[s * 200 + d]; }
      o[b][d] = rbf(a / L);
    }
    w = wo + (size_t)blk * DH * D;
  } else {
    for (int i = tid; i < 2 * DH; i += 320) { int b = i / DH, d = i % DH; o[b][d] = hbuf[b * F + (blk - 32) * DH + d]; }
    w = wd + (size_t)(blk - 32) * DH * D;
  }
  __syncthreads();
  float a00 = 0, a01 = 0, a10 = 0, a11 = 0;
#pragma unroll 16
  for (int k = 0; k < DH; ++k) {
    unsigned ww = *(const unsigned*)(w + (size_t)k * D + tid * 2);
    float w0 = __uint_as_float(ww << 16), w1 = __uint_as_float(ww & 0xffff0000u);
    a00 += o[0][k] * w0; a01 += o[0][k] * w1; a10 += o[1][k] * w0; a11 += o[1][k] * w1;
  }
  float* y = ypart + (size_t)blk * 2 * D;
  y[tid * 2] = a00; y[tid * 2 + 1] = a01; y[D + tid * 2] = a10; y[D + tid * 2 + 1] = a11;
}

__global__ void __launch_bounds__(256) k4(const float* ypart, const u16* x, u16* out) {
  int i = blockIdx.x * 256 + threadIdx.x;
  if (i >= 2 * D) return;
  float s = 0, gp = 0;
  for (int h = 0; h < 32; ++h) s += ypart[(size_t)h * 2 * D + i];
  for (int h = 32; h < 40; ++h) gp += ypart[(size_t)h * 2 * D + i];
  s = rbf(s); gp = rbf(gp);
  float g = rbf(gp / (1.f + __expf(-gp)));
  out[i] = f2bf(bf2f(x[i]) + rbf(s * g));
}

__global__ void __launch_bounds__(256) kdiag(const char* p, unsigned* sink) {
  const int tid = threadIdx.x, wave = __builtin_amdgcn_readfirstlane(tid >> 6), lane = tid & 63;
  constexpr int NST = NBYTES / (1024 * 1024);
  const char* a = p + (size_t)(blockIdx.x * 4 + wave) * 1024 + lane * 16;
  v4u acc = {0, 0, 0, 0};
  if (NST >= 6) {
    v4u b[6];
#pragma unroll
    for (int d = 0; d < 6; ++d) b[d] = DLD(a + (size_t)d * 1048576);
#pragma unroll 1
    for (int it = 1; it < NST / 6; ++it) {
      a += 6 * 1048576;
#pragma unroll
      for (int d = 0; d < 6; ++d) { acc ^= b[d]; asm volatile("" : "+v"(acc)); __builtin_amdgcn_sched_barrier(0); b[d] = DLD(a + (size_t)d * 1048576); __builtin_amdgcn_sched_barrier(0); }
    }
#pragma unroll
    for (int d = 0; d < 6; ++d) acc ^= b[d];
  }
  if ((acc.x ^ acc.y ^ acc.z ^ acc.w) == 0x12345678u) sink[tid] = 1;
}
extern "C" void kernel_launch(void* const* d_in, const int* in_sizes, int n_in, void* d_out, int out_size, void* d_ws, size_t ws_size,
                              hipStream_t stream) {
  const u16* x = (const u16*)d_in[0];
  const u16* kc = (const u16*)d_in[1];
  const u16* vc = (const u16*)d_in[2];
  const u16* wq = (const u16*)d_in[3];
  const u16* wo = (const u16*)d_in[4];
  const u16* wg = (const u16*)d_in[6];
  const u16* wu = (const u16*)d_in[7];
  const u16* wd = (const u16*)d_in[8];
  char* ws = (char*)d_ws;
  u16* q = (u16*)ws;
  float* hbuf = (float*)(ws + 32768);
  float* part = (float*)(ws + 65536);
  float* ypart = (float*)(ws + 65536 + 262144);
  kdiag<<<256, 256, 0, stream>>>((const char*)DSRC, (unsigned*)(ws + (1 << 20)));
  k1<<<60, 512, 0, stream>>>(x, wq, wg, wu, q, hbuf);
  k2<<<256, NW * 64, 0, stream>>>(q, kc, vc, part);
  k3<<<40, 320, 0, stream>>>(part, wo, hbuf, wd, ypart);
  k4<<<5, 256, 0, stream>>>(ypart, x, (u16*)d_out);
}
```

```cpp
#include <hip/hip_runtime.h>
#include <stdint.h>

typedef unsigned short u16;
typedef unsigned long long u64;
typedef unsigned v4u __attribute__((ext_vector_type(4)));
typedef unsigned v2u __attribute__((ext_vector_type(2)));
typedef float f4 __attribute__((ext_vector_type(4)));
typedef short s4 __attribute__((ext_vector_type(4)));
typedef __bf16 bf2 __attribute__((ext_vector_type(2)));
typedef __bf16 bf8 __attribute__((ext_vector_type(8)));
#define GAS __attribute__((address_space(1)))
#define LAS __attribute__((address_space(3)))
#define RLX_AGENT __ATOMIC_RELAXED, __HIP_MEMORY_SCOPE_AGENT
typedef const GAS char* gptr;
typedef GAS u64 gu64;

#define ROT_MODE 0
#define NP 10
#define T_Q 3
#define T_ST 4
#define T_O 28
#define T_WO 30
#define T_F 7
#define T_H 11
#define T_G 18
#define LAZY 24
#define QDELAY 64
#define CLEAR 0

constexpr int D = 640, H = 32, SK = 4096, DH = 192, F = 1536, NTHREADS = 512;
constexpr int STEP = 32 * 3072;
constexpr unsigned SPIN_CAP = 1u << 16;

constexpr int WS_EPOCH = 0;
constexpr int WS_ST = 4096;
constexpr int WS_H = WS_ST + H * 2 * 16 * 8;
constexpr int WS_F = WS_H + 24 * 128 * 8;
constexpr int WS_X = WS_F + 24 * 64 * 32 * 8;
constexpr int WS_Y = WS_X + H * 4 * 8 * DH * 8;
constexpr int WS_G = WS_Y + 80 * 32 * 16 * 8;
constexpr int L_WO = 0;
constexpr int L_WD = 30720;
constexpr int L_XX = L_WD + 10240;
constexpr int L_PART = L_XX + 5120;
constexpr int L_QO = L_PART + 12288;
constexpr int L_HB = L_QO + 1536;
constexpr int L_SC = L_HB + 256;
constexpr int L_PP = L_SC + 4096;
constexpr int L_FL = L_PP + 4096;
constexpr int L_DG = L_FL + 64;
constexpr int L_TOTAL = 84 * 1024;

__device__ __forceinline__ float rbf(float f) { return (float)(__bf16)f; }
__device__ __forceinline__ unsigned bfbits(float f) { return (unsigned)__builtin_bit_cast(u16, (__bf16)f); }
__device__ __forceinline__ float dot2(unsigned a, unsigned b, float acc) {
  return __builtin_amdgcn_fdot2_f32_bf16(__builtin_bit_cast(bf2, a), __builtin_bit_cast(bf2, b), acc, false);
}
__device__ __forceinline__ float dot8(v4u a, v4u b, float acc) {
  return dot2(a.w, b.w, dot2(a.z, b.z, dot2(a.y, b.y, dot2(a.x, b.x, acc))));
}
__device__ __forceinline__ f4 mfma(v4u a, v4u b, f4 c) {
  return __builtin_amdgcn_mfma_f32_16x16x32_bf16(__builtin_bit_cast(bf8, a), __builtin_bit_cast(bf8, b), c, 0, 0, 0);
}
template <int CTRL> __device__ __forceinline__ float dpp(float x) {
  return __builtin_bit_cast(float, __builtin_amdgcn_mov_dpp(__builtin_bit_cast(int, x), CTRL, 0xf, 0xf, true));
}
constexpr int XOR1 = 0xB1, XOR2 = 0x4E, HMIR = 0x141, ROR2 = 0x122, ROR4 = 0x124, ROR8 = 0x128;
__device__ __forceinline__ float xrow_sum(float x) {
  auto s = __builtin_amdgcn_permlane16_swap(__float_as_uint(x), __float_as_uint(x), false, false);
  x = __uint_as_float(s[0]) + __uint_as_float(s[1]);
  auto t = __builtin_amdgcn_permlane32_swap(__float_as_uint(x), __float_as_uint(x), false, false);
  return __uint_as_float(t[0]) + __uint_as_float(t[1]);
}
__device__ __forceinline__ float xrow_max(float x) {
  auto s = __builtin_amdgcn_permlane16_swap(__float_as_uint(x), __float_as_uint(x), false, false);
  x = fmaxf(__uint_as_float(s[0]), __uint_as_float(s[1]));
  auto t = __builtin_amdgcn_permlane32_swap(__float_as_uint(x), __float_as_uint(x), false, false);
  return fmaxf(__uint_as_float(t[0]), __uint_as_float(t[1]));
}
__device__ __forceinline__ float wave_sum(float x) {
  x += dpp<XOR1>(x); x += dpp<XOR2>(x); x += dpp<HMIR>(x); x += dpp<ROR8>(x);
  return xrow_sum(x);
}
__device__ __forceinline__ float wave_max(float x) {
  x = fmaxf(x, dpp<XOR1>(x)); x = fmaxf(x, dpp<XOR2>(x)); x = fmaxf(x, dpp<HMIR>(x)); x = fmaxf(x, dpp<ROR8>(x));
  return xrow_max(x);
}
__device__ __forceinline__ v4u ld_nt(gptr p) { return __builtin_nontemporal_load((const GAS v4u*)p); }
__device__ __forceinline__ v4u ld_pl(gptr p) { return *(const GAS v4u*)p; }
#define SB() __builtin_amdgcn_sched_barrier(0)
#define BAR() asm volatile("s_waitcnt lgkmcnt(0)\n\ts_barrier" ::: "memory")
__device__ __forceinline__ void put(gu64* g, unsigned tag, float v) {
  __hip_atomic_store(g, ((u64)tag << 32) | __float_as_uint(v), RLX_AGENT);
}
template <int N, int SLEEP> __device__ __forceinline__ void sweep(gu64* g, int stride, unsigned tag, float (&v)[N], bool lazy = false) {
#pragma unroll 1
  for (unsigned spins = 0; spins < SPIN_CAP; ++spins) {
    bool ok = true;
#pragma unroll
    for (int k = 0; k < N; ++k) {
      const u64 x = __hip_atomic_load(g + k * stride, RLX_AGENT);
      v[k] = __uint_as_float((unsigned)x);
      ok &= (unsigned)(x >> 32) == tag;
    }
    if (__all(ok)) return;
    if (SLEEP) __builtin_amdgcn_s_sleep(SLEEP);
    if (lazy) __builtin_amdgcn_s_sleep(LAZY);
  }
#pragma unroll
  for (int k = 0; k < N; ++k) v[k] = __builtin_nanf("");
}
__device__ __forceinline__ void post(LAS char* lds, int word, unsigned v, int lane) {
  asm volatile("" ::: "memory");
  if (lane == 0) *(volatile LAS unsigned*)(lds + L_FL + word * 4) = v;
  asm volatile("" ::: "memory");
}
template <int SLEEP> __device__ __forceinline__ void wait1(LAS char* lds, int word, unsigned want) {
  while (__builtin_amdgcn_readfirstlane(*(volatile LAS unsigned*)(lds + L_FL + word * 4)) < want) { if (SLEEP) __builtin_amdgcn_s_sleep(SLEEP); }
  asm volatile("" ::: "memory");
}
template <int SLEEP, int N> __device__ __forceinline__ void waitn(LAS char* lds, int word, unsigned want) {
  for (;;) {
    const v4u v = *(volatile LAS v4u*)(lds + L_FL + word * 4);
    unsigned m = min(min(v.x, v.y), v.z);
    if (N == 4) m = min(m, v.w);
    if (__builtin_amdgcn_readfirstlane(m) >= want) break;
    if (SLEEP) __builtin_amdgcn_s_sleep(SLEEP);
  }
  asm volatile("" ::: "memory");
}
__device__ __forceinline__ void stage_rows(gptr src, unsigned lds_dst, int n, int lane) {
#pragma unroll 1
  for (int i = 0; i < n; ++i) {
    const unsigned ci = i * 64 + lane, row = (ci * 52429u) >> 19;
    const gptr p = src + (row * (D * 2 - 160) + ci * 16);
    unsigned keep;
    asm volatile("s_mov_b32 %0, m0\n\ts_mov_b32 m0, %2\n\ts_nop 0\n\tglobal_load_lds_dwordx4 %1, off\n\ts_mov_b32 m0, %0"
                 : "=&s"(keep) : "v"(p), "s"(lds_dst + i * 1024) : "memory");
  }
}
__device__ __forceinline__ v4u tr_frag(const LAS char* p) {
  const v2u b0 = __builtin_bit_cast(v2u, __builtin_amdgcn_ds_read_tr16_b64_v4i16((LAS s4*)p));
  const v2u b1 = __builtin_bit_cast(v2u, __builtin_amdgcn_ds_read_tr16_b64_v4i16((LAS s4*)(p + 640)));
  return (v4u){b0.x, b0.y, b1.x, b1.y};
}
__device__ __forceinline__ f4 fold(f4 a0, f4 a1) {
  f4 o;
#pragma unroll
  for (int r = 0; r < 4; ++r) {
    auto t = __builtin_amdgcn_permlane32_swap(__float_as_uint(a0[r]), __float_as_uint(a1[r]), false, false);
    o[r] = __uint_as_float(t[0]) + dpp<ROR8>(__uint_as_float(t[1]));
  }
  return o;
}

#define STAMP(i)
#define ULOG(i)
#define STAMPG(i)

extern "C" __global__ void __launch_bounds__(512, 2)
fused_decode(const u16* x_, const u16* kc_, const u16* vc_, const u16* wq_, const u16* wo_, const u16* wg_, const u16* wu_,
             const u16* wd_, u16* out_, char* ws_) {
  __shared__ __attribute__((aligned(16))) char lds_raw[L_TOTAL];
  LAS char* lds = (LAS char*)lds_raw;
  GAS char* ws = (GAS char*)ws_;
  const int tid = threadIdx.x, lane = tid & 63, wave = __builtin_amdgcn_readfirstlane(tid >> 6);
  const int w = blockIdx.x, h = (w & 7) * 4 + (w >> 6), mem = (w >> 3) & 7;
  const int ww = wave - 4;
  const int rot = ROT_MODE == 0 ? 0 : ROT_MODE == 1 ? h * 5 : (h & 3) * 4;
  const unsigned loff = (lane >> 3) * 384 + (lane & 7) * 16;
  const size_t bh0 = (size_t)h * (SK * DH * 2) + (size_t)(mem * 4 + ww) * 3072, bstr = (size_t)H * SK * DH * 2;
  const gptr kb0 = (gptr)kc_ + bh0;
  v4u R[10];
  if (tid < 4) {
    u64 pc; unsigned back, len, keep;
    asm volatile("s_getpc_b64 %0\n.Lfd_here:\n\ts_mov_b32 %1, .Lfd_here-fused_decode\n\ts_mov_b32 %2, .Lfd_last-fused_decode" : "=s"(pc), "=s"(back), "=s"(len));
    const unsigned c0 = ((w >> 3) * 4 + tid) * 128u;
    asm volatile("s_mov_b32 %0, m0\n\ts_mov_b32 m0, %3\n\ts_nop 0\n\tglobal_load_lds_dword %1, %2\n\ts_mov_b32 m0, %0"
                 : "=&s"(keep) : "v"(c0 < len ? c0 : 0u), "s"(pc - back), "s"((unsigned)(size_t)lds + L_FL + 48) : "memory");
  }
  SB();
  STAMP(wave);
  unsigned xr[5];
  if (wave < 3) {
    const GAS unsigned* xp = (const GAS unsigned*)x_ + (wave & 1) * (D / 2) + lane;
#pragma unroll
    for (int i = 0; i < 5; ++i) xr[i] = xp[64 * i];
    SB();
    gptr qp = (gptr)wq_ + ((size_t)mem * (80 * H * DH * 2) + h * (DH * 2) + wave * 128);
    const unsigned qoff = (lane >> 3) * (H * DH * 2) + (lane & 7) * 16;
#pragma unroll
    for (int i = 0; i < 10; ++i) { asm volatile("" : "+s"(qp)); R[i] = ld_nt(qp + qoff); qp += 8 * H * DH * 2; SB(); }
  }
  unsigned tag = *(const __attribute__((address_space(4))) unsigned*)(ws_ + WS_EPOCH) + 1u;
  asm volatile("s_barrier" ::: "memory");
  if (wave >= 4) {
#pragma unroll
    for (int u = 0; u < 2; ++u) {
      gptr p = kb0 + (size_t)((rot + u) & 15) * STEP;
      asm volatile("" : "+s"(p));
#pragma unroll
      for (int j = 0; j < 3; ++j) { R[4 + u * 3 + j] = ld_nt(p + loff + j * 128); SB(); }
    }
    if (tid < 264) *(volatile LAS unsigned*)(lds + L_FL + lane * 4) = 0u;
  } else if (wave < 2) {
    float v[10], s = 0.f;
#pragma unroll
    for (int i = 0; i < 5; ++i) { v[2 * i] = __uint_as_float(xr[i] << 16); v[2 * i + 1] = __uint_as_float(xr[i] & 0xffff0000u); s += v[2 * i] + v[2 * i + 1]; }
    const float mean = wave_sum(s) * (1.f / D);
    float q = 0.f;
#pragma unroll
    for (int i = 0; i < 10; ++i) { const float dd = v[i] - mean; q += dd * dd; }
    const float var = rbf(wave_sum(q) * (1.f / D)), mb = rbf(mean);
    const float inv = __builtin_amdgcn_rcpf(rbf(__builtin_amdgcn_sqrtf(rbf(var + 1e-5f))));
#pragma unroll
    for (int i = 0; i < 10; ++i) {
      const unsigned t = bfbits(rbf(v[i] - mb) * inv);
      *(LAS unsigned*)(lds + L_XX + ((lane + 64 * (i >> 1)) * 2 + (i & 1)) * 8 + wave * 4) = t | (t << 16);
    }
  }
  v4u msk;
  {
    const unsigned mm = (lane & 1) ? 0xffff0000u : 0x0000ffffu;
    const int q = (lane & 7) >> 1;
    msk = (v4u){q == 0 ? mm : 0u, q == 1 ? mm : 0u, q == 2 ? mm : 0u, q == 3 ? mm : 0u};
  }
  asm volatile("" : "+s"(tag), "+v"(msk));
  BAR();
  if (wave == 0) STAMP(8);

  if (wave >= 4) {
    const gptr vb0 = (gptr)vc_ + bh0;
    v4u U[NP + 2][3];
#pragma unroll
    for (int u = 0; u < NP + 2; ++u) {
      if (u < 2) {
#pragma unroll
        for (int j = 0; j < 3; ++j) U[u][j] = R[4 + u * 3 + j];
      } else {
        gptr p = kb0 + (size_t)((rot + u) & 15) * STEP;
        asm volatile("" : "+s"(p));
#pragma unroll
        for (int j = 0; j < 3; ++j) {
          asm volatile("s_waitcnt vmcnt(5)" ::: "memory");
          U[u][j] = ld_nt(p + loff + j * 128);
          SB();
        }
        if (u & 1) post(lds, ww, u >> 1, lane);
      }
      if (u >= 2) ULOG(u - 2);
    }
    v4u (&A)[3] = U[NP], (&Bq)[3] = U[NP + 1];
    auto score = [&](float d, LAS float* dst) {
      d += dpp<XOR1>(d); d += dpp<XOR2>(d); d += dpp<HMIR>(d);
      if ((lane & 7) == 0) *dst = rbf(rbf(d) * 0.07216878364870322f);
      SB();
    };
#pragma unroll 1
    for (int b = 0; b < 2; ++b) {
      waitn<0, 3>(lds, 4, b + 1);
      if (ww == 0) STAMP(12 + b);
      v4u q[3];
#pragma unroll
      for (int j = 0; j < 3; ++j) q[j] = *(const LAS v4u*)(lds + L_QO + b * 384 + j * 128 + (lane & 7) * 16);
      const gptr cur = kb0 + b * bstr, nxt = b ? vb0 : kb0 + bstr;
      LAS float* sc = (LAS float*)(lds + L_SC) + b * 512 + ww * 8 + (lane >> 3);
#pragma unroll 1
      for (int it = b ? 0 : NP / 2; it < 8; ++it) {
        post(lds, ww, b * 8 + it, lane);
        const gptr base = it == 7 ? nxt : cur;
        const gptr pa = base + (size_t)((2 * it + 2 + rot) & 15) * STEP + loff, pb = base + (size_t)((2 * it + 3 + rot) & 15) * STEP + loff;
        float d = 0.f;
#pragma unroll
        for (int j = 0; j < 3; ++j) { d = dot8(A[j], q[j], d); asm volatile("" : "+v"(d)); SB(); A[j] = ld_nt(pa + j * 128); SB(); }
        score(d, sc + it * 64);
        ULOG(b * 16 + it * 2);
        d = 0.f;
#pragma unroll
        for (int j = 0; j < 3; ++j) { d = dot8(Bq[j], q[j], d); asm volatile("" : "+v"(d)); SB(); Bq[j] = ld_nt(pb + j * 128); SB(); }
        score(d, sc + it * 64 + 32);
        ULOG(b * 16 + it * 2 + 1);
      }
      if (b == 0) {
#pragma unroll
        for (int u = 0; u < NP; ++u) score(dot8(U[u][2], q[2], dot8(U[u][1], q[1], dot8(U[u][0], q[0], 0.f))), sc + u * 32);
      }
      STAMP(16 + b * 4 + ww);
    }
    f4 o[3];
    const bool useful = ((lane >> 3) & 1) == (lane >> 5);
    LAS char* part = lds + L_PART + ((ww * 2 + (lane >> 5)) * DH + (lane & 7) * 8 + ((lane >> 4) & 1) * 4) * 4;
    auto vstep = [&](const LAS unsigned* pp, gptr pa, gptr pb, bool more, int n) {
      const unsigned p0 = pp[0], p1 = pp[32];
      const v4u a0 = msk & p0, a1 = msk & p1;
      SB();
#pragma unroll
      for (int j = 0; j < 3; ++j) { o[j] = mfma(a0, A[j], o[j]); SB(); if (more) A[j] = ld_nt(pa + j * 128); SB(); }
      ULOG(32 + 2 * n);
#pragma unroll
      for (int j = 0; j < 3; ++j) { o[j] = mfma(a1, Bq[j], o[j]); SB(); if (more) Bq[j] = ld_nt(pb + j * 128); SB(); }
      ULOG(33 + 2 * n);
    };
    auto vflush = [&](int b) {
      if (useful) {
#pragma unroll
        for (int j = 0; j < 3; ++j) *(LAS f4*)(part + b * (8 * DH * 4) + j * 256) = o[j];
      }
    };
    const LAS unsigned* pp = (const LAS unsigned*)(lds + L_PP) + ww * 8 + (lane >> 3);
#pragma unroll 1
    for (int n = 0; n < 15; ++n) {
      post(lds, ww, 16 + n, lane);
      if ((n & 7) == 0) {
        wait1<0>(lds, 7, 1 + (n >> 3));
#pragma unroll
        for (int j = 0; j < 3; ++j) o[j] = (f4){0.f, 0.f, 0.f, 0.f};
        STAMP(24 + (n >> 3) * 4 + ww);
      }
      const gptr base = n >= 7 ? vb0 + bstr : vb0;
      vstep(pp + n * 64, base + (size_t)((2 * n + 2 + rot) & 15) * STEP + loff, base + (size_t)((2 * n + 3 + rot) & 15) * STEP + loff, true, n);
      if (n == 7) vflush(0);
    }
    post(lds, ww, 31, lane);
    vstep(pp + 15 * 64, vb0, vb0, false, 15);
    vflush(1);
    post(lds, ww, 32, lane);
    STAMP(32 + ww);
  } else if (wave < 3) {
    gu64* xbox = (gu64*)(ws + WS_X) + h * (4 * 8 * DH);
    gu64* stbox = (gu64*)(ws + WS_ST) + h * 32;
    {
      f4 a0 = {0.f, 0.f, 0.f, 0.f}, a1 = a0;
      const LAS char* xp = lds + L_XX + (mem * 80 + (lane >> 3)) * 8;
      v2u xv[10];
#pragma unroll
      for (int i = 0; i < 10; ++i) xv[i] = *(const LAS v2u*)(xp + i * 64);
      SB();
#pragma unroll
      for (int i = 0; i < 10; ++i) { a0 = mfma(msk & xv[i].x, R[i], a0); a1 = mfma(msk & xv[i].y, R[i], a1); }
      const f4 s = fold(a0, a1);
      gu64* dst = xbox + ((lane >> 5) * 8 + mem) * DH + wave * 64 + ((lane >> 4) & 1) * 8 + (lane & 7);
      if ((lane & 8) == 0) {
#pragma unroll
        for (int r = 0; r < 4; ++r) put(dst + r * 16, tag, s[r]);
      }
      if (wave == 0) STAMP(9);
      wait1<4>(lds, 0, T_Q);
    }
    const int ot = wave * 64 + lane, qd = wave * 64 + (lane & 7) * 8 + ((lane >> 3) & 1) * 4 + (lane >> 4);
#pragma unroll 1
    for (int st = 0; st < 4; ++st) {
      if (st == 2 && wave == 0) {
#pragma unroll 1
        for (int b = 0; b < 2; ++b) {
          waitn<4, 4>(lds, 0, b * 8 + 8);
          const f4 s0 = *(const LAS f4*)(lds + L_SC + b * 2048 + lane * 32), s1 = *(const LAS f4*)(lds + L_SC + b * 2048 + lane * 32 + 16);
          const float s[8] = {s0.x, s0.y, s0.z, s0.w, s1.x, s1.y, s1.z, s1.w};
          constexpr float L2E = 1.4426950408889634f;
          float m = s[0];
#pragma unroll
          for (int i = 1; i < 8; ++i) m = fmaxf(m, s[i]);
          m = wave_max(m);
          const float ms = m == -INFINITY ? 0.f : m;
          float l = 0.f;
#pragma unroll
          for (int i = 0; i < 8; ++i) l += __builtin_amdgcn_exp2f((s[i] - ms) * L2E);
          l = wave_sum(l);
          if (lane < 2) put(stbox + b * 16 + mem * 2 + lane, tag, lane ? l : m);
          wait1<4>(lds, 0, b * 8 + 8 + T_ST);
          float st1[1];
          sweep<1, 16>(stbox + b * 16 + (lane & 15), 0, tag, st1);
          const float oth = dpp<XOR1>(st1[0]), mj = (lane & 1) ? oth : st1[0], lj = (lane & 1) ? st1[0] : oth;
          float M = fmaxf(mj, dpp<ROR2>(mj)); M = fmaxf(M, dpp<ROR4>(M)); M = fmaxf(M, dpp<ROR8>(M));
          float Ls = lj * __builtin_amdgcn_exp2f((mj - M) * L2E);
          Ls += dpp<ROR2>(Ls); Ls += dpp<ROR4>(Ls); Ls += dpp<ROR8>(Ls);
          const float inv = __builtin_amdgcn_rcpf(__uint_as_float(__builtin_amdgcn_readfirstlane(__float_as_uint(Ls))));
          unsigned p[8];
#pragma unroll
          for (int i = 0; i < 8; ++i) { const unsigned t = bfbits(__builtin_amdgcn_exp2f((s[i] - M) * L2E) * inv); p[i] = t | (t << 16); }
          *(LAS v4u*)(lds + L_PP + b * 2048 + lane * 32) = (v4u){p[0], p[1], p[2], p[3]};
          *(LAS v4u*)(lds + L_PP + b * 2048 + lane * 32 + 16) = (v4u){p[4], p[5], p[6], p[7]};
          post(lds, 7, b + 1, lane);
          STAMP(36 + b);
        }
      }
      if (st >= 2) {
        if (st == 2) waitn<4, 4>(lds, 0, 24); else { wait1<2>(lds, 0, 31); waitn<0, 4>(lds, 0, 32); }
        float s = 0.f;
#pragma unroll
        for (int r = 0; r < 8; ++r) s += *(const LAS float*)(lds + L_PART + (((st - 2) * 8 + r) * DH + ot) * 4);
        put(xbox + (st * 8 + mem) * DH + ot, tag, s);
        if (wave == 0) STAMP(36 + st);
        if (st == 2) wait1<4>(lds, 0, T_O);
      }
      float v[8];
      sweep<8, 1>(xbox + st * 8 * DH + ot, DH, tag, v, st < 3);
      *(LAS u16*)(lds + L_QO + (st * DH + (st < 2 ? qd : ot)) * 2) = (u16)bfbits(((v[0] + v[1]) + (v[2] + v[3])) + ((v[4] + v[5]) + (v[6] + v[7])));
      post(lds, 4 + wave, st + 1, lane);
      if (wave == 0) STAMP(40 + st);
    }
  } else {
    const unsigned lds0 = (unsigned)(size_t)lds;
    if (w >= 64) {
      const int fb = (w & 7) * 3 + (w >> 6) - 1;
      const size_t goff = (size_t)mem * (80 * F * 2) + fb * 128 + ((lane >> 3) * (F * 2) + (lane & 7) * 16);
      v4u g[10], u[10];
#pragma unroll
      for (int i = 0; i < 10; ++i) { g[i] = ld_nt((gptr)wg_ + goff + (size_t)i * (8 * F * 2)); u[i] = ld_nt((gptr)wu_ + goff + (size_t)i * (8 * F * 2)); }
      SB();
      f4 g0 = {0.f, 0.f, 0.f, 0.f}, g1 = g0, u0 = g0, u1 = g0;
      const LAS char* xp = lds + L_XX + (mem * 80 + (lane >> 3)) * 8;
#pragma unroll
      for (int i = 0; i < 10; ++i) {
        const v2u xv = *(const LAS v2u*)(xp + i * 64);
        const v4u a0 = msk & xv.x, a1 = msk & xv.y;
        g0 = mfma(a0, g[i], g0); g1 = mfma(a1, g[i], g1); u0 = mfma(a0, u[i], u0); u1 = mfma(a1, u[i], u1);
      }
      const f4 gs = fold(g0, g1), us = fold(u0, u1);
      gu64* fbox = (gu64*)(ws + WS_F) + fb * (64 * 32);
      if ((lane & 8) == 0) {
        gu64* dst = fbox + (((lane >> 4) & 1) * 32 + mem) * 32 + (lane >> 5) * 8 + (lane & 7);
#pragma unroll
        for (int r = 0; r < 4; ++r) { put(dst + r * 256, tag, gs[r]); put(dst + r * 256 + 16, tag, us[r]); }
      }
      stage_rows((gptr)wd_ + (size_t)fb * (64 * D * 2) + mem * 160, lds0 + L_WD, 10, lane);
      STAMP(44);
      float v[4];
      wait1<8>(lds, 0, T_F);
      sweep<4, 16>(fbox + mem * 256 + lane, 64, tag, v);
      const float sv = (v[0] + v[1]) + (v[2] + v[3]);
      auto t = __builtin_amdgcn_permlane32_swap(__float_as_uint(sv), __float_as_uint(sv), false, false);
      const unsigned tot = __float_as_uint(__uint_as_float(t[0]) + __uint_as_float(t[1]));
      auto gu = __builtin_amdgcn_permlane16_swap(tot, tot, false, false);
      const float gt = rbf(__uint_as_float(gu[0])), up = rbf(__uint_as_float(gu[1]));
      const float t2 = 2.f * 0.7978845608028654f * (gt + 0.044715f * gt * gt * gt);
      gu64* hbox = (gu64*)(ws + WS_H) + fb * 128;
      if (lane < 16) put(hbox + (lane >> 3) * 64 + mem * 8 + (lane & 7), tag, rbf(gt / (1.f + __expf(-t2))) * up);
      float hv[2];
      wait1<8>(lds, 0, T_H);
      sweep<2, 16>(hbox + lane, 64, tag, hv);
      *(LAS u16*)(lds + L_HB + (lane & 7) * 16 + (lane >> 3) * 2) = (u16)bfbits(hv[0]);
      *(LAS u16*)(lds + L_HB + 128 + (lane & 7) * 16 + (lane >> 3) * 2) = (u16)bfbits(hv[1]);
      asm volatile("s_waitcnt vmcnt(0) lgkmcnt(0)" ::: "memory");
      const LAS char* ap = lds + L_HB + ((lane >> 2) & 1) * 128 + (lane >> 4) * 16;
      const v4u a0 = *(const LAS v4u*)ap, a1 = *(const LAS v4u*)(ap + 64);
      const LAS char* bp = lds + L_WD + ((lane >> 4) * 8 + ((lane >> 2) & 3)) * 160 + (lane & 3) * 8;
      gu64* gdst = (gu64*)(ws + WS_G) + ((mem * 10 + ((lane >> 4) & 1)) * 24 + fb) * 16 + (lane & 15);
#pragma unroll 1
      for (int tt = 0; tt < 5; ++tt) {
        f4 y = mfma(a0, tr_frag(bp + tt * 32), (f4){0.f, 0.f, 0.f, 0.f});
        y = mfma(a1, tr_frag(bp + tt * 32 + 5120), y);
        if (lane < 32) put(gdst + tt * (2 * 24 * 16), tag, y.x);
      }
      STAMP(45);
    }
    float gate = 0.f, xres = 0.f;
    if (w < 80) {
      xres = __uint_as_float((unsigned)((const GAS u16*)x_)[(w & 1) * D + (w >> 1) * 16 + (lane & 15)] << 16);
      wait1<8>(lds, 0, T_G);
      float v[6];
      sweep<6, 16>((gu64*)(ws + WS_G) + w * (24 * 16) + lane, 64, tag, v);
      const float gp = rbf(xrow_sum(((v[0] + v[1]) + (v[2] + v[3])) + (v[4] + v[5])));
      gate = rbf(gp / (1.f + __expf(-gp)));
      STAMP(46);
    }
    wait1<2>(lds, 0, T_WO);
    STAMP(47);
    stage_rows((gptr)wo_ + (size_t)h * (DH * D * 2) + mem * 160, lds0 + L_WO, 30, lane);
    asm volatile("s_waitcnt vmcnt(0)" ::: "memory");
    STAMP(48);
    BAR();
    if (w < 80) {
      float v[8];
      sweep<8, 1>((gu64*)(ws + WS_Y) + w * (32 * 16) + lane, 64, tag, v);
      const float s = rbf(xrow_sum(((v[0] + v[1]) + (v[2] + v[3])) + ((v[4] + v[5]) + (v[6] + v[7]))));
      if (lane < 16) ((GAS u16*)out_)[(w & 1) * D + (w >> 1) * 16 + lane] = (u16)bfbits(xres + rbf(s * gate));
      if (w == 0 && lane == 0) __hip_atomic_store((GAS unsigned*)(ws + WS_EPOCH), tag, RLX_AGENT);
      STAMPG(56);
    }
    return;
  }
  const int tt = wave >= 4 ? ww : 4;
  const LAS char* ap = lds + L_QO + 768 + ((lane >> 2) & 1) * 384 + (lane >> 4) * 16;
  const LAS char* bp = lds + L_WO + ((lane >> 4) * 8 + ((lane >> 2) & 3)) * 160 + tt * 32 + (lane & 3) * 8;
  gu64* ydst = (gu64*)(ws + WS_Y) + (((mem * 5 + tt) * 2 + ((lane >> 4) & 1)) * 32 + h) * 16 + (lane & 15);
  asm volatile("" : "+v"(ap), "+v"(bp), "+v"(ydst));
  BAR();
  if (wave >= 2) {
    v4u a[6], bf[6];
#pragma unroll
    for (int ks = 0; ks < 6; ++ks) { a[ks] = *(const LAS v4u*)(ap + ks * 64); bf[ks] = tr_frag(bp + ks * 5120); }
    SB();
    f4 y = {0.f, 0.f, 0.f, 0.f};
#pragma unroll
    for (int ks = 0; ks < 6; ++ks) y = mfma(a[ks], bf[ks], y);
    if (lane < 32) put(ydst, tag, y.x);
    if (wave == 2) STAMPG(55);
  }
  asm volatile(".Lfd_last:");
}

extern "C" void kernel_launch(void* const* d_in, const int* in_sizes, int n_in, void* d_out, int out_size, void* d_ws, size_t ws_size,
                              hipStream_t stream) {
  fused_decode<<<256, 512, 0, stream>>>((const u16*)d_in[0], (const u16*)d_in[1], (const u16*)d_in[2], (const u16*)d_in[3],
                                        (const u16*)d_in[4], (const u16*)d_in[6], (const u16*)d_in[7], (const u16*)d_in[8],
                                        (u16*)d_out, (char*)d_ws);
}
```

```cpp
#define DIAG
#define SIDX 56
#define KIND 3
#include <hip/hip_runtime.h>
#include <stdint.h>

typedef unsigned short u16;
typedef unsigned long long u64;
typedef unsigned v4u __attribute__((ext_vector_type(4)));
typedef unsigned v2u __attribute__((ext_vector_type(2)));
typedef float f4 __attribute__((ext_vector_type(4)));
typedef short s4 __attribute__((ext_vector_type(4)));
typedef __bf16 bf2 __attribute__((ext_vector_type(2)));
typedef __bf16 bf8 __attribute__((ext_vector_type(8)));
#define GAS __attribute__((address_space(1)))
#define LAS __attribute__((address_space(3)))
#define RLX_AGENT __ATOMIC_RELAXED, __HIP_MEMORY_SCOPE_AGENT
typedef const GAS char* gptr;
typedef GAS u64 gu64;

#define ROT_MODE 0
#define NP 10
#define T_Q 3
#define T_ST 4
#define T_O 28
#define T_WO 30
#define T_F 7
#define T_H 11
#define T_G 18
#define LAZY 24
#define QDELAY 64
#define CLEAR 0

constexpr int D = 640, H = 32, SK = 4096, DH = 192, F = 1536, NTHREADS = 512;
constexpr int STEP = 32 * 3072;
constexpr unsigned SPIN_CAP = 1u << 16;

constexpr int WS_EPOCH = 0;
constexpr int WS_ST = 4096;
constexpr int WS_H = WS_ST + H * 2 * 16 * 8;
constexpr int WS_F = WS_H + 24 * 128 * 8;
constexpr int WS_X = WS_F + 24 * 64 * 32 * 8;
constexpr int WS_Y = WS_X + H * 4 * 8 * DH * 8;
constexpr int WS_G = WS_Y + 80 * 32 * 16 * 8;
constexpr int L_WO = 0;
constexpr int L_WD = 30720;
constexpr int L_XX = L_WD + 10240;
constexpr int L_PART = L_XX + 5120;
constexpr int L_QO = L_PART + 12288;
constexpr int L_HB = L_QO + 1536;
constexpr int L_SC = L_HB + 256;
constexpr int L_PP = L_SC + 4096;
constexpr int L_FL = L_PP + 4096;
constexpr int L_DG = L_FL + 64;
constexpr int L_TOTAL = 84 * 1024;

__device__ __forceinline__ float rbf(float f) { return (float)(__bf16)f; }
__device__ __forceinline__ unsigned bfbits(float f) { return (unsigned)__builtin_bit_cast(u16, (__bf16)f); }
__device__ __forceinline__ float dot2(unsigned a, unsigned b, float acc) {
  return __builtin_amdgcn_fdot2_f32_bf16(__builtin_bit_cast(bf2, a), __builtin_bit_cast(bf2, b), acc, false);
}
__device__ __forceinline__ float dot8(v4u a, v4u b, float acc) {
  return dot2(a.w, b.w, dot2(a.z, b.z, dot2(a.y, b.y, dot2(a.x, b.x, acc))));
}
__device__ __forceinline__ f4 mfma(v4u a, v4u b, f4 c) {
  return __builtin_amdgcn_mfma_f32_16x16x32_bf16(__builtin_bit_cast(bf8, a), __builtin_bit_cast(bf8, b), c, 0, 0, 0);
}
template <int CTRL> __device__ __forceinline__ float dpp(float x) {
  return __builtin_bit_cast(float, __builtin_amdgcn_mov_dpp(__builtin_bit_cast(int, x), CTRL, 0xf, 0xf, true));
}
constexpr int XOR1 = 0xB1, XOR2 = 0x4E, HMIR = 0x141, ROR2 = 0x122, ROR4 = 0x124, ROR8 = 0x128;
__device__ __forceinline__ float xrow_sum(float x) {
  auto s = __builtin_amdgcn_permlane16_swap(__float_as_uint(x), __float_as_uint(x), false, false);
  x = __uint_as_float(s[0]) + __uint_as_float(s[1]);
  auto t = __builtin_amdgcn_permlane32_swap(__float_as_uint(x), __float_as_uint(x), false, false);
  return __uint_as_float(t[0]) + __uint_as_float(t[1]);
}
__device__ __forceinline__ float xrow_max(float x) {
  auto s = __builtin_amdgcn_permlane16_swap(__float_as_uint(x), __float_as_uint(x), false, false);
  x = fmaxf(__uint_as_float(s[0]), __uint_as_float(s[1]));
  auto t = __builtin_amdgcn_permlane32_swap(__float_as_uint(x), __float_as_uint(x), false, false);
  return fmaxf(__uint_as_float(t[0]), __uint_as_float(t[1]));
}
__device__ __forceinline__ float wave_sum(float x) {
  x += dpp<XOR1>(x); x += dpp<XOR2>(x); x += dpp<HMIR>(x); x += dpp<ROR8>(x);
  return xrow_sum(x);
}
__device__ __forceinline__ float wave_max(float x) {
  x = fmaxf(x, dpp<XOR1>(x)); x = fmaxf(x, dpp<XOR2>(x)); x = fmaxf(x, dpp<HMIR>(x)); x = fmaxf(x, dpp<ROR8>(x));
  return xrow_max(x);
}
__device__ __forceinline__ v4u ld_nt(gptr p) { return __builtin_nontemporal_load((const GAS v4u*)p); }
__device__ __forceinline__ v4u ld_pl(gptr p) { return *(const GAS v4u*)p; }
#define SB() __builtin_amdgcn_sched_barrier(0)
#define BAR() asm volatile("s_waitcnt lgkmcnt(0)\n\ts_barrier" ::: "memory")
__device__ __forceinline__ void put(gu64* g, unsigned tag, float v) {
  __hip_atomic_store(g, ((u64)tag << 32) | __float_as_uint(v), RLX_AGENT);
}
template <int N, int SLEEP> __device__ __forceinline__ void sweep(gu64* g, int stride, unsigned tag, float (&v)[N], bool lazy = false) {
#pragma unroll 1
  for (unsigned spins = 0; spins < SPIN_CAP; ++spins) {
    bool ok = true;
#pragma unroll
    for (int k = 0; k < N; ++k) {
      const u64 x = __hip_atomic_load(g + k * stride, RLX_AGENT);
      v[k] = __uint_as_float((unsigned)x);
      ok &= (unsigned)(x >> 32) == tag;
    }
    if (__all(ok)) return;
    if (SLEEP) __builtin_amdgcn_s_sleep(SLEEP);
    if (lazy) __builtin_amdgcn_s_sleep(LAZY);
  }
#pragma unroll
  for (int k = 0; k < N; ++k) v[k] = __builtin_nanf("");
}
__device__ __forceinline__ void post(LAS char* lds, int word, unsigned v, int lane) {
  asm volatile("" ::: "memory");
  if (lane == 0) *(volatile LAS unsigned*)(lds + L_FL + word * 4) = v;
  asm volatile("" ::: "memory");
}
template <int SLEEP> __device__ __forceinline__ void wait1(LAS char* lds, int word, unsigned want) {
  while (__builtin_amdgcn_readfirstlane(*(volatile LAS unsigned*)(lds + L_FL + word * 4)) < want) { if (SLEEP) __builtin_amdgcn_s_sleep(SLEEP); }
  asm volatile("" ::: "memory");
}
template <int SLEEP, int N> __device__ __forceinline__ void waitn(LAS char* lds, int word, unsigned want) {
  for (;;) {
    const v4u v = *(volatile LAS v4u*)(lds + L_FL + word * 4);
    unsigned m = min(min(v.x, v.y), v.z);
    if (N == 4) m = min(m, v.w);
    if (__builtin_amdgcn_readfirstlane(m) >= want) break;
    if (SLEEP) __builtin_amdgcn_s_sleep(SLEEP);
  }
  asm volatile("" ::: "memory");
}
__device__ __forceinline__ void stage_rows(gptr src, unsigned lds_dst, int n, int lane) {
#pragma unroll 1
  for (int i = 0; i < n; ++i) {
    const unsigned ci = i * 64 + lane, row = (ci * 52429u) >> 19;
    const gptr p = src + (row * (D * 2 - 160) + ci * 16);
    unsigned keep;
    asm volatile("s_mov_b32 %0, m0\n\ts_mov_b32 m0, %2\n\ts_nop 0\n\tglobal_load_lds_dwordx4 %1, off\n\ts_mov_b32 m0, %0"
                 : "=&s"(keep) : "v"(p), "s"(lds_dst + i * 1024) : "memory");
  }
}
__device__ __forceinline__ v4u tr_frag(const LAS char* p) {
  const v2u b0 = __builtin_bit_cast(v2u, __builtin_amdgcn_ds_read_tr16_b64_v4i16((LAS s4*)p));
  const v2u b1 = __builtin_bit_cast(v2u, __builtin_amdgcn_ds_read_tr16_b64_v4i16((LAS s4*)(p + 640)));
  return (v4u){b0.x, b0.y, b1.x, b1.y};
}
__device__ __forceinline__ f4 fold(f4 a0, f4 a1) {
  f4 o;
#pragma unroll
  for (int r = 0; r < 4; ++r) {
    auto t = __builtin_amdgcn_permlane32_swap(__float_as_uint(a0[r]), __float_as_uint(a1[r]), false, false);
    o[r] = __uint_as_float(t[0]) + dpp<ROR8>(__uint_as_float(t[1]));
  }
  return o;
}

#define STAMP(i) do { SB(); if (lane == 0) ((LAS unsigned*)(lds + L_DG))[i] = (unsigned)__builtin_amdgcn_s_memrealtime(); SB(); } while (0)
#define ULOG(i) do { SB(); if (lane == 0) ((LAS unsigned*)(lds + L_DG + 256))[ww * 64 + (i)] = (unsigned)__builtin_amdgcn_s_memrealtime(); SB(); } while (0)
#define STAMPG(i) do { SB(); if (lane == 0) ((GAS unsigned*)(ws + (8 << 20)))[w * 64 + (i)] = (unsigned)__builtin_amdgcn_s_memrealtime(); SB(); } while (0)

extern "C" __global__ void __launch_bounds__(512, 2)
fused_decode(const u16* x_, const u16* kc_, const u16* vc_, const u16* wq_, const u16* wo_, const u16* wg_, const u16* wu_,
             const u16* wd_, u16* out_, char* ws_) {
  __shared__ __attribute__((aligned(16))) char lds_raw[L_TOTAL];
  LAS char* lds = (LAS char*)lds_raw;
  GAS char* ws = (GAS char*)ws_;
  const int tid = threadIdx.x, lane = tid & 63, wave = __builtin_amdgcn_readfirstlane(tid >> 6);
  const int w = blockIdx.x, h = (w & 7) * 4 + (w >> 6), mem = (w >> 3) & 7;
  const int ww = wave - 4;
  const int rot = ROT_MODE == 0 ? 0 : ROT_MODE == 1 ? h * 5 : (h & 3) * 4;
  const unsigned loff = (lane >> 3) * 384 + (lane & 7) * 16;
  const size_t bh0 = (size_t)h * (SK * DH * 2) + (size_t)(mem * 4 + ww) * 3072, bstr = (size_t)H * SK * DH * 2;
  const gptr kb0 = (gptr)kc_ + bh0;
  v4u R[10];
  if (tid < 4) {
    u64 pc; unsigned back, len, keep;
    asm volatile("s_getpc_b64 %0\n.Lfd_here:\n\ts_mov_b32 %1, .Lfd_here-fused_decode\n\ts_mov_b32 %2, .Lfd_last-fused_decode" : "=s"(pc), "=s"(back), "=s"(len));
    const unsigned c0 = ((w >> 3) * 4 + tid) * 128u;
    asm volatile("s_mov_b32 %0, m0\n\ts_mov_b32 m0, %3\n\ts_nop 0\n\tglobal_load_lds_dword %1, %2\n\ts_mov_b32 m0, %0"
                 : "=&s"(keep) : "v"(c0 < len ? c0 : 0u), "s"(pc - back), "s"((unsigned)(size_t)lds + L_FL + 48) : "memory");
  }
  SB();
  STAMP(wave);
  unsigned xr[5];
  if (wave < 3) {
    const GAS unsigned* xp = (const GAS unsigned*)x_ + (wave & 1) * (D / 2) + lane;
#pragma unroll
    for (int i = 0; i < 5; ++i) xr[i] = xp[64 * i];
    SB();
    gptr qp = (gptr)wq_ + ((size_t)mem * (80 * H * DH * 2) + h * (DH * 2) + wave * 128);
    const unsigned qoff = (lane >> 3) * (H * DH * 2) + (lane & 7) * 16;
#pragma unroll
    for (int i = 0; i < 10; ++i) { asm volatile("" : "+s"(qp)); R[i] = ld_nt(qp + qoff); qp += 8 * H * DH * 2; SB(); }
  }
  unsigned tag = *(const __attribute__((address_space(4))) unsigned*)(ws_ + WS_EPOCH) + 1u;
  asm volatile("s_barrier" ::: "memory");
  if (wave >= 4) {
#pragma unroll
    for (int u = 0; u < 2; ++u) {
      gptr p = kb0 + (size_t)((rot + u) & 15) * STEP;
      asm volatile("" : "+s"(p));
#pragma unroll
      for (int j = 0; j < 3; ++j) { R[4 + u * 3 + j] = ld_nt(p + loff + j * 128); SB(); }
    }
    if (tid < 264) *(volatile LAS unsigned*)(lds + L_FL + lane * 4) = 0u;
  } else if (wave < 2) {
    float v[10], s = 0.f;
#pragma unroll
    for (int i = 0; i < 5; ++i) { v[2 * i] = __uint_as_float(xr[i] << 16); v[2 * i + 1] = __uint_as_float(xr[i] & 0xffff0000u); s += v[2 * i] + v[2 * i + 1]; }
    const float mean = wave_sum(s) * (1.f / D);
    float q = 0.f;
#pragma unroll
    for (int i = 0; i < 10; ++i) { const float dd = v[i] - mean; q += dd * dd; }
    const float var = rbf(wave_sum(q) * (1.f / D)), mb = rbf(mean);
    const float inv = __builtin_amdgcn_rcpf(rbf(__builtin_amdgcn_sqrtf(rbf(var + 1e-5f))));
#pragma unroll
    for (int i = 0; i < 10; ++i) {
      const unsigned t = bfbits(rbf(v[i] - mb) * inv);
      *(LAS unsigned*)(lds + L_XX + ((lane + 64 * (i >> 1)) * 2 + (i & 1)) * 8 + wave * 4) = t | (t << 16);
    }
  }
  v4u msk;
  {
    const unsigned mm = (lane & 1) ? 0xffff0000u : 0x0000ffffu;
    const int q = (lane & 7) >> 1;
    msk = (v4u){q == 0 ? mm : 0u, q == 1 ? mm : 0u, q == 2 ? mm : 0u, q == 3 ? mm : 0u};
  }
  asm volatile("" : "+s"(tag), "+v"(msk));
  BAR();
  if (wave == 0) STAMP(8);

  if (wave >= 4) {
    const gptr vb0 = (gptr)vc_ + bh0;
    v4u U[NP + 2][3];
#pragma unroll
    for (int u = 0; u < NP + 2; ++u) {
      if (u < 2) {
#pragma unroll
        for (int j = 0; j < 3; ++j) U[u][j] = R[4 + u * 3 + j];
      } else {
        gptr p = kb0 + (size_t)((rot + u) & 15) * STEP;
        asm volatile("" : "+s"(p));
#pragma unroll
        for (int j = 0; j < 3; ++j) {
          asm volatile("s_waitcnt vmcnt(5)" ::: "memory");
          U[u][j] = ld_nt(p + loff + j * 128);
          SB();
        }
        if (u & 1) post(lds, ww, u >> 1, lane);
      }
      if (u >= 2) ULOG(u - 2);
    }
    v4u (&A)[3] = U[NP], (&Bq)[3] = U[NP + 1];
    auto score = [&](float d, LAS float* dst) {
      d += dpp<XOR1>(d); d += dpp<XOR2>(d); d += dpp<HMIR>(d);
      if ((lane & 7) == 0) *dst = rbf(rbf(d) * 0.07216878364870322f);
      SB();
    };
#pragma unroll 1
    for (int b = 0; b < 2; ++b) {
      waitn<0, 3>(lds, 4, b + 1);
      if (ww == 0) STAMP(12 + b);
      v4u q[3];
#pragma unroll
      for (int j = 0; j < 3; ++j) q[j] = *(const LAS v4u*)(lds + L_QO + b * 384 + j * 128 + (lane & 7) * 16);
      const gptr cur = kb0 + b * bstr, nxt = b ? vb0 : kb0 + bstr;
      LAS float* sc = (LAS float*)(lds + L_SC) + b * 512 + ww * 8 + (lane >> 3);
#pragma unroll 1
      for (int it = b ? 0 : NP / 2; it < 8; ++it) {
        post(lds, ww, b * 8 + it, lane);
        const gptr base = it == 7 ? nxt : cur;
        const gptr pa = base + (size_t)((2 * it + 2 + rot) & 15) * STEP + loff, pb = base + (size_t)((2 * it + 3 + rot) & 15) * STEP + loff;
        float d = 0.f;
#pragma unroll
        for (int j = 0; j < 3; ++j) { d = dot8(A[j], q[j], d); asm volatile("" : "+v"(d)); SB(); A[j] = ld_nt(pa + j * 128); SB(); }
        score(d, sc + it * 64);
        ULOG(b * 16 + it * 2);
        d = 0.f;
#pragma unroll
        for (int j = 0; j < 3; ++j) { d = dot8(Bq[j], q[j], d); asm volatile("" : "+v"(d)); SB(); Bq[j] = ld_nt(pb + j * 128); SB(); }
        score(d, sc + it * 64 + 32);
        ULOG(b * 16 + it * 2 + 1);
      }
      if (b == 0) {
#pragma unroll
        for (int u = 0; u < NP; ++u) score(dot8(U[u][2], q[2], dot8(U[u][1], q[1], dot8(U[u][0], q[0], 0.f))), sc + u * 32);
      }
      STAMP(16 + b * 4 + ww);
    }
    f4 o[3];
    const bool useful = ((lane >> 3) & 1) == (lane >> 5);
    LAS char* part = lds + L_PART + ((ww * 2 + (lane >> 5)) * DH + (lane & 7) * 8 + ((lane >> 4) & 1) * 4) * 4;
    auto vstep = [&](const LAS unsigned* pp, gptr pa, gptr pb, bool more, int n) {
      const unsigned p0 = pp[0], p1 = pp[32];
      const v4u a0 = msk & p0, a1 = msk & p1;
      SB();
#pragma unroll
      for (int j = 0; j < 3; ++j) { o[j] = mfma(a0, A[j], o[j]); SB(); if (more) A[j] = ld_nt(pa + j * 128); SB(); }
      ULOG(32 + 2 * n);
#pragma unroll
      for (int j = 0; j < 3; ++j) { o[j] = mfma(a1, Bq[j], o[j]); SB(); if (more) Bq[j] = ld_nt(pb + j * 128); SB(); }
      ULOG(33 + 2 * n);
    };
    auto vflush = [&](int b) {
      if (useful) {
#pragma unroll
        for (int j = 0; j < 3; ++j) *(LAS f4*)(part + b * (8 * DH * 4) + j * 256) = o[j];
      }
    };
    const LAS unsigned* pp = (const LAS unsigned*)(lds + L_PP) + ww * 8 + (lane >> 3);
#pragma unroll 1
    for (int n = 0; n < 15; ++n) {
      post(lds, ww, 16 + n, lane);
      if ((n & 7) == 0) {
        wait1<0>(lds, 7, 1 + (n >> 3));
#pragma unroll
        for (int j = 0; j < 3; ++j) o[j] = (f4){0.f, 0.f, 0.f, 0.f};
        STAMP(24 + (n >> 3) * 4 + ww);
      }
      const gptr base = n >= 7 ? vb0 + bstr : vb0;
      vstep(pp + n * 64, base + (size_t)((2 * n + 2 + rot) & 15) * STEP + loff, base + (size_t)((2 * n + 3 + rot) & 15) * STEP + loff, true, n);
      if (n == 7) vflush(0);
    }
    post(lds, ww, 31, lane);
    vstep(pp + 15 * 64, vb0, vb0, false, 15);
    vflush(1);
    post(lds, ww, 32, lane);
    STAMP(32 + ww);
  } else if (wave < 3) {
    gu64* xbox = (gu64*)(ws + WS_X) + h * (4 * 8 * DH);
    gu64* stbox = (gu64*)(ws + WS_ST) + h * 32;
    {
      f4 a0 = {0.f, 0.f, 0.f, 0.f}, a1 = a0;
      const LAS char* xp = lds + L_XX + (mem * 80 + (lane >> 3)) * 8;
      v2u xv[10];
#pragma unroll
      for (int i = 0; i < 10; ++i) xv[i] = *(const LAS v2u*)(xp + i * 64);
      SB();
#pragma unroll
      for (int i = 0; i < 10; ++i) { a0 = mfma(msk & xv[i].x, R[i], a0); a1 = mfma(msk & xv[i].y, R[i], a1); }
      const f4 s = fold(a0, a1);
      gu64* dst = xbox + ((lane >> 5) * 8 + mem) * DH + wave * 64 + ((lane >> 4) & 1) * 8 + (lane & 7);
      if ((lane & 8) == 0) {
#pragma unroll
        for (int r = 0; r < 4; ++r) put(dst + r * 16, tag, s[r]);
      }
      if (wave == 0) STAMP(9);
      wait1<4>(lds, 0, T_Q);
    }
    const int ot = wave * 64 + lane, qd = wave * 64 + (lane & 7) * 8 + ((lane >> 3) & 1) * 4 + (lane >> 4);
#pragma unroll 1
    for (int st = 0; st < 4; ++st) {
      if (st == 2 && wave == 0) {
#pragma unroll 1
        for (int b = 0; b < 2; ++b) {
          waitn<4, 4>(lds, 0, b * 8 + 8);
          const f4 s0 = *(const LAS f4*)(lds + L_SC + b * 2048 + lane * 32), s1 = *(const LAS f4*)(lds + L_SC + b * 2048 + lane * 32 + 16);
          const float s[8] = {s0.x, s0.y, s0.z, s0.w, s1.x, s1.y, s1.z, s1.w};
          constexpr float L2E = 1.4426950408889634f;
          float m = s[0];
#pragma unroll
          for (int i = 1; i < 8; ++i) m = fmaxf(m, s[i]);
          m = wave_max(m);
          const float ms = m == -INFINITY ? 0.f : m;
          float l = 0.f;
#pragma unroll
          for (int i = 0; i < 8; ++i) l += __builtin_amdgcn_exp2f((s[i] - ms) * L2E);
          l = wave_sum(l);
          if (lane < 2) put(stbox + b * 16 + mem * 2 + lane, tag, lane ? l : m);
          wait1<4>(lds, 0, b * 8 + 8 + T_ST);
          float st1[1];
          sweep<1, 16>(stbox + b * 16 + (lane & 15), 0, tag, st1);
          const float oth = dpp<XOR1>(st1[0]), mj = (lane & 1) ? oth : st1[0], lj = (lane & 1) ? st1[0] : oth;
          float M = fmaxf(mj, dpp<ROR2>(mj)); M = fmaxf(M, dpp<ROR4>(M)); M = fmaxf(M, dpp<ROR8>(M));
          float Ls = lj * __builtin_amdgcn_exp2f((mj - M) * L2E);
          Ls += dpp<ROR2>(Ls); Ls += dpp<ROR4>(Ls); Ls += dpp<ROR8>(Ls);
          const float inv = __builtin_amdgcn_rcpf(__uint_as_float(__builtin_amdgcn_readfirstlane(__float_as_uint(Ls))));
          unsigned p[8];
#pragma unroll
          for (int i = 0; i < 8; ++i) { const unsigned t = bfbits(__builtin_amdgcn_exp2f((s[i] - M) * L2E) * inv); p[i] = t | (t << 16); }
          *(LAS v4u*)(lds + L_PP + b * 2048 + lane * 32) = (v4u){p[0], p[1], p[2], p[3]};
          *(LAS v4u*)(lds + L_PP + b * 2048 + lane * 32 + 16) = (v4u){p[4], p[5], p[6], p[7]};
          post(lds, 7, b + 1, lane);
          STAMP(36 + b);
        }
      }
      if (st >= 2) {
        if (st == 2) waitn<4, 4>(lds, 0, 24); else { wait1<2>(lds, 0, 31); waitn<0, 4>(lds, 0, 32); }
        float s = 0.f;
#pragma unroll
        for (int r = 0; r < 8; ++r) s += *(const LAS float*)(lds + L_PART + (((st - 2) * 8 + r) * DH + ot) * 4);
        put(xbox + (st * 8 + mem) * DH + ot, tag, s);
        if (wave == 0) STAMP(36 + st);
        if (st == 2) wait1<4>(lds, 0, T_O);
      }
      float v[8];
      sweep<8, 1>(xbox + st * 8 * DH + ot, DH, tag, v, st < 3);
      *(LAS u16*)(lds + L_QO + (st * DH + (st < 2 ? qd : ot)) * 2) = (u16)bfbits(((v[0] + v[1]) + (v[2] + v[3])) + ((v[4] + v[5]) + (v[6] + v[7])));
      post(lds, 4 + wave, st + 1, lane);
      if (wave == 0) STAMP(40 + st);
    }
  } else {
    const unsigned lds0 = (unsigned)(size_t)lds;
    if (w >= 64) {
      const int fb = (w & 7) * 3 + (w >> 6) - 1;
      const size_t goff = (size_t)mem * (80 * F * 2) + fb * 128 + ((lane >> 3) * (F * 2) + (lane & 7) * 16);
      v4u g[10], u[10];
#pragma unroll
      for (int i = 0; i < 10; ++i) { g[i] = ld_nt((gptr)wg_ + goff + (size_t)i * (8 * F * 2)); u[i] = ld_nt((gptr)wu_ + goff + (size_t)i * (8 * F * 2)); }
      SB();
      f4 g0 = {0.f, 0.f, 0.f, 0.f}, g1 = g0, u0 = g0, u1 = g0;
      const LAS char* xp = lds + L_XX + (mem * 80 + (lane >> 3)) * 8;
#pragma unroll
      for (int i = 0; i < 10; ++i) {
        const v2u xv = *(const LAS v2u*)(xp + i * 64);
        const v4u a0 = msk & xv.x, a1 = msk & xv.y;
        g0 = mfma(a0, g[i], g0); g1 = mfma(a1, g[i], g1); u0 = mfma(a0, u[i], u0); u1 = mfma(a1, u[i], u1);
      }
      const f4 gs = fold(g0, g1), us = fold(u0, u1);
      gu64* fbox = (gu64*)(ws + WS_F) + fb * (64 * 32);
      if ((lane & 8) == 0) {
        gu64* dst = fbox + (((lane >> 4) & 1) * 32 + mem) * 32 + (lane >> 5) * 8 + (lane & 7);
#pragma unroll
        for (int r = 0; r < 4; ++r) { put(dst + r * 256, tag, gs[r]); put(dst + r * 256 + 16, tag, us[r]); }
      }
      stage_rows((gptr)wd_ + (size_t)fb * (64 * D * 2) + mem * 160, lds0 + L_WD, 10, lane);
      STAMP(44);
      float v[4];
      wait1<8>(lds, 0, T_F);
      sweep<4, 16>(fbox + mem * 256 + lane, 64, tag, v);
      const float sv = (v[0] + v[1]) + (v[2] + v[3]);
      auto t = __builtin_amdgcn_permlane32_swap(__float_as_uint(sv), __float_as_uint(sv), false, false);
      const unsigned tot = __float_as_uint(__uint_as_float(t[0]) + __uint_as_float(t[1]));
      auto gu = __builtin_amdgcn_permlane16_swap(tot, tot, false, false);
      const float gt = rbf(__uint_as_float(gu[0])), up = rbf(__uint_as_float(gu[1]));
      const float t2 = 2.f * 0.7978845608028654f * (gt + 0.044715f * gt * gt * gt);
      gu64* hbox = (gu64*)(ws + WS_H) + fb * 128;
      if (lane < 16) put(hbox + (lane >> 3) * 64 + mem * 8 + (lane & 7), tag, rbf(gt / (1.f + __expf(-t2))) * up);
      float hv[2];
      wait1<8>(lds, 0, T_H);
      sweep<2, 16>(hbox + lane, 64, tag, hv);
      *(LAS u16*)(lds + L_HB + (lane & 7) * 16 + (lane >> 3) * 2) = (u16)bfbits(hv[0]);
      *(LAS u16*)(lds + L_HB + 128 + (lane & 7) * 16 + (lane >> 3) * 2) = (u16)bfbits(hv[1]);
      asm volatile("s_waitcnt vmcnt(0) lgkmcnt(0)" ::: "memory");
      const LAS char* ap = lds + L_HB + ((lane >> 2) & 1) * 128 + (lane >> 4) * 16;
      const v4u a0 = *(const LAS v4u*)ap, a1 = *(const LAS v4u*)(ap + 64);
      const LAS char* bp = lds + L_WD + ((lane >> 4) * 8 + ((lane >> 2) & 3)) * 160 + (lane & 3) * 8;
      gu64* gdst = (gu64*)(ws + WS_G) + ((mem * 10 + ((lane >> 4) & 1)) * 24 + fb) * 16 + (lane & 15);
#pragma unroll 1
      for (int tt = 0; tt < 5; ++tt) {
        f4 y = mfma(a0, tr_frag(bp + tt * 32), (f4){0.f, 0.f, 0.f, 0.f});
        y = mfma(a1, tr_frag(bp + tt * 32 + 5120), y);
        if (lane < 32) put(gdst + tt * (2 * 24 * 16), tag, y.x);
      }
      STAMP(45);
    }
    float gate = 0.f, xres = 0.f;
    if (w < 80) {
      xres = __uint_as_float((unsigned)((const GAS u16*)x_)[(w & 1) * D + (w >> 1) * 16 + (lane & 15)] << 16);
      wait1<8>(lds, 0, T_G);
      float v[6];
      sweep<6, 16>((gu64*)(ws + WS_G) + w * (24 * 16) + lane, 64, tag, v);
      const float gp = rbf(xrow_sum(((v[0] + v[1]) + (v[2] + v[3])) + (v[4] + v[5])));
      gate = rbf(gp / (1.f + __expf(-gp)));
      STAMP(46);
    }
    wait1<2>(lds, 0, T_WO);
    STAMP(47);
    stage_rows((gptr)wo_ + (size_t)h * (DH * D * 2) + mem * 160, lds0 + L_WO, 30, lane);
    asm volatile("s_waitcnt vmcnt(0)" ::: "memory");
    STAMP(48);
    BAR();
    if (w < 80) {
      float v[8];
      sweep<8, 1>((gu64*)(ws + WS_Y) + w * (32 * 16) + lane, 64, tag, v);
      const float s = rbf(xrow_sum(((v[0] + v[1]) + (v[2] + v[3])) + ((v[4] + v[5]) + (v[6] + v[7]))));
      if (lane < 16) ((GAS u16*)out_)[(w & 1) * D + (w >> 1) * 16 + lane] = (u16)bfbits(xres + rbf(s * gate));
      if (w == 0 && lane == 0) __hip_atomic_store((GAS unsigned*)(ws + WS_EPOCH), tag, RLX_AGENT);
      STAMPG(56);
    }
    return;
  }
  const int tt = wave >= 4 ? ww : 4;
  const LAS char* ap = lds + L_QO + 768 + ((lane >> 2) & 1) * 384 + (lane >> 4) * 16;
  const LAS char* bp = lds + L_WO + ((lane >> 4) * 8 + ((lane >> 2) & 3)) * 160 + tt * 32 + (lane & 3) * 8;
  gu64* ydst = (gu64*)(ws + WS_Y) + (((mem * 5 + tt) * 2 + ((lane >> 4) & 1)) * 32 + h) * 16 + (lane & 15);
  asm volatile("" : "+v"(ap), "+v"(bp), "+v"(ydst));
  BAR();
  if (wave == 1) { STAMPG(54); if (lane < 54) ((GAS unsigned*)(ws + (8 << 20)))[w * 64 + lane] = ((LAS unsigned*)(lds + L_DG))[lane];
    for (int i = 0; i < 4; ++i) ((GAS unsigned*)(ws + (9 << 20)))[w * 256 + i * 64 + lane] = ((LAS unsigned*)(lds + L_DG + 256))[i * 64 + lane];
    if (lane == 0) ((GAS unsigned*)(ws + (8 << 20)))[w * 64 + 63] = (unsigned)__builtin_amdgcn_s_getreg(20 | (3 << 11)) & 0xfu; }
  if (wave >= 2) {
    v4u a[6], bf[6];
#pragma unroll
    for (int ks = 0; ks < 6; ++ks) { a[ks] = *(const LAS v4u*)(ap + ks * 64); bf[ks] = tr_frag(bp + ks * 5120); }
    SB();
    f4 y = {0.f, 0.f, 0.f, 0.f};
#pragma unroll
    for (int ks = 0; ks < 6; ++ks) y = mfma(a[ks], bf[ks], y);
    if (lane < 32) put(ydst, tag, y.x);
    if (wave == 2) STAMPG(55);
  }
  asm volatile(".Lfd_last:");
}

__global__ void __launch_bounds__(256) kspin(const unsigned* st) {
  __shared__ unsigned v[256], s0[256];
  const int t = threadIdx.x;
  s0[t] = st[t * 64]; v[t] = st[t * 64 + SIDX];
  __syncthreads();
  unsigned t0 = s0[0];
  for (int i = 1; i < 256; ++i) if ((int)(s0[i] - t0) < 0) t0 = s0[i];
  const int n = SIDX == 56 ? 80 : 256;
  int rank = 0; const unsigned mine = v[t] - t0;
  for (int i = 0; i < n; ++i) { const unsigned o = v[i] - t0; rank += (o < mine) || (o == mine && i < t); }
  const int want = KIND == 0 ? 0 : KIND == 1 ? n / 2 : n - 1;
  if (t < n && rank == want) {
    unsigned m = KIND == 3 ? 0u : mine; if (m > 8000u) m = 8000u;
    const unsigned long long a = __builtin_amdgcn_s_memrealtime();
    while (__builtin_amdgcn_s_memrealtime() - a < (unsigned long long)m * 10u) __builtin_amdgcn_s_sleep(1);
  }
}
extern "C" void kernel_launch(void* const* d_in, const int* in_sizes, int n_in, void* d_out, int out_size, void* d_ws, size_t ws_size,
                              hipStream_t stream) {
  fused_decode<<<256, 512, 0, stream>>>((const u16*)d_in[0], (const u16*)d_in[1], (const u16*)d_in[2], (const u16*)d_in[3],
                                        (const u16*)d_in[4], (const u16*)d_in[6], (const u16*)d_in[7], (const u16*)d_in[8],
                                        (u16*)d_out, (char*)d_ws);
  kspin<<<1, 256, 0, stream>>>((const unsigned*)((char*)d_ws + (8 << 20)));
}
```

```cpp
#define PRE 0
#define SPIN 0
#define METRIC 0
#include <hip/hip_runtime.h>
#include <stdint.h>

typedef unsigned short u16;
typedef unsigned long long u64;
typedef unsigned v4u __attribute__((ext_vector_type(4)));
typedef unsigned v2u __attribute__((ext_vector_type(2)));
typedef float f4 __attribute__((ext_vector_type(4)));
typedef short s4 __attribute__((ext_vector_type(4)));
typedef __bf16 bf2 __attribute__((ext_vector_type(2)));
typedef __bf16 bf8 __attribute__((ext_vector_type(8)));
#define GAS __attribute__((address_space(1)))
#define LAS __attribute__((address_space(3)))
#define RLX_AGENT __ATOMIC_RELAXED, __HIP_MEMORY_SCOPE_AGENT
typedef const GAS char* gptr;
typedef GAS u64 gu64;

#define ROT_MODE 0
#define NP 10
#define T_Q 3
#define T_ST 4
#define T_O 28
#define T_WO 30
#define T_F 7
#define T_H 11
#define T_G 18
#define LAZY 24
#define QDELAY 64
#define CLEAR 0

constexpr int D = 640, H = 32, SK = 4096, DH = 192, F = 1536, NTHREADS = 512;
constexpr int STEP = 32 * 3072;
constexpr unsigned SPIN_CAP = 1u << 16;

constexpr int WS_EPOCH = 0;
constexpr int WS_ST = 4096;
constexpr int WS_H = WS_ST + H * 2 * 16 * 8;
constexpr int WS_F = WS_H + 24 * 128 * 8;
constexpr int WS_X = WS_F + 24 * 64 * 32 * 8;
constexpr int WS_Y = WS_X + H * 4 * 8 * DH * 8;
constexpr int WS_G = WS_Y + 80 * 32 * 16 * 8;
constexpr int L_WO = 0;
constexpr int L_WD = 30720;
constexpr int L_XX = L_WD + 10240;
constexpr int L_PART = L_XX + 5120;
constexpr int L_QO = L_PART + 12288;
constexpr int L_HB = L_QO + 1536;
constexpr int L_SC = L_HB + 256;
constexpr int L_PP = L_SC + 4096;
constexpr int L_FL = L_PP + 4096;
constexpr int L_DG = L_FL + 64;
constexpr int L_TOTAL = 84 * 1024;

__device__ __forceinline__ float rbf(float f) { return (float)(__bf16)f; }
__device__ __forceinline__ unsigned bfbits(float f) { return (unsigned)__builtin_bit_cast(u16, (__bf16)f); }
__device__ __forceinline__ float dot2(unsigned a, unsigned b, float acc) {
  return __builtin_amdgcn_fdot2_f32_bf16(__builtin_bit_cast(bf2, a), __builtin_bit_cast(bf2, b), acc, false);
}
__device__ __forceinline__ float dot8(v4u a, v4u b, float acc) {
  return dot2(a.w, b.w, dot2(a.z, b.z, dot2(a.y, b.y, dot2(a.x, b.x, acc))));
}
__device__ __forceinline__ f4 mfma(v4u a, v4u b, f4 c) {
  return __builtin_amdgcn_mfma_f32_16x16x32_bf16(__builtin_bit_cast(bf8, a), __builtin_bit_cast(bf8, b), c, 0, 0, 0);
}
template <int CTRL> __device__ __forceinline__ float dpp(float x) {
  return __builtin_bit_cast(float, __builtin_amdgcn_mov_dpp(__builtin_bit_cast(int, x), CTRL, 0xf, 0xf, true));
}
constexpr int XOR1 = 0xB1, XOR2 = 0x4E, HMIR = 0x141, ROR2 = 0x122, ROR4 = 0x124, ROR8 = 0x128;
__device__ __forceinline__ float xrow_sum(float x) {
  auto s = __builtin_amdgcn_permlane16_swap(__float_as_uint(x), __float_as_uint(x), false, false);
  x = __uint_as_float(s[0]) + __uint_as_float(s[1]);
  auto t = __builtin_amdgcn_permlane32_swap(__float_as_uint(x), __float_as_uint(x), false, false);
  return __uint_as_float(t[0]) + __uint_as_float(t[1]);
}
__device__ __forceinline__ float xrow_max(float x) {
  auto s = __builtin_amdgcn_permlane16_swap(__float_as_uint(x), __float_as_uint(x), false, false);
  x = fmaxf(__uint_as_float(s[0]), __uint_as_float(s[1]));
  auto t = __builtin_amdgcn_permlane32_swap(__float_as_uint(x), __float_as_uint(x), false, false);
  return fmaxf(__uint_as_float(t[0]), __uint_as_float(t[1]));
}
__device__ __forceinline__ float wave_sum(float x) {
  x += dpp<XOR1>(x); x += dpp<XOR2>(x); x += dpp<HMIR>(x); x += dpp<ROR8>(x);
  return xrow_sum(x);
}
__device__ __forceinline__ float wave_max(float x) {
  x = fmaxf(x, dpp<XOR1>(x)); x = fmaxf(x, dpp<XOR2>(x)); x = fmaxf(x, dpp<HMIR>(x)); x = fmaxf(x, dpp<ROR8>(x));
  return xrow_max(x);
}
__device__ __forceinline__ v4u ld_nt(gptr p) { return __builtin_nontemporal_load((const GAS v4u*)p); }
__device__ __forceinline__ v4u ld_pl(gptr p) { return *(const GAS v4u*)p; }
#define SB() __builtin_amdgcn_sched_barrier(0)
#define BAR() asm volatile("s_waitcnt lgkmcnt(0)\n\ts_barrier" ::: "memory")
__device__ __forceinline__ void put(gu64* g, unsigned tag, float v) {
  __hip_atomic_store(g, ((u64)tag << 32) | __float_as_uint(v), RLX_AGENT);
}
template <int N, int SLEEP> __device__ __forceinline__ void sweep(gu64* g, int stride, unsigned tag, float (&v)[N], bool lazy = false) {
#pragma unroll 1
  for (unsigned spins = 0; spins < SPIN_CAP; ++spins) {
    bool ok = true;
#pragma unroll
    for (int k = 0; k < N; ++k) {
      const u64 x = __hip_atomic_load(g + k * stride, RLX_AGENT);
      v[k] = __uint_as_float((unsigned)x);
      ok &= (unsigned)(x >> 32) == tag;
    }
    if (__all(ok)) return;
    if (SLEEP) __builtin_amdgcn_s_sleep(SLEEP);
    if (lazy) __builtin_amdgcn_s_sleep(LAZY);
  }
#pragma unroll
  for (int k = 0; k < N; ++k) v[k] = __builtin_nanf("");
}
__device__ __forceinline__ void post(LAS char* lds, int word, unsigned v, int lane) {
  asm volatile("" ::: "memory");
  if (lane == 0) *(volatile LAS unsigned*)(lds + L_FL + word * 4) = v;
  asm volatile("" ::: "memory");
}
template <int SLEEP> __device__ __forceinline__ void wait1(LAS char* lds, int word, unsigned want) {
  while (__builtin_amdgcn_readfirstlane(*(volatile LAS unsigned*)(lds + L_FL + word * 4)) < want) { if (SLEEP) __builtin_amdgcn_s_sleep(SLEEP); }
  asm volatile("" ::: "memory");
}
template <int SLEEP, int N> __device__ __forceinline__ void waitn(LAS char* lds, int word, unsigned want) {
  for (;;) {
    const v4u v = *(volatile LAS v4u*)(lds + L_FL + word * 4);
    unsigned m = min(min(v.x, v.y), v.z);
    if (N == 4) m = min(m, v.w);
    if (__builtin_amdgcn_readfirstlane(m) >= want) break;
    if (SLEEP) __builtin_amdgcn_s_sleep(SLEEP);
  }
  asm volatile("" ::: "memory");
}
__device__ __forceinline__ void stage_rows(gptr src, unsigned lds_dst, int n, int lane) {
#pragma unroll 1
  for (int i = 0; i < n; ++i) {
    const unsigned ci = i * 64 + lane, row = (ci * 52429u) >> 19;
    const gptr p = src + (row * (D * 2 - 160) + ci * 16);
    unsigned keep;
    asm volatile("s_mov_b32 %0, m0\n\ts_mov_b32 m0, %2\n\ts_nop 0\n\tglobal_load_lds_dwordx4 %1, off\n\ts_mov_b32 m0, %0"
                 : "=&s"(keep) : "v"(p), "s"(lds_dst + i * 1024) : "memory");
  }
}
__device__ __forceinline__ v4u tr_frag(const LAS char* p) {
  const v2u b0 = __builtin_bit_cast(v2u, __builtin_amdgcn_ds_read_tr16_b64_v4i16((LAS s4*)p));
  const v2u b1 = __builtin_bit_cast(v2u, __builtin_amdgcn_ds_read_tr16_b64_v4i16((LAS s4*)(p + 640)));
  return (v4u){b0.x, b0.y, b1.x, b1.y};
}
__device__ __forceinline__ f4 fold(f4 a0, f4 a1) {
  f4 o;
#pragma unroll
  for (int r = 0; r < 4; ++r) {
    auto t = __builtin_amdgcn_permlane32_swap(__float_as_uint(a0[r]), __float_as_uint(a1[r]), false, false);
    o[r] = __uint_as_float(t[0]) + dpp<ROR8>(__uint_as_float(t[1]));
  }
  return o;
}

#define STAMP(i)
#define ULOG(i)
#define STAMPG(i)

extern "C" __global__ void __launch_bounds__(512, 2)
fused_decode(const u16* x_, const u16* kc_, const u16* vc_, const u16* wq_, const u16* wo_, const u16* wg_, const u16* wu_,
             const u16* wd_, u16* out_, char* ws_) {
  __shared__ __attribute__((aligned(16))) char lds_raw[L_TOTAL];
  LAS char* lds = (LAS char*)lds_raw;
  GAS char* ws = (GAS char*)ws_;
  const int tid = threadIdx.x, lane = tid & 63, wave = __builtin_amdgcn_readfirstlane(tid >> 6);
  const int w = blockIdx.x, h = (w & 7) * 4 + (w >> 6), mem = (w >> 3) & 7;
  const int ww = wave - 4;
  const int rot = ROT_MODE == 0 ? 0 : ROT_MODE == 1 ? h * 5 : (h & 3) * 4;
  const unsigned loff = (lane >> 3) * 384 + (lane & 7) * 16;
  const size_t bh0 = (size_t)h * (SK * DH * 2) + (size_t)(mem * 4 + ww) * 3072, bstr = (size_t)H * SK * DH * 2;
  const gptr kb0 = (gptr)kc_ + bh0;
  v4u R[10];
  if (tid < 4) {
    u64 pc; unsigned back, len, keep;
    asm volatile("s_getpc_b64 %0\n.Lfd_here:\n\ts_mov_b32 %1, .Lfd_here-fused_decode\n\ts_mov_b32 %2, .Lfd_last-fused_decode" : "=s"(pc), "=s"(back), "=s"(len));
    const unsigned c0 = ((w >> 3) * 4 + tid) * 128u;
    asm volatile("s_mov_b32 %0, m0\n\ts_mov_b32 m0, %3\n\ts_nop 0\n\tglobal_load_lds_dword %1, %2\n\ts_mov_b32 m0, %0"
                 : "=&s"(keep) : "v"(c0 < len ? c0 : 0u), "s"(pc - back), "s"((unsigned)(size_t)lds + L_FL + 48) : "memory");
  }
  SB();
  STAMP(wave);
  unsigned xr[5];
  if (wave < 3) {
    const GAS unsigned* xp = (const GAS unsigned*)x_ + (wave & 1) * (D / 2) + lane;
#pragma unroll
    for (int i = 0; i < 5; ++i) xr[i] = xp[64 * i];
    SB();
    gptr qp = (gptr)wq_ + ((size_t)mem * (80 * H * DH * 2) + h * (DH * 2) + wave * 128);
    const unsigned qoff = (lane >> 3) * (H * DH * 2) + (lane & 7) * 16;
#pragma unroll
    for (int i = 0; i < 10; ++i) { asm volatile("" : "+s"(qp)); R[i] = ld_nt(qp + qoff); qp += 8 * H * DH * 2; SB(); }
  }
  unsigned tag = *(const __attribute__((address_space(4))) unsigned*)(ws_ + WS_EPOCH) + 1u;
  asm volatile("s_barrier" ::: "memory");
  if (wave >= 4) {
#pragma unroll
    for (int u = 0; u < 2; ++u) {
      gptr p = kb0 + (size_t)((rot + u) & 15) * STEP;
      asm volatile("" : "+s"(p));
#pragma unroll
      for (int j = 0; j < 3; ++j) { R[4 + u * 3 + j] = ld_nt(p + loff + j * 128); SB(); }
    }
    if (tid < 264) *(volatile LAS unsigned*)(lds + L_FL + lane * 4) = 0u;
  } else if (wave < 2) {
    float v[10], s = 0.f;
#pragma unroll
    for (int i = 0; i < 5; ++i) { v[2 * i] = __uint_as_float(xr[i] << 16); v[2 * i + 1] = __uint_as_float(xr[i] & 0xffff0000u); s += v[2 * i] + v[2 * i + 1]; }
    const float mean = wave_sum(s) * (1.f / D);
    float q = 0.f;
#pragma unroll
    for (int i = 0; i < 10; ++i) { const float dd = v[i] - mean; q += dd * dd; }
    const float var = rbf(wave_sum(q) * (1.f / D)), mb = rbf(mean);
    const float inv = __builtin_amdgcn_rcpf(rbf(__builtin_amdgcn_sqrtf(rbf(var + 1e-5f))));
#pragma unroll
    for (int i = 0; i < 10; ++i) {
      const unsigned t = bfbits(rbf(v[i] - mb) * inv);
      *(LAS unsigned*)(lds + L_XX + ((lane + 64 * (i >> 1)) * 2 + (i & 1)) * 8 + wave * 4) = t | (t << 16);
    }
  }
  v4u msk;
  {
    const unsigned mm = (lane & 1) ? 0xffff0000u : 0x0000ffffu;
    const int q = (lane & 7) >> 1;
    msk = (v4u){q == 0 ? mm : 0u, q == 1 ? mm : 0u, q == 2 ? mm : 0u, q == 3 ? mm : 0u};
  }
  asm volatile("" : "+s"(tag), "+v"(msk));
  BAR();
  if (wave == 0) STAMP(8);

  if (wave >= 4) {
    const gptr vb0 = (gptr)vc_ + bh0;
    v4u U[NP + 2][3];
#pragma unroll
    for (int u = 0; u < NP + 2; ++u) {
      if (u < 2) {
#pragma unroll
        for (int j = 0; j < 3; ++j) U[u][j] = R[4 + u * 3 + j];
      } else {
        gptr p = kb0 + (size_t)((rot + u) & 15) * STEP;
        asm volatile("" : "+s"(p));
#pragma unroll
        for (int j = 0; j < 3; ++j) {
          asm volatile("s_waitcnt vmcnt(5)" ::: "memory");
          U[u][j] = ld_nt(p + loff + j * 128);
          SB();
        }
        if (u & 1) post(lds, ww, u >> 1, lane);
      }
      if (u >= 2) ULOG(u - 2);
    }
    v4u (&A)[3] = U[NP], (&Bq)[3] = U[NP + 1];
    auto score = [&](float d, LAS float* dst) {
      d += dpp<XOR1>(d); d += dpp<XOR2>(d); d += dpp<HMIR>(d);
      if ((lane & 7) == 0) *dst = rbf(rbf(d) * 0.07216878364870322f);
      SB();
    };
#pragma unroll 1
    for (int b = 0; b < 2; ++b) {
      waitn<0, 3>(lds, 4, b + 1);
      if (ww == 0) STAMP(12 + b);
      v4u q[3];
#pragma unroll
      for (int j = 0; j < 3; ++j) q[j] = *(const LAS v4u*)(lds + L_QO + b * 384 + j * 128 + (lane & 7) * 16);
      const gptr cur = kb0 + b * bstr, nxt = b ? vb0 : kb0 + bstr;
      LAS float* sc = (LAS float*)(lds + L_SC) + b * 512 + ww * 8 + (lane >> 3);
#pragma unroll 1
      for (int it = b ? 0 : NP / 2; it < 8; ++it) {
        post(lds, ww, b * 8 + it, lane);
        const gptr base = it == 7 ? nxt : cur;
        const gptr pa = base + (size_t)((2 * it + 2 + rot) & 15) * STEP + loff, pb = base + (size_t)((2 * it + 3 + rot) & 15) * STEP + loff;
        float d = 0.f;
#pragma unroll
        for (int j = 0; j < 3; ++j) { d = dot8(A[j], q[j], d); asm volatile("" : "+v"(d)); SB(); A[j] = ld_nt(pa + j * 128); SB(); }
        score(d, sc + it * 64);
        ULOG(b * 16 + it * 2);
        d = 0.f;
#pragma unroll
        for (int j = 0; j < 3; ++j) { d = dot8(Bq[j], q[j], d); asm volatile("" : "+v"(d)); SB(); Bq[j] = ld_nt(pb + j * 128); SB(); }
        score(d, sc + it * 64 + 32);
        ULOG(b * 16 + it * 2 + 1);
      }
      if (b == 0) {
#pragma unroll
        for (int u = 0; u < NP; ++u) score(dot8(U[u][2], q[2], dot8(U[u][1], q[1], dot8(U[u][0], q[0], 0.f))), sc + u * 32);
      }
      STAMP(16 + b * 4 + ww);
    }
    f4 o[3];
    const bool useful = ((lane >> 3) & 1) == (lane >> 5);
    LAS char* part = lds + L_PART + ((ww * 2 + (lane >> 5)) * DH + (lane & 7) * 8 + ((lane >> 4) & 1) * 4) * 4;
    auto vstep = [&](const LAS unsigned* pp, gptr pa, gptr pb, bool more, int n) {
      const unsigned p0 = pp[0], p1 = pp[32];
      const v4u a0 = msk & p0, a1 = msk & p1;
      SB();
#pragma unroll
      for (int j = 0; j < 3; ++j) { o[j] = mfma(a0, A[j], o[j]); SB(); if (more) A[j] = ld_nt(pa + j * 128); SB(); }
      ULOG(32 + 2 * n);
#pragma unroll
      for (int j = 0; j < 3; ++j) { o[j] = mfma(a1, Bq[j], o[j]); SB(); if (more) Bq[j] = ld_nt(pb + j * 128); SB(); }
      ULOG(33 + 2 * n);
    };
    auto vflush = [&](int b) {
      if (useful) {
#pragma unroll
        for (int j = 0; j < 3; ++j) *(LAS f4*)(part + b * (8 * DH * 4) + j * 256) = o[j];
      }
    };
    const LAS unsigned* pp = (const LAS unsigned*)(lds + L_PP) + ww * 8 + (lane >> 3);
#pragma unroll 1
    for (int n = 0; n < 15; ++n) {
      post(lds, ww, 16 + n, lane);
      if ((n & 7) == 0) {
        wait1<0>(lds, 7, 1 + (n >> 3));
#pragma unroll
        for (int j = 0; j < 3; ++j) o[j] = (f4){0.f, 0.f, 0.f, 0.f};
        STAMP(24 + (n >> 3) * 4 + ww);
      }
      const gptr base = n >= 7 ? vb0 + bstr : vb0;
      vstep(pp + n * 64, base + (size_t)((2 * n + 2 + rot) & 15) * STEP + loff, base + (size_t)((2 * n + 3 + rot) & 15) * STEP + loff, true, n);
      if (n == 7) vflush(0);
    }
    post(lds, ww, 31, lane);
    vstep(pp + 15 * 64, vb0, vb0, false, 15);
    vflush(1);
    post(lds, ww, 32, lane);
    STAMP(32 + ww);
  } else if (wave < 3) {
    gu64* xbox = (gu64*)(ws + WS_X) + h * (4 * 8 * DH);
    gu64* stbox = (gu64*)(ws + WS_ST) + h * 32;
    {
      f4 a0 = {0.f, 0.f, 0.f, 0.f}, a1 = a0;
      const LAS char* xp = lds + L_XX + (mem * 80 + (lane >> 3)) * 8;
      v2u xv[10];
#pragma unroll
      for (int i = 0; i < 10; ++i) xv[i] = *(const LAS v2u*)(xp + i * 64);
      SB();
#pragma unroll
      for (int i = 0; i < 10; ++i) { a0 = mfma(msk & xv[i].x, R[i], a0); a1 = mfma(msk & xv[i].y, R[i], a1); }
      const f4 s = fold(a0, a1);
      gu64* dst = xbox + ((lane >> 5) * 8 + mem) * DH + wave * 64 + ((lane >> 4) & 1) * 8 + (lane & 7);
      if ((lane & 8) == 0) {
#pragma unroll
        for (int r = 0; r < 4; ++r) put(dst + r * 16, tag, s[r]);
      }
      if (wave == 0) STAMP(9);
      wait1<4>(lds, 0, T_Q);
    }
    const int ot = wave * 64 + lane, qd = wave * 64 + (lane & 7) * 8 + ((lane >> 3) & 1) * 4 + (lane >> 4);
#pragma unroll 1
    for (int st = 0; st < 4; ++st) {
      if (st == 2 && wave == 0) {
#pragma unroll 1
        for (int b = 0; b < 2; ++b) {
          waitn<4, 4>(lds, 0, b * 8 + 8);
          const f4 s0 = *(const LAS f4*)(lds + L_SC + b * 2048 + lane * 32), s1 = *(const LAS f4*)(lds + L_SC + b * 2048 + lane * 32 + 16);
          const float s[8] = {s0.x, s0.y, s0.z, s0.w, s1.x, s1.y, s1.z, s1.w};
          constexpr float L2E = 1.4426950408889634f;
          float m = s[0];
#pragma unroll
          for (int i = 1; i < 8; ++i) m = fmaxf(m, s[i]);
          m = wave_max(m);
          const float ms = m == -INFINITY ? 0.f : m;
          float l = 0.f;
#pragma unroll
          for (int i = 0; i < 8; ++i) l += __builtin_amdgcn_exp2f((s[i] - ms) * L2E);
          l = wave_sum(l);
          if (lane < 2) put(stbox + b * 16 + mem * 2 + lane, tag, lane ? l : m);
          wait1<4>(lds, 0, b * 8 + 8 + T_ST);
          float st1[1];
          sweep<1, 16>(stbox + b * 16 + (lane & 15), 0, tag, st1);
          const float oth = dpp<XOR1>(st1[0]), mj = (lane & 1) ? oth : st1[0], lj = (lane & 1) ? st1[0] : oth;
          float M = fmaxf(mj, dpp<ROR2>(mj)); M = fmaxf(M, dpp<ROR4>(M)); M = fmaxf(M, dpp<ROR8>(M));
          float Ls = lj * __builtin_amdgcn_exp2f((mj - M) * L2E);
          Ls += dpp<ROR2>(Ls); Ls += dpp<ROR4>(Ls); Ls += dpp<ROR8>(Ls);
          const float inv = __builtin_amdgcn_rcpf(__uint_as_float(__builtin_amdgcn_readfirstlane(__float_as_uint(Ls))));
          unsigned p[8];
#pragma unroll
          for (int i = 0; i < 8; ++i) { const unsigned t = bfbits(__builtin_amdgcn_exp2f((s[i] - M) * L2E) * inv); p[i] = t | (t << 16); }
          *(LAS v4u*)(lds + L_PP + b * 2048 + lane * 32) = (v4u){p[0], p[1], p[2], p[3]};
          *(LAS v4u*)(lds + L_PP + b * 2048 + lane * 32 + 16) = (v4u){p[4], p[5], p[6], p[7]};
          post(lds, 7, b + 1, lane);
          STAMP(36 + b);
        }
      }
      if (st >= 2) {
        if (st == 2) waitn<4, 4>(lds, 0, 24); else { wait1<2>(lds, 0, 31); waitn<0, 4>(lds, 0, 32); }
        float s = 0.f;
#pragma unroll
        for (int r = 0; r < 8; ++r) s += *(const LAS float*)(lds + L_PART + (((st - 2) * 8 + r) * DH + ot) * 4);
        put(xbox + (st * 8 + mem) * DH + ot, tag, s);
        if (wave == 0) STAMP(36 + st);
        if (st == 2) wait1<4>(lds, 0, T_O);
      }
      float v[8];
      sweep<8, 1>(xbox + st * 8 * DH + ot, DH, tag, v, st < 3);
      *(LAS u16*)(lds + L_QO + (st * DH + (st < 2 ? qd : ot)) * 2) = (u16)bfbits(((v[0] + v[1]) + (v[2] + v[3])) + ((v[4] + v[5]) + (v[6] + v[7])));
      post(lds, 4 + wave, st + 1, lane);
      if (wave == 0) STAMP(40 + st);
    }
  } else {
    const unsigned lds0 = (unsigned)(size_t)lds;
    if (w >= 64) {
      const int fb = (w & 7) * 3 + (w >> 6) - 1;
      const size_t goff = (size_t)mem * (80 * F * 2) + fb * 128 + ((lane >> 3) * (F * 2) + (lane & 7) * 16);
      v4u g[10], u[10];
#pragma unroll
      for (int i = 0; i < 10; ++i) { g[i] = ld_nt((gptr)wg_ + goff + (size_t)i * (8 * F * 2)); u[i] = ld_nt((gptr)wu_ + goff + (size_t)i * (8 * F * 2)); }
      SB();
      f4 g0 = {0.f, 0.f, 0.f, 0.f}, g1 = g0, u0 = g0, u1 = g0;
      const LAS char* xp = lds + L_XX + (mem * 80 + (lane >> 3)) * 8;
#pragma unroll
      for (int i = 0; i < 10; ++i) {
        const v2u xv = *(const LAS v2u*)(xp + i * 64);
        const v4u a0 = msk & xv.x, a1 = msk & xv.y;
        g0 = mfma(a0, g[i], g0); g1 = mfma(a1, g[i], g1); u0 = mfma(a0, u[i], u0); u1 = mfma(a1, u[i], u1);
      }
      const f4 gs = fold(g0, g1), us = fold(u0, u1);
      gu64* fbox = (gu64*)(ws + WS_F) + fb * (64 * 32);
      if ((lane & 8) == 0) {
        gu64* dst = fbox + (((lane >> 4) & 1) * 32 + mem) * 32 + (lane >> 5) * 8 + (lane & 7);
#pragma unroll
        for (int r = 0; r < 4; ++r) { put(dst + r * 256, tag, gs[r]); put(dst + r * 256 + 16, tag, us[r]); }
      }
      stage_rows((gptr)wd_ + (size_t)fb * (64 * D * 2) + mem * 160, lds0 + L_WD, 10, lane);
      STAMP(44);
      float v[4];
      wait1<8>(lds, 0, T_F);
      sweep<4, 16>(fbox + mem * 256 + lane, 64, tag, v);
      const float sv = (v[0] + v[1]) + (v[2] + v[3]);
      auto t = __builtin_amdgcn_permlane32_swap(__float_as_uint(sv), __float_as_uint(sv), false, false);
      const unsigned tot = __float_as_uint(__uint_as_float(t[0]) + __uint_as_float(t[1]));
      auto gu = __builtin_amdgcn_permlane16_swap(tot, tot, false, false);
      const float gt = rbf(__uint_as_float(gu[0])), up = rbf(__uint_as_float(gu[1]));
      const float t2 = 2.f * 0.7978845608028654f * (gt + 0.044715f * gt * gt * gt);
      gu64* hbox = (gu64*)(ws + WS_H) + fb * 128;
      if (lane < 16) put(hbox + (lane >> 3) * 64 + mem * 8 + (lane & 7), tag, rbf(gt / (1.f + __expf(-t2))) * up);
      float hv[2];
      wait1<8>(lds, 0, T_H);
      sweep<2, 16>(hbox + lane, 64, tag, hv);
      *(LAS u16*)(lds + L_HB + (lane & 7) * 16 + (lane >> 3) * 2) = (u16)bfbits(hv[0]);
      *(LAS u16*)(lds + L_HB + 128 + (lane & 7) * 16 + (lane >> 3) * 2) = (u16)bfbits(hv[1]);
      asm volatile("s_waitcnt vmcnt(0) lgkmcnt(0)" ::: "memory");
      const LAS char* ap = lds + L_HB + ((lane >> 2) & 1) * 128 + (lane >> 4) * 16;
      const v4u a0 = *(const LAS v4u*)ap, a1 = *(const LAS v4u*)(ap + 64);
      const LAS char* bp = lds + L_WD + ((lane >> 4) * 8 + ((lane >> 2) & 3)) * 160 + (lane & 3) * 8;
      gu64* gdst = (gu64*)(ws + WS_G) + ((mem * 10 + ((lane >> 4) & 1)) * 24 + fb) * 16 + (lane & 15);
#pragma unroll 1
      for (int tt = 0; tt < 5; ++tt) {
        f4 y = mfma(a0, tr_frag(bp + tt * 32), (f4){0.f, 0.f, 0.f, 0.f});
        y = mfma(a1, tr_frag(bp + tt * 32 + 5120), y);
        if (lane < 32) put(gdst + tt * (2 * 24 * 16), tag, y.x);
      }
      STAMP(45);
    }
    float gate = 0.f, xres = 0.f;
    if (w < 80) {
      xres = __uint_as_float((unsigned)((const GAS u16*)x_)[(w & 1) * D + (w >> 1) * 16 + (lane & 15)] << 16);
      wait1<8>(lds, 0, T_G);
      float v[6];
      sweep<6, 16>((gu64*)(ws + WS_G) + w * (24 * 16) + lane, 64, tag, v);
      const float gp = rbf(xrow_sum(((v[0] + v[1]) + (v[2] + v[3])) + (v[4] + v[5])));
      gate = rbf(gp / (1.f + __expf(-gp)));
      STAMP(46);
    }
    wait1<2>(lds, 0, T_WO);
    STAMP(47);
    stage_rows((gptr)wo_ + (size_t)h * (DH * D * 2) + mem * 160, lds0 + L_WO, 30, lane);
    asm volatile("s_waitcnt vmcnt(0)" ::: "memory");
    STAMP(48);
    BAR();
    if (w < 80) {
      float v[8];
      sweep<8, 1>((gu64*)(ws + WS_Y) + w * (32 * 16) + lane, 64, tag, v);
      const float s = rbf(xrow_sum(((v[0] + v[1]) + (v[2] + v[3])) + ((v[4] + v[5]) + (v[6] + v[7]))));
      if (lane < 16) ((GAS u16*)out_)[(w & 1) * D + (w >> 1) * 16 + lane] = (u16)bfbits(xres + rbf(s * gate));
      if (w == 0 && lane == 0) __hip_atomic_store((GAS unsigned*)(ws + WS_EPOCH), tag, RLX_AGENT);
      STAMPG(56);
    }
    return;
  }
  const int tt = wave >= 4 ? ww : 4;
  const LAS char* ap = lds + L_QO + 768 + ((lane >> 2) & 1) * 384 + (lane >> 4) * 16;
  const LAS char* bp = lds + L_WO + ((lane >> 4) * 8 + ((lane >> 2) & 3)) * 160 + tt * 32 + (lane & 3) * 8;
  gu64* ydst = (gu64*)(ws + WS_Y) + (((mem * 5 + tt) * 2 + ((lane >> 4) & 1)) * 32 + h) * 16 + (lane & 15);
  asm volatile("" : "+v"(ap), "+v"(bp), "+v"(ydst));
  BAR();
  if (wave >= 2) {
    v4u a[6], bf[6];
#pragma unroll
    for (int ks = 0; ks < 6; ++ks) { a[ks] = *(const LAS v4u*)(ap + ks * 64); bf[ks] = tr_frag(bp + ks * 5120); }
    SB();
    f4 y = {0.f, 0.f, 0.f, 0.f};
#pragma unroll
    for (int ks = 0; ks < 6; ++ks) y = mfma(a[ks], bf[ks], y);
    if (lane < 32) put(ydst, tag, y.x);
    if (wave == 2) STAMPG(55);
  }
  asm volatile(".Lfd_last:");
}

__global__ void __launch_bounds__(64) ktiny(unsigned* st) { if (threadIdx.x == 0 && blockIdx.x == 0) st[0] = (unsigned)__builtin_amdgcn_s_memrealtime(); }
__global__ void __launch_bounds__(256) kspin(const unsigned* st, const unsigned* tiny) {
  const unsigned now = (unsigned)__builtin_amdgcn_s_memrealtime();
  __shared__ unsigned v[256], s0[256];
  const int t = threadIdx.x;
  s0[t] = st[t * 64]; v[t] = st[(t % 80) * 64 + 56];
  __syncthreads();
  if (t == 0) {
    unsigned t0 = s0[0], t1 = v[0];
    for (int i = 1; i < 256; ++i) { if ((int)(s0[i] - t0) < 0) t0 = s0[i]; if ((int)(v[i] - t1) > 0) t1 = v[i]; }
    unsigned m = METRIC == 1 ? t0 - tiny[0] : METRIC == 2 ? now - t1 : (t0 == 12345u && t1 == 5u && tiny[0] == 7u);
    if (m > 8000u) m = 8000u;
    const unsigned long long a = __builtin_amdgcn_s_memrealtime();
    while (__builtin_amdgcn_s_memrealtime() - a < (unsigned long long)m * 20u) __builtin_amdgcn_s_sleep(1);
  }
}
extern "C" void kernel_launch(void* const* d_in, const int* in_sizes, int n_in, void* d_out, int out_size, void* d_ws, size_t ws_size,
                              hipStream_t stream) {
  fused_decode<<<256, 512, 0, stream>>>((const u16*)d_in[0], (const u16*)d_in[1], (const u16*)d_in[2], (const u16*)d_in[3],
                                        (const u16*)d_in[4], (const u16*)d_in[6], (const u16*)d_in[7], (const u16*)d_in[8],
                                        (u16*)d_out, (char*)d_ws);
}
```

```cpp
#define TAU16 684
#define ORG 200
#include <hip/hip_runtime.h>
#include <stdint.h>

typedef unsigned short u16;
typedef unsigned long long u64;
typedef unsigned v4u __attribute__((ext_vector_type(4)));
typedef unsigned v2u __attribute__((ext_vector_type(2)));
typedef float f4 __attribute__((ext_vector_type(4)));
typedef short s4 __attribute__((ext_vector_type(4)));
typedef __bf16 bf2 __attribute__((ext_vector_type(2)));
typedef __bf16 bf8 __attribute__((ext_vector_type(8)));
#define GAS __attribute__((address_space(1)))
#define LAS __attribute__((address_space(3)))
#define RLX_AGENT __ATOMIC_RELAXED, __HIP_MEMORY_SCOPE_AGENT
typedef const GAS char* gptr;
typedef GAS u64 gu64;

#define ROT_MODE 3
#define NP 8
#define T_Q 2
#define T_ST 4
#define T_O 29
#define T_WO 32
#define T_F 7
#define T_H 11
#define T_G 18
#define WIN 8
#define PBASE 2
#define WO_W 3
#define WPOL ""
#define OSLEEP 20
#define YSLEEP 20
#define WO_EARLY 0
#define XOFF 1
#define LAZY 24
#define QDELAY 64
#define CLEAR 0

constexpr int D = 640, H = 32, SK = 4096, DH = 192, F = 1536, NTHREADS = 512;
constexpr int STEP = 32 * 3072;
constexpr unsigned SPIN_CAP = 1u << 16;

constexpr int WS_EPOCH = 0;
constexpr int WS_ST = 4096;
constexpr int WS_H = WS_ST + H * 2 * 16 * 8;
constexpr int WS_F = WS_H + 24 * 128 * 8;
constexpr int WS_X = WS_F + 24 * 64 * 32 * 8;
constexpr int WS_Y = WS_X + H * 4 * 8 * DH * 8;
constexpr int WS_G = WS_Y + 80 * 32 * 16 * 8;
constexpr int L_WO = 0;
constexpr int L_WD = 30720;
constexpr int L_XX = L_WD + 10240;
constexpr int L_PART = L_XX + 5120;
constexpr int L_QO = L_PART + 12288;
constexpr int L_HB = L_QO + 1536;
constexpr int L_SC = L_HB + 256;
constexpr int L_PP = L_SC + 4096;
constexpr int L_FL = L_PP + 4096;
constexpr int L_DG = L_FL + 64;
constexpr int L_TOTAL = 84 * 1024;

__device__ __forceinline__ float rbf(float f) { return (float)(__bf16)f; }
__device__ __forceinline__ unsigned bfbits(float f) { return (unsigned)__builtin_bit_cast(u16, (__bf16)f); }
__device__ __forceinline__ float dot2(unsigned a, unsigned b, float acc) {
  return __builtin_amdgcn_fdot2_f32_bf16(__builtin_bit_cast(bf2, a), __builtin_bit_cast(bf2, b), acc, false);
}
__device__ __forceinline__ float dot8(v4u a, v4u b, float acc) {
  return dot2(a.w, b.w, dot2(a.z, b.z, dot2(a.y, b.y, dot2(a.x, b.x, acc))));
}
__device__ __forceinline__ f4 mfma(v4u a, v4u b, f4 c) {
  return __builtin_amdgcn_mfma_f32_16x16x32_bf16(__builtin_bit_cast(bf8, a), __builtin_bit_cast(bf8, b), c, 0, 0, 0);
}
template <int CTRL> __device__ __forceinline__ float dpp(float x) {
  return __builtin_bit_cast(float, __builtin_amdgcn_mov_dpp(__builtin_bit_cast(int, x), CTRL, 0xf, 0xf, true));
}
constexpr int XOR1 = 0xB1, XOR2 = 0x4E, HMIR = 0x141, ROR2 = 0x122, ROR4 = 0x124, ROR8 = 0x128;
__device__ __forceinline__ float xrow_sum(float x) {
  auto s = __builtin_amdgcn_permlane16_swap(__float_as_uint(x), __float_as_uint(x), false, false);
  x = __uint_as_float(s[0]) + __uint_as_float(s[1]);
  auto t = __builtin_amdgcn_permlane32_swap(__float_as_uint(x), __float_as_uint(x), false, false);
  return __uint_as_float(t[0]) + __uint_as_float(t[1]);
}
__device__ __forceinline__ float xrow_max(float x) {
  auto s = __builtin_amdgcn_permlane16_swap(__float_as_uint(x), __float_as_uint(x), false, false);
  x = fmaxf(__uint_as_float(s[0]), __uint_as_float(s[1]));
  auto t = __builtin_amdgcn_permlane32_swap(__float_as_uint(x), __float_as_uint(x), false, false);
  return fmaxf(__uint_as_float(t[0]), __uint_as_float(t[1]));
}
__device__ __forceinline__ float wave_sum(float x) {
  x += dpp<XOR1>(x); x += dpp<XOR2>(x); x += dpp<HMIR>(x); x += dpp<ROR8>(x);
  return xrow_sum(x);
}
__device__ __forceinline__ float wave_max(float x) {
  x = fmaxf(x, dpp<XOR1>(x)); x = fmaxf(x, dpp<XOR2>(x)); x = fmaxf(x, dpp<HMIR>(x)); x = fmaxf(x, dpp<ROR8>(x));
  return xrow_max(x);
}
__device__ __forceinline__ v4u ld_nt(gptr p) { return __builtin_nontemporal_load((const GAS v4u*)p); }
__device__ __forceinline__ v4u ld_pl(gptr p) { return *(const GAS v4u*)p; }
#define SB() __builtin_amdgcn_sched_barrier(0)
#define BAR() asm volatile("s_waitcnt lgkmcnt(0)\n\ts_barrier" ::: "memory")
__device__ __forceinline__ void put(gu64* g, unsigned tag, float v) {
  __hip_atomic_store(g, ((u64)tag << 32) | __float_as_uint(v), RLX_AGENT);
}
template <int N, int SLEEP> __device__ __forceinline__ void sweep(gu64* g, int stride, unsigned tag, float (&v)[N], bool lazy = false) {
#pragma unroll 1
  for (unsigned spins = 0; spins < SPIN_CAP; ++spins) {
    bool ok = true;
#pragma unroll
    for (int k = 0; k < N; ++k) {
      const u64 x = __hip_atomic_load(g + k * stride, RLX_AGENT);
      v[k] = __uint_as_float((unsigned)x);
      ok &= (unsigned)(x >> 32) == tag;
    }
    if (__all(ok)) return;
    if (SLEEP) __builtin_amdgcn_s_sleep(SLEEP);
    if (lazy) __builtin_amdgcn_s_sleep(LAZY);
  }
#pragma unroll
  for (int k = 0; k < N; ++k) v[k] = __builtin_nanf("");
}
__device__ __forceinline__ void post(LAS char* lds, int word, unsigned v, int lane) {
  asm volatile("" ::: "memory");
  if (lane == 0) *(volatile LAS unsigned*)(lds + L_FL + word * 4) = v;
  asm volatile("" ::: "memory");
}
template <int SLEEP> __device__ __forceinline__ void wait1(LAS char* lds, int word, unsigned want) {
  while (__builtin_amdgcn_readfirstlane(*(volatile LAS unsigned*)(lds + L_FL + word * 4)) < want) { if (SLEEP) __builtin_amdgcn_s_sleep(SLEEP); }
  asm volatile("" ::: "memory");
}
template <int SLEEP, int N> __device__ __forceinline__ void waitn(LAS char* lds, int word, unsigned want) {
  for (;;) {
    const v4u v = *(volatile LAS v4u*)(lds + L_FL + word * 4);
    unsigned m = min(min(v.x, v.y), v.z);
    if (N == 4) m = min(m, v.w);
    if (__builtin_amdgcn_readfirstlane(m) >= want) break;
    if (SLEEP) __builtin_amdgcn_s_sleep(SLEEP);
  }
  asm volatile("" ::: "memory");
}
struct RowMap { unsigned o[5]; };
__device__ __forceinline__ RowMap row_map(int lane) {
  RowMap m;
#pragma unroll
  for (int i = 0; i < 5; ++i) { const unsigned ci = i * 64 + lane, row = (ci * 52429u) >> 19; m.o[i] = row * (D * 2 - 160) + ci * 16; }
  asm volatile("" : "+v"(m.o[0]), "+v"(m.o[1]), "+v"(m.o[2]), "+v"(m.o[3]), "+v"(m.o[4]));
  return m;
}
__device__ __forceinline__ void stage_rows(gptr src, unsigned lds_dst, int ng, const RowMap& m) {
#pragma unroll 1
  for (int g = 0; g < ng; ++g) {
    unsigned keep;
    asm volatile("s_mov_b32 %0, m0\n\ts_mov_b32 m0, %7\n\ts_nop 0\n\tglobal_load_lds_dwordx4 %1, %6" WPOL "\n\t"
                 "s_add_u32 m0, m0, 0x400\n\ts_nop 0\n\tglobal_load_lds_dwordx4 %2, %6" WPOL "\n\t"
                 "s_add_u32 m0, m0, 0x400\n\ts_nop 0\n\tglobal_load_lds_dwordx4 %3, %6" WPOL "\n\t"
                 "s_add_u32 m0, m0, 0x400\n\ts_nop 0\n\tglobal_load_lds_dwordx4 %4, %6" WPOL "\n\t"
                 "s_add_u32 m0, m0, 0x400\n\ts_nop 0\n\tglobal_load_lds_dwordx4 %5, %6" WPOL "\n\ts_mov_b32 m0, %0"
                 : "=&s"(keep) : "v"(m.o[0]), "v"(m.o[1]), "v"(m.o[2]), "v"(m.o[3]), "v"(m.o[4]), "s"(src), "s"(lds_dst) : "scc", "memory");
    src += 32 * D * 2; lds_dst += 5120;
  }
}
__device__ __forceinline__ v4u tr_frag(const LAS char* p) {
  const v2u b0 = __builtin_bit_cast(v2u, __builtin_amdgcn_ds_read_tr16_b64_v4i16((LAS s4*)p));
  const v2u b1 = __builtin_bit_cast(v2u, __builtin_amdgcn_ds_read_tr16_b64_v4i16((LAS s4*)(p + 640)));
  return (v4u){b0.x, b0.y, b1.x, b1.y};
}
__device__ __forceinline__ f4 fold(f4 a0, f4 a1) {
  f4 o;
#pragma unroll
  for (int r = 0; r < 4; ++r) {
    auto t = __builtin_amdgcn_permlane32_swap(__float_as_uint(a0[r]), __float_as_uint(a1[r]), false, false);
    o[r] = __uint_as_float(t[0]) + dpp<ROR8>(__uint_as_float(t[1]));
  }
  return o;
}

#define STAMP(i)
#define ULOG(i)
#define STAMPG(i)

extern "C" __global__ void __launch_bounds__(512, 2)
fused_decode(const u16* x_, const u16* kc_, const u16* vc_, const u16* wq_, const u16* wo_, const u16* wg_, const u16* wu_,
             const u16* wd_, u16* out_, char* ws_) {
  __shared__ __attribute__((aligned(16))) char lds_raw[L_TOTAL];
  LAS char* lds = (LAS char*)lds_raw;
  GAS char* ws = (GAS char*)ws_;
  const int tid = threadIdx.x, lane = tid & 63, wave = __builtin_amdgcn_readfirstlane(tid >> 6);
  const int w = blockIdx.x, h = (w & 7) * 4 + (w >> 6), mem = (w >> 3) & 7;
  const int ww = wave - 4;
  const int hc = (h + 4 * XOFF) & 31;
  const unsigned t0 = (unsigned)__builtin_amdgcn_s_memrealtime();
  u64 code0; unsigned codeoff;
  {
    u64 pc; unsigned back, len;
    asm volatile("s_getpc_b64 %0\n.Lfd_here:\n\ts_mov_b32 %1, .Lfd_here-fused_decode\n\ts_mov_b32 %2, .Lfd_last-fused_decode" : "=s"(pc), "=s"(back), "=s"(len));
    const unsigned c0 = ((w >> 3) * 4 + (lane & 3)) * 128u;
    code0 = pc - back; codeoff = c0 < len ? c0 : 0u;
  }
  auto warm_code = [&]() {
    unsigned keep;
    asm volatile("s_mov_b32 %0, m0\n\ts_mov_b32 m0, %3\n\ts_nop 0\n\tglobal_load_lds_dword %1, %2\n\ts_mov_b32 m0, %0"
                 : "=&s"(keep) : "v"(codeoff), "s"(code0), "s"((unsigned)(size_t)lds + L_FL + 48) : "memory");
  };
  if (tid < 4) warm_code();
  SB();
  STAMP(wave);
  unsigned tag;
  v4u msk;
  auto meet = [&]() {
    tag = *(const __attribute__((address_space(4))) unsigned*)(ws_ + WS_EPOCH) + 1u;
    const unsigned mm = (lane & 1) ? 0xffff0000u : 0x0000ffffu;
    const int q = (lane & 7) >> 1;
    msk = (v4u){q == 0 ? mm : 0u, q == 1 ? mm : 0u, q == 2 ? mm : 0u, q == 3 ? mm : 0u};
    asm volatile("" : "+s"(tag), "+v"(msk));
    BAR();
  };

  if (wave >= 4) {
    if (tid < 268) *(volatile LAS unsigned*)(lds + L_FL + lane * 4) = 0u;
    meet();
    const int rot = ROT_MODE == 0 ? 0 : ROT_MODE == 2 ? (h & 3) * 4 : (h & 3) * 4 + ((h >> 2) & 3);
    const unsigned loff = (mem * 4 + ww) * 3072 + (lane >> 3) * 384 + (lane & 7) * 16;
    constexpr size_t HSTR = (size_t)SK * DH * 2, BSTR = H * HSTR;
    char* const kb0 = (char*)kc_ + h * HSTR;
    char* const vb0 = (char*)vc_ + h * HSTR;
    unsigned due16 = (t0 + ORG) * 16u + ww * (TAU16 / 4);
    auto pace = [&]() {
      asm volatile("s_branch 2f\n1:\n\ts_sleep 1\n2:\n\ts_memrealtime vcc\n\ts_waitcnt lgkmcnt(0)\n\ts_lshl_b32 vcc_lo, vcc_lo, 4\n\t"
                   "s_sub_i32 vcc_lo, vcc_lo, %0\n\ts_cmp_lt_i32 vcc_lo, 0\n\ts_cbranch_scc1 1b" :: "s"(due16) : "vcc", "scc", "memory");
      due16 += TAU16;
      SB();
    };
    auto fetch = [&](char* base, int s, int j) {
      asm volatile("s_waitcnt vmcnt(%0)" :: "n"(WIN - 1) : "memory");
      const v4u r = __builtin_amdgcn_raw_buffer_load_b128(__builtin_amdgcn_make_buffer_rsrc(base, 0, (int)HSTR, 0x00020000),
                                                          loff + j * 128, ((s + rot) & 15) * STEP, 2  );
      SB();
      return r;
    };
    v4u U[NP + 4][3];
#pragma unroll
    for (int u = 0; u < NP + 4; ++u) {
      pace();
#pragma unroll
      for (int j = 0; j < 3; ++j) U[u][j] = fetch(kb0, u, j);
      if (u & 1) post(lds, ww, u >> 1, lane);
      if (u >= 4) ULOG(u - 4);
    }
#pragma unroll 1
    for (int b = 0; b < 2; ++b) {
      waitn<0, 3>(lds, 4, b + 1);
      if (ww == 0) STAMP(12 + b);
      v4u q[3];
#pragma unroll
      for (int j = 0; j < 3; ++j) q[j] = *(const LAS v4u*)(lds + L_QO + b * 384 + j * 128 + (lane & 7) * 16);
      char* const cur = kb0 + b * BSTR;
      char* const nxt = b ? vb0 : kb0 + BSTR;
      LAS float* sc = (LAS float*)(lds + L_SC) + b * 512 + ww * 8 + (lane >> 3);
#pragma unroll 1
      for (int it = b ? 0 : NP / 4; it < 4; ++it) {
        char* const base = it == 3 ? nxt : cur;
        float pick = 0.f;
#pragma unroll
        for (int k = 0; k < 4; ++k) {
          if (!(k & 1)) post(lds, ww, PBASE + b * 8 + it * 2 + (k >> 1), lane);
          pace();
          float d = 0.f;
#pragma unroll
          for (int j = 0; j < 3; ++j) { d = dot8(U[NP + k][j], q[j], d); asm volatile("" : "+v"(d)); SB(); U[NP + k][j] = fetch(base, 4 * it + 4 + k, j); }
          d += dpp<XOR1>(d); d += dpp<XOR2>(d); d += dpp<HMIR>(d);
          pick = (lane & 3) == k ? d : pick;
          ULOG(b * 16 + it * 4 + k);
        }
        sc[it * 128 + (lane & 3) * 32] = rbf(rbf(pick) * 0.07216878364870322f);
        SB();
      }
      if (b == 0) {
        static_assert(NP == 8 || NP == 4, "");
        float d[NP], pick = 0.f;
#pragma unroll
        for (int u = 0; u < NP; ++u) d[u] = dot8(U[u][2], q[2], dot8(U[u][1], q[1], dot8(U[u][0], q[0], 0.f)));
#pragma unroll
        for (int u = 0; u < NP; ++u) { d[u] += dpp<XOR1>(d[u]); d[u] += dpp<XOR2>(d[u]); d[u] += dpp<HMIR>(d[u]); pick = (lane & (NP - 1)) == u ? d[u] : pick; }
        sc[(lane & (NP - 1)) * 32] = rbf(rbf(pick) * 0.07216878364870322f);
        SB();
      }
      STAMP(16 + b * 4 + ww);
    }
    f4 o[3];
    const bool useful = ((lane >> 3) & 1) == (lane >> 5);
    LAS char* part = lds + L_PART + ((ww * 2 + (lane >> 5)) * DH + (lane & 7) * 8 + ((lane >> 4) & 1) * 4) * 4;
    const LAS unsigned* pp = (const LAS unsigned*)(lds + L_PP) + ww * 8 + (lane >> 3);
    auto vstep = [&](int n, char* base, bool more) {
      unsigned pv[4];
#pragma unroll
      for (int k = 0; k < 4; ++k) pv[k] = pp[n * 128 + k * 32];
#pragma unroll
      for (int k = 0; k < 4; ++k) {
        if (k == 2 && more) post(lds, ww, PBASE + 17 + 2 * n, lane);
        const v4u a = msk & pv[k];
        SB();
        if (more) pace();
#pragma unroll
        for (int j = 0; j < 3; ++j) { o[j] = mfma(a, U[NP + k][j], o[j]); SB(); if (more) U[NP + k][j] = fetch(base, 4 * n + 4 + k, j); }
        ULOG(32 + 4 * n + k);
      }
    };
    auto vflush = [&](int b) {
      if (useful) {
#pragma unroll
        for (int j = 0; j < 3; ++j) *(LAS f4*)(part + b * (8 * DH * 4) + j * 256) = o[j];
      }
    };
#pragma unroll 1
    for (int n = 0; n < 7; ++n) {
      post(lds, ww, PBASE + 16 + 2 * n, lane);
      if ((n & 3) == 0) {
        wait1<0>(lds, 7, 1 + (n >> 2));
#pragma unroll
        for (int j = 0; j < 3; ++j) o[j] = (f4){0.f, 0.f, 0.f, 0.f};
        STAMP(24 + (n >> 2) * 4 + ww);
      }
      vstep(n, n >= 3 ? vb0 + BSTR : vb0, true);
      if (n == 3) vflush(0);
    }
    post(lds, ww, PBASE + 30, lane);
    vstep(7, vb0, false);
    vflush(1);
    post(lds, ww, PBASE + 32, lane);
    STAMP(32 + ww);
  } else if (wave == 3) {
    const unsigned lds0 = (unsigned)(size_t)lds;
    const RowMap rm = row_map(lane);
    const gptr wob = (gptr)wo_ + (size_t)hc * (DH * D * 2) + mem * 160;
    if (w < 64) { meet(); if (WO_EARLY) stage_rows(wob, lds0 + L_WO, WO_EARLY, rm); }
    else {
      const int fb = (w & 7) * 3 + (w >> 6) - 1;
      gptr gp = (gptr)wg_ + ((size_t)mem * (80 * F * 2) + fb * 128), up_ = (gptr)wu_ + ((size_t)mem * (80 * F * 2) + fb * 128);
      const unsigned goff = (lane >> 3) * (F * 2) + (lane & 7) * 16;
      v4u g[10], u[10];
      meet();
      if (WO_EARLY) stage_rows(wob, lds0 + L_WO, WO_EARLY, rm);
      stage_rows((gptr)wd_ + (size_t)fb * (64 * D * 2) + mem * 160, lds0 + L_WD, 2, rm);
#pragma unroll
      for (int i = 0; i < 10; ++i) {
        asm volatile("" : "+s"(gp), "+s"(up_));
        g[i] = ld_nt(gp + goff); u[i] = ld_nt(up_ + goff);
        gp += 8 * F * 2; up_ += 8 * F * 2;
        SB();
      }
      waitn<1, 3>(lds, 8, 1);
      f4 g0 = {0.f, 0.f, 0.f, 0.f}, g1 = g0, u0 = g0, u1 = g0;
      const LAS char* xp = lds + L_XX + (mem * 80 + (lane >> 3)) * 8;
#pragma unroll
      for (int i = 0; i < 10; ++i) {
        const v2u xv = *(const LAS v2u*)(xp + i * 64);
        const v4u a0 = msk & xv.x, a1 = msk & xv.y;
        g0 = mfma(a0, g[i], g0); g1 = mfma(a1, g[i], g1); u0 = mfma(a0, u[i], u0); u1 = mfma(a1, u[i], u1);
      }
      const f4 gs = fold(g0, g1), us = fold(u0, u1);
      gu64* fbox = (gu64*)(ws + WS_F) + fb * (64 * 32);
      if ((lane & 8) == 0) {
        gu64* dst = fbox + (((lane >> 4) & 1) * 32 + mem) * 32 + (lane >> 5) * 8 + (lane & 7);
#pragma unroll
        for (int r = 0; r < 4; ++r) { put(dst + r * 256, tag, gs[r]); put(dst + r * 256 + 16, tag, us[r]); }
      }
      STAMP(44);
      float v[4];
      wait1<8>(lds, 0, T_F);
      sweep<4, 16>(fbox + mem * 256 + lane, 64, tag, v);
      const float sv = (v[0] + v[1]) + (v[2] + v[3]);
      auto t = __builtin_amdgcn_permlane32_swap(__float_as_uint(sv), __float_as_uint(sv), false, false);
      const unsigned tot = __float_as_uint(__uint_as_float(t[0]) + __uint_as_float(t[1]));
      auto gu = __builtin_amdgcn_permlane16_swap(tot, tot, false, false);
      const float gt = rbf(__uint_as_float(gu[0])), up = rbf(__uint_as_float(gu[1]));
      const float t2 = 2.f * 0.7978845608028654f * (gt + 0.044715f * gt * gt * gt);
      gu64* hbox = (gu64*)(ws + WS_H) + fb * 128;
      if (lane < 16) put(hbox + (lane >> 3) * 64 + mem * 8 + (lane & 7), tag, rbf(gt / (1.f + __expf(-t2))) * up);
      float hv[2];
      wait1<8>(lds, 0, T_H);
      sweep<2, 16>(hbox + lane, 64, tag, hv);
      *(LAS u16*)(lds + L_HB + (lane & 7) * 16 + (lane >> 3) * 2) = (u16)bfbits(hv[0]);
      *(LAS u16*)(lds + L_HB + 128 + (lane & 7) * 16 + (lane >> 3) * 2) = (u16)bfbits(hv[1]);
      asm volatile("s_waitcnt vmcnt(0) lgkmcnt(0)" ::: "memory");
      const LAS char* ap = lds + L_HB + ((lane >> 2) & 1) * 128 + (lane >> 4) * 16;
      const v4u a0 = *(const LAS v4u*)ap, a1 = *(const LAS v4u*)(ap + 64);
      const LAS char* bp = lds + L_WD + ((lane >> 4) * 8 + ((lane >> 2) & 3)) * 160 + (lane & 3) * 8;
      gu64* gdst = (gu64*)(ws + WS_G) + ((mem * 10 + ((lane >> 4) & 1)) * 24 + fb) * 16 + (lane & 15);
#pragma unroll 1
      for (int tt = 0; tt < 5; ++tt) {
        f4 y = mfma(a0, tr_frag(bp + tt * 32), (f4){0.f, 0.f, 0.f, 0.f});
        y = mfma(a1, tr_frag(bp + tt * 32 + 5120), y);
        if (lane < 32) put(gdst + tt * (2 * 24 * 16), tag, y.x);
      }
      STAMP(45);
    }
    float gate = 0.f, xres = 0.f;
    if (w < 80) {
      xres = __uint_as_float((unsigned)((const GAS u16*)x_)[(w & 1) * D + (w >> 1) * 16 + (lane & 15)] << 16);
      wait1<8>(lds, 0, T_G);
      float v[6];
      sweep<6, 16>((gu64*)(ws + WS_G) + w * (24 * 16) + lane, 64, tag, v);
      const float gp = rbf(xrow_sum(((v[0] + v[1]) + (v[2] + v[3])) + (v[4] + v[5])));
      gate = rbf(gp / (1.f + __expf(-gp)));
      STAMP(46);
    }
    wait1<2>(lds, WO_W, T_WO);
    STAMP(47);
    if (WO_EARLY < 6) stage_rows(wob + WO_EARLY * (32 * D * 2), lds0 + L_WO + WO_EARLY * 5120, 6 - WO_EARLY, rm);
    asm volatile("s_waitcnt vmcnt(0)" ::: "memory");
    STAMP(48);
    BAR();
    if (w < 80) {
      float v[8];
      if (YSLEEP) __builtin_amdgcn_s_sleep(YSLEEP);
      sweep<8, 1>((gu64*)(ws + WS_Y) + w * (32 * 16) + lane, 64, tag, v);
      const float s = rbf(xrow_sum(((v[0] + v[1]) + (v[2] + v[3])) + ((v[4] + v[5]) + (v[6] + v[7]))));
      if (lane < 16) ((GAS u16*)out_)[(w & 1) * D + (w >> 1) * 16 + lane] = (u16)bfbits(xres + rbf(s * gate));
      if (w == 0 && lane == 0) __hip_atomic_store((GAS unsigned*)(ws + WS_EPOCH), tag, RLX_AGENT);
      STAMPG(56);
    }
    __builtin_amdgcn_s_waitcnt(0x0F70);
    return;
  } else {
    gu64* xbox = (gu64*)(ws + WS_X) + h * (4 * 8 * DH);
    gu64* stbox = (gu64*)(ws + WS_ST) + h * 32;
    {
      const GAS unsigned* xg = (const GAS unsigned*)x_ + (wave & 1) * (D / 2) + lane;
      unsigned xr[5];
#pragma unroll
      for (int i = 0; i < 5; ++i) xr[i] = xg[64 * i];
      SB();
      v4u R[10];
      gptr qp = (gptr)wq_ + ((size_t)mem * (80 * H * DH * 2) + h * (DH * 2) + wave * 128);
      const unsigned qoff = (lane >> 3) * (H * DH * 2) + (lane & 7) * 16;
#pragma unroll
      for (int i = 0; i < 10; ++i) { asm volatile("" : "+s"(qp)); R[i] = ld_nt(qp + qoff); qp += 8 * H * DH * 2; SB(); }
      meet();
      float v[10], sum = 0.f;
#pragma unroll
      for (int i = 0; i < 5; ++i) { v[2 * i] = __uint_as_float(xr[i] << 16); v[2 * i + 1] = __uint_as_float(xr[i] & 0xffff0000u); sum += v[2 * i] + v[2 * i + 1]; }
      const float mean = wave_sum(sum) * (1.f / D);
      float q = 0.f;
#pragma unroll
      for (int i = 0; i < 10; ++i) { const float dd = v[i] - mean; q += dd * dd; }
      const float var = rbf(wave_sum(q) * (1.f / D)), mb = rbf(mean);
      const float inv = __builtin_amdgcn_rcpf(rbf(__builtin_amdgcn_sqrtf(rbf(var + 1e-5f))));
#pragma unroll
      for (int i = 0; i < 10; ++i) {
        const unsigned t = bfbits(rbf(v[i] - mb) * inv);
        *(LAS unsigned*)(lds + L_XX + ((lane + 64 * (i >> 1)) * 2 + (i & 1)) * 8 + (wave & 1) * 4) = t | (t << 16);
      }
      post(lds, 8 + wave, 1, lane);
      waitn<0, 3>(lds, 8, 1);
      if (wave == 0) STAMP(8);
      f4 a0 = {0.f, 0.f, 0.f, 0.f}, a1 = a0;
      const LAS char* xp = lds + L_XX + (mem * 80 + (lane >> 3)) * 8;
      v2u xv[10];
#pragma unroll
      for (int i = 0; i < 10; ++i) xv[i] = *(const LAS v2u*)(xp + i * 64);
      SB();
#pragma unroll
      for (int i = 0; i < 10; ++i) { a0 = mfma(msk & xv[i].x, R[i], a0); a1 = mfma(msk & xv[i].y, R[i], a1); }
      const f4 s = fold(a0, a1);
      gu64* dst = xbox + ((lane >> 5) * 8 + mem) * DH + wave * 64 + ((lane >> 4) & 1) * 8 + (lane & 7);
      if ((lane & 8) == 0) {
#pragma unroll
        for (int r = 0; r < 4; ++r) put(dst + r * 16, tag, s[r]);
      }
      if (wave == 0) STAMP(9);
      wait1<4>(lds, 0, T_Q);
    }
    const int ot = wave * 64 + lane, qd = wave * 64 + (lane & 7) * 8 + ((lane >> 3) & 1) * 4 + (lane >> 4);
#pragma unroll 1
    for (int st = 0; st < 4; ++st) {
      if (st == 2 && wave == 0) {
#pragma unroll 1
        for (int b = 0; b < 2; ++b) {
          waitn<4, 4>(lds, 0, PBASE + b * 8 + 8);
          const f4 s0 = *(const LAS f4*)(lds + L_SC + b * 2048 + lane * 32), s1 = *(const LAS f4*)(lds + L_SC + b * 2048 + lane * 32 + 16);
          const float s[8] = {s0.x, s0.y, s0.z, s0.w, s1.x, s1.y, s1.z, s1.w};
          constexpr float L2E = 1.4426950408889634f;
          float m = s[0];
#pragma unroll
          for (int i = 1; i < 8; ++i) m = fmaxf(m, s[i]);
          m = wave_max(m);
          const float ms = m == -INFINITY ? 0.f : m;
          float l = 0.f;
#pragma unroll
          for (int i = 0; i < 8; ++i) l += __builtin_amdgcn_exp2f((s[i] - ms) * L2E);
          l = wave_sum(l);
          if (lane < 2) put(stbox + b * 16 + mem * 2 + lane, tag, lane ? l : m);
          wait1<4>(lds, 0, PBASE + b * 8 + 8 + T_ST);
          float st1[1];
          sweep<1, 16>(stbox + b * 16 + (lane & 15), 0, tag, st1);
          const float oth = dpp<XOR1>(st1[0]), mj = (lane & 1) ? oth : st1[0], lj = (lane & 1) ? st1[0] : oth;
          float M = fmaxf(mj, dpp<ROR2>(mj)); M = fmaxf(M, dpp<ROR4>(M)); M = fmaxf(M, dpp<ROR8>(M));
          float Ls = lj * __builtin_amdgcn_exp2f((mj - M) * L2E);
          Ls += dpp<ROR2>(Ls); Ls += dpp<ROR4>(Ls); Ls += dpp<ROR8>(Ls);
          const float inv = __builtin_amdgcn_rcpf(__uint_as_float(__builtin_amdgcn_readfirstlane(__float_as_uint(Ls))));
          unsigned p[8];
#pragma unroll
          for (int i = 0; i < 8; ++i) { const unsigned t = bfbits(__builtin_amdgcn_exp2f((s[i] - M) * L2E) * inv); p[i] = t | (t << 16); }
          *(LAS v4u*)(lds + L_PP + b * 2048 + lane * 32) = (v4u){p[0], p[1], p[2], p[3]};
          *(LAS v4u*)(lds + L_PP + b * 2048 + lane * 32 + 16) = (v4u){p[4], p[5], p[6], p[7]};
          post(lds, 7, b + 1, lane);
          STAMP(36 + b);
        }
      }
      if (st >= 2) {
        if (st == 2) waitn<4, 4>(lds, 0, PBASE + 24); else { wait1<2>(lds, 0, PBASE + 30); waitn<0, 4>(lds, 0, PBASE + 32); }
        float s = 0.f;
#pragma unroll
        for (int r = 0; r < 8; ++r) s += *(const LAS float*)(lds + L_PART + (((st - 2) * 8 + r) * DH + ot) * 4);
        put(xbox + (st * 8 + mem) * DH + ot, tag, s);
        if (wave == 0) STAMP(36 + st);
        if (st == 2) { wait1<4>(lds, 0, T_O); if (tid < 4) warm_code(); } else if (OSLEEP) __builtin_amdgcn_s_sleep(OSLEEP);
      }
      float v[8];
      sweep<8, 1>((gu64*)(ws + WS_X) + ((st < 2 ? h : hc) * 4 + st) * (8 * DH) + ot, DH, tag, v, st < 3);
      *(LAS u16*)(lds + L_QO + (st * DH + (st < 2 ? qd : ot)) * 2) = (u16)bfbits(((v[0] + v[1]) + (v[2] + v[3])) + ((v[4] + v[5]) + (v[6] + v[7])));
      post(lds, 4 + wave, st + 1, lane);
      if (wave == 0) STAMP(40 + st);
    }
    __builtin_amdgcn_s_waitcnt(0x0F70);
  }
  const int tt = wave >= 4 ? ww : 4;
  const LAS char* ap = lds + L_QO + 768 + ((lane >> 2) & 1) * 384 + (lane >> 4) * 16;
  const LAS char* bp = lds + L_WO + ((lane >> 4) * 8 + ((lane >> 2) & 3)) * 160 + tt * 32 + (lane & 3) * 8;
  gu64* ydst = (gu64*)(ws + WS_Y) + (((mem * 5 + tt) * 2 + ((lane >> 4) & 1)) * 32 + hc) * 16 + (lane & 15);
  asm volatile("" : "+v"(ap), "+v"(bp), "+v"(ydst));
  BAR();
  if (wave >= 2) {
    v4u a[6], bf[6];
#pragma unroll
    for (int ks = 0; ks < 6; ++ks) { a[ks] = *(const LAS v4u*)(ap + ks * 64); bf[ks] = tr_frag(bp + ks * 5120); }
    SB();
    f4 y = {0.f, 0.f, 0.f, 0.f};
#pragma unroll
    for (int ks = 0; ks < 6; ++ks) y = mfma(a[ks], bf[ks], y);
    if (lane < 32) put(ydst, tag, y.x);
    if (wave == 2) STAMPG(55);
  }
  asm volatile(".Lfd_last:");
}

extern "C" void kernel_launch(void* const* d_in, const int* in_sizes, int n_in, void* d_out, int out_size, void* d_ws, size_t ws_size,
                              hipStream_t stream) {
  fused_decode<<<256, 512, 0, stream>>>((const u16*)d_in[0], (const u16*)d_in[1], (const u16*)d_in[2], (const u16*)d_in[3],
                                        (const u16*)d_in[4], (const u16*)d_in[6], (const u16*)d_in[7], (const u16*)d_in[8],
                                        (u16*)d_out, (char*)d_ws);
}
```

```cpp
#include <hip/hip_runtime.h>
#include <stdint.h>

typedef unsigned short u16;
typedef unsigned long long u64;
typedef unsigned v4u __attribute__((ext_vector_type(4)));
typedef unsigned v2u __attribute__((ext_vector_type(2)));
typedef float f4 __attribute__((ext_vector_type(4)));
typedef short s4 __attribute__((ext_vector_type(4)));
typedef __bf16 bf16x2 __attribute__((ext_vector_type(2)));
typedef __bf16 bf16x8 __attribute__((ext_vector_type(8)));
#define GAS __attribute__((address_space(1)))
#define LAS __attribute__((address_space(3)))
#define RLX_AGENT __ATOMIC_RELAXED, __HIP_MEMORY_SCOPE_AGENT
typedef const GAS char* gptr;
typedef GAS u64 gu64;
typedef __amdgpu_buffer_rsrc_t rsrc_t;

#define NP 8
#define PBASE 2
#define T_Q 2
#define T_ST 4
#define T_H 12
#define TAU 650
#define ORG 2500
#define TEST_PH 0
#define TEST_D 64
#define FIRST 1
#define WBASE 24
#define WSH 7
#define WMAX 10
#define NCP 5
#define CP0 8800
#define CPW0 4400
#define CPW 2400
#define CPD 6400
#define CPL 8800
#define LAM 0
#define WEND 24
#define OWNREF 2
#define KAP 1.f
#define MARG0 100
#define MARG 100
#define MUL 23
#define FAST 14
#define T_O1G 2800
#define T_YG 4900
#define OUTWT 0
#define CPMEM 1
#define WO0 600
#define TAU_WO 270

constexpr int D = 640, H = 32, SK = 4096, DH = 192, F = 1536, NTHREADS = 512;
constexpr int STEP = 32 * 3072;
constexpr unsigned SPIN_CAP = 1u << 16;

constexpr int WS_EPOCH = 0;
constexpr int WS_CP = 4096;
constexpr int WS_ST = WS_CP + 8 * 256 * 8;
constexpr int WS_P = WS_ST + H * 2 * 16 * 8;
constexpr int WS_O = WS_P + H * 16 * 64 * 16;
constexpr int WS_Y = WS_O + H * 8 * DH * 16;
constexpr int WS_END = WS_Y + 80 * 64 * 16 * 16;
constexpr int L_WO = 0;
constexpr int L_WD = 30720;
constexpr int L_XX = L_WD + 10240;
constexpr int L_PART = L_XX + 5120;
constexpr int L_QO = L_PART + 12288;
constexpr int L_HB = L_QO + 1536;
constexpr int L_SC = L_HB + 256;
constexpr int L_PP = L_SC + 4096;
constexpr int L_FL = L_PP + 4096;
constexpr int L_DG = L_FL + 128;
constexpr int L_TOTAL = 84 * 1024;

__device__ __forceinline__ float rbf(float f) { return (float)(__bf16)f; }
__device__ __forceinline__ unsigned bfbits(float f) { return (unsigned)__builtin_bit_cast(u16, (__bf16)f); }
__device__ __forceinline__ float dot2(unsigned a, unsigned b, float acc) {
  return __builtin_amdgcn_fdot2_f32_bf16(__builtin_bit_cast(bf16x2, a), __builtin_bit_cast(bf16x2, b), acc, false);
}
__device__ __forceinline__ float dot8(v4u a, v4u b, float acc) {
  return dot2(a.w, b.w, dot2(a.z, b.z, dot2(a.y, b.y, dot2(a.x, b.x, acc))));
}
__device__ __forceinline__ f4 mfma(v4u a, v4u b, f4 c) {
  return __builtin_amdgcn_mfma_f32_16x16x32_bf16(__builtin_bit_cast(bf16x8, a), __builtin_bit_cast(bf16x8, b), c, 0, 0, 0);
}
template <int CTRL> __device__ __forceinline__ float dpp(float x) {
  return __builtin_bit_cast(float, __builtin_amdgcn_mov_dpp(__builtin_bit_cast(int, x), CTRL, 0xf, 0xf, true));
}
constexpr int XOR1 = 0xB1, XOR2 = 0x4E, HMIR = 0x141, ROR2 = 0x122, ROR4 = 0x124, ROR8 = 0x128;
__device__ __forceinline__ float xrow_sum(float x) {
  auto s = __builtin_amdgcn_permlane16_swap(__float_as_uint(x), __float_as_uint(x), false, false);
  x = __uint_as_float(s[0]) + __uint_as_float(s[1]);
  auto t = __builtin_amdgcn_permlane32_swap(__float_as_uint(x), __float_as_uint(x), false, false);
  return __uint_as_float(t[0]) + __uint_as_float(t[1]);
}
__device__ __forceinline__ float xrow_max(float x) {
  auto s = __builtin_amdgcn_permlane16_swap(__float_as_uint(x), __float_as_uint(x), false, false);
  x = fmaxf(__uint_as_float(s[0]), __uint_as_float(s[1]));
  auto t = __builtin_amdgcn_permlane32_swap(__float_as_uint(x), __float_as_uint(x), false, false);
  return fmaxf(__uint_as_float(t[0]), __uint_as_float(t[1]));
}
__device__ __forceinline__ float wave_sum(float x) {
  x += dpp<XOR1>(x); x += dpp<XOR2>(x); x += dpp<HMIR>(x); x += dpp<ROR8>(x);
  return xrow_sum(x);
}
__device__ __forceinline__ float wave_max(float x) {
  x = fmaxf(x, dpp<XOR1>(x)); x = fmaxf(x, dpp<XOR2>(x)); x = fmaxf(x, dpp<HMIR>(x)); x = fmaxf(x, dpp<ROR8>(x));
  return xrow_max(x);
}
__device__ __forceinline__ float fold(float a, float b) {
  auto t = __builtin_amdgcn_permlane32_swap(__float_as_uint(a), __float_as_uint(b), false, false);
  return __uint_as_float(t[0]) + dpp<ROR8>(__uint_as_float(t[1]));
}
__device__ __forceinline__ rsrc_t mk_rsrc(const void* p, int bytes) { return __builtin_amdgcn_make_buffer_rsrc((void*)p, 0, bytes, 0x00020000); }
__device__ __forceinline__ v4u ld_nt(rsrc_t r, unsigned voff, unsigned soff) { return __builtin_amdgcn_raw_buffer_load_b128(r, voff, soff, 2); }
#define SB() __builtin_amdgcn_sched_barrier(0)
#define BAR() asm volatile("s_waitcnt lgkmcnt(0)\n\ts_barrier" ::: "memory")
__device__ __forceinline__ void put(gu64* g, unsigned tag, unsigned v) { __hip_atomic_store(g, ((u64)tag << 32) | v, RLX_AGENT); }
__device__ __forceinline__ void put2(rsrc_t r, unsigned off, unsigned tag, float a, float b) {
  __builtin_amdgcn_raw_buffer_store_b128((v4u){__float_as_uint(a), tag, __float_as_uint(b), tag}, r, off, 0, 16);
}
template <int N, int SLEEP> __device__ __forceinline__ void sweep(gu64* g, int stride, unsigned tag, float (&v)[N]) {
#pragma unroll 1
  for (unsigned spins = 0; spins < SPIN_CAP; ++spins) {
    bool ok = true;
#pragma unroll
    for (int k = 0; k < N; ++k) {
      const u64 x = __hip_atomic_load(g + k * stride, RLX_AGENT);
      v[k] = __uint_as_float((unsigned)x);
      ok &= (unsigned)(x >> 32) == tag;
    }
    if (__all(ok)) return;
    if (SLEEP) __builtin_amdgcn_s_sleep(SLEEP);
  }
#pragma unroll
  for (int k = 0; k < N; ++k) v[k] = __builtin_nanf("");
}
template <int N> __device__ __forceinline__ void sweep2(rsrc_t r, unsigned off, int stride, unsigned tag, bool lazy, float& a, float& b) {
#pragma unroll 1
  for (unsigned spins = 0; spins < SPIN_CAP; ++spins) {
    asm volatile("" ::: "memory");
    v4u x[N];
#pragma unroll
    for (int k = 0; k < N; ++k) x[k] = __builtin_amdgcn_raw_buffer_load_b128(r, off, k * stride, 16);
    unsigned lo = tag, hi = tag;
#pragma unroll
    for (int k = 0; k < N; ++k) {
      lo = min(min(lo, x[k].y), x[k].w); hi = max(max(hi, x[k].y), x[k].w);
      a = k ? a + __uint_as_float(x[k].x) : __uint_as_float(x[k].x);
      b = k ? b + __uint_as_float(x[k].z) : __uint_as_float(x[k].z);
      asm volatile("" : "+v"(a), "+v"(b), "+v"(lo), "+v"(hi));
    }
    if (__builtin_amdgcn_ballot_w64(lo != hi) == 0) return;
    if (lazy) __builtin_amdgcn_s_sleep(24);
  }
  a = b = __builtin_nanf("");
}
__device__ __forceinline__ void post(LAS char* lds, int word, unsigned v, int lane) {
  asm volatile("" ::: "memory");
  if (lane == 0) *(volatile LAS unsigned*)(lds + L_FL + word * 4) = v;
  asm volatile("" ::: "memory");
}
__device__ __forceinline__ unsigned peek(LAS char* lds, int word) {
  return __builtin_amdgcn_readfirstlane(*(volatile LAS unsigned*)(lds + L_FL + word * 4));
}
template <int SLEEP> __device__ __forceinline__ void wait1(LAS char* lds, int word, unsigned want) {
  while (peek(lds, word) < want) { if (SLEEP) __builtin_amdgcn_s_sleep(SLEEP); }
  asm volatile("" ::: "memory");
}
template <int SLEEP, int N> __device__ __forceinline__ void waitn(LAS char* lds, int word, unsigned want) {
  for (;;) {
    const v4u v = *(volatile LAS v4u*)(lds + L_FL + word * 4);
    unsigned m = min(v.x, v.y);
    if (N >= 3) m = min(m, v.z);
    if (N == 4) m = min(m, v.w);
    if (__builtin_amdgcn_readfirstlane(m) >= want) break;
    if (SLEEP) __builtin_amdgcn_s_sleep(SLEEP);
  }
  asm volatile("" ::: "memory");
}
__device__ __forceinline__ unsigned ref_time(LAS char* lds) {
  const unsigned plan = peek(lds, 11), own = peek(lds, 15);
  return plan + max((int)(own - plan), 0) * OWNREF / 2;
}
__device__ __forceinline__ int until(unsigned due) {
  int late;
  asm volatile("s_branch 2f\n1:\n\ts_sleep 1\n2:\n\ts_memrealtime vcc\n\ts_waitcnt lgkmcnt(0)\n\ts_lshl_b32 %0, vcc_lo, 4\n\t"
               "s_sub_i32 %0, %0, %1\n\ts_cmp_lt_i32 %0, 0\n\ts_cbranch_scc1 1b" : "=&s"(late) : "s"(due) : "vcc", "scc", "memory");
  return late;
}
struct RowMap { unsigned o[5]; };
__device__ __forceinline__ RowMap row_map(int lane) {
  RowMap m;
#pragma unroll
  for (int i = 0; i < 5; ++i) { const unsigned ci = i * 64 + lane, row = (ci * 52429u) >> 20; m.o[i] = row * (D * 2 - 320) + ci * 16; }
  asm volatile("" : "+v"(m.o[0]), "+v"(m.o[1]), "+v"(m.o[2]), "+v"(m.o[3]), "+v"(m.o[4]));
  return m;
}
__device__ __forceinline__ void stage_rows(gptr src, unsigned lds_dst, int ng, const RowMap& m, unsigned due, unsigned tau) {
#pragma unroll 1
  for (int g = 0; g < ng; ++g) {
    unsigned keep;
    until(due); due += tau;
    asm volatile("s_mov_b32 %0, m0\n\ts_mov_b32 m0, %7\n\ts_nop 0\n\tglobal_load_lds_dwordx4 %1, %6\n\t"
                 "s_add_u32 m0, m0, 0x400\n\ts_nop 0\n\tglobal_load_lds_dwordx4 %2, %6\n\t"
                 "s_add_u32 m0, m0, 0x400\n\ts_nop 0\n\tglobal_load_lds_dwordx4 %3, %6\n\t"
                 "s_add_u32 m0, m0, 0x400\n\ts_nop 0\n\tglobal_load_lds_dwordx4 %4, %6\n\t"
                 "s_add_u32 m0, m0, 0x400\n\ts_nop 0\n\tglobal_load_lds_dwordx4 %5, %6\n\ts_mov_b32 m0, %0"
                 : "=&s"(keep) : "v"(m.o[0]), "v"(m.o[1]), "v"(m.o[2]), "v"(m.o[3]), "v"(m.o[4]), "s"(src), "s"(lds_dst) : "scc", "memory");
    src += 16 * D * 2; lds_dst += 5120;
  }
}
__device__ __forceinline__ v4u tr_frag(const LAS char* p) {
  const v2u b0 = __builtin_bit_cast(v2u, __builtin_amdgcn_ds_read_tr16_b64_v4i16((LAS s4*)p));
  const v2u b1 = __builtin_bit_cast(v2u, __builtin_amdgcn_ds_read_tr16_b64_v4i16((LAS s4*)(p + 1280)));
  return (v4u){b0.x, b0.y, b1.x, b1.y};
}

#define STAMP(i)
#define STAMPV(i, v)
#define ULOG(i)
#define STAMPG(i)

extern "C" __global__ void __launch_bounds__(512, 2)
fused_decode(const u16* x_, const u16* kc_, const u16* vc_, const u16* wq_, const u16* wo_, const u16* wg_, const u16* wu_,
             const u16* wd_, u16* out_, char* ws_) {
  unsigned ka0, ka1;
  asm volatile("s_load_dword %0, %2, 0x0\n\ts_load_dword %1, %2, 0x40" : "=&s"(ka0), "=&s"(ka1) : "s"(__builtin_amdgcn_kernarg_segment_ptr()));
  SB();
  __shared__ __attribute__((aligned(16))) char lds_raw[L_TOTAL];
  LAS char* lds = (LAS char*)lds_raw;
  GAS char* ws = (GAS char*)ws_;
  const int tid = threadIdx.x, lane = tid & 63, wave = __builtin_amdgcn_readfirstlane(tid >> 6);
  const int w = blockIdx.x, h = (w & 7) * 4 + (w >> 6), mem = (w >> 3) & 7;
  const int ww = wave - 4;
  const int hc = (h + 4) & 31;
  u64 code0;
  {
    u64 pc; unsigned back;
    asm volatile("s_getpc_b64 %0\n.Lfd_here:\n\ts_mov_b32 %1, .Lfd_here-fused_decode" : "=s"(pc), "=s"(back));
    code0 = pc - back;
  }
  auto warm_code = [&](int who) {
    u64 ex; unsigned keep;
    const unsigned c0 = (unsigned)(tid - who * 64) * 4096u + (w >> 3) * 128u;
    asm volatile("v_cmp_gt_u32 vcc, .Lfd_last-fused_decode, %2\n\ts_and_saveexec_b64 %0, vcc\n\ts_cbranch_execz 1f\n\t"
                 "s_mov_b32 %1, m0\n\ts_mov_b32 m0, %4\n\ts_nop 0\n\tglobal_load_lds_dword %2, %3\n\ts_mov_b32 m0, %1\n"
                 "1:\n\ts_mov_b64 exec, %0"
                 : "=&s"(ex), "=&s"(keep) : "v"(c0), "s"(code0), "s"((unsigned)(size_t)lds + L_FL + 96) : "vcc", "scc", "memory");
  };
  warm_code(0);
  u64 t64 = __builtin_amdgcn_s_memrealtime();
  const unsigned t0 = (unsigned)t64;
  const bool ffn = w >= 64, isq = mem < 6;
  const int fb = (w & 7) * 3 + (w >> 6) - 1;
  const int row0 = (mem & 1) * 320 + (lane >> 3), qm = (mem - 6) >> 31;
  unsigned rs8 = isq ? 8 * H * DH * 2 : 8 * F * 2;
  int jobw = wave + ((w >> 4) == 3 ? 4 : 0);
  unsigned woff = (row0 + wave * 80) * (rs8 >> 3) + (((h * 3 + (mem >> 1)) & qm) | (fb & ~qm)) * 128 + (lane & 7) * 16;
  unsigned xoff = (wave & 1) * (D * 2) + lane * 4;
  asm volatile("" : "+s"(rs8), "+s"(jobw));
  asm volatile("" : "+v"(woff), "+v"(xoff));
  SB();
  const bool job = jobw < 4;
  unsigned xr[5];
  v4u R[10];
  if (job) {
#pragma unroll
    for (int i = 0; i < 5; ++i) xr[i] = *(const GAS unsigned*)((gptr)x_ + xoff + 256 * i);
    SB();
    const rsrc_t wr = mk_rsrc(isq ? wq_ : wg_, 80 * rs8);
    unsigned so = 0;
#pragma unroll
    for (int i = 0; i < 10; ++i) { R[i] = ld_nt(wr, woff, so); so += rs8; SB(); }
  }
  STAMP(wave);
  const rsrc_t wsr = mk_rsrc(ws_, WS_END);
  const unsigned fin0 = t0 * 16u + (ORG + 63 * TAU - 16 * TEST_D * __builtin_popcount(TEST_PH));
  unsigned tag;
  v4u msk;
  const int oi = ((w >> 1) & 1) * D + (w >> 2) * 16 + (w & 1) * 8 + (lane & 7);
  float xres = 0.f;
  auto meet = [&]() {
    tag = *(const __attribute__((address_space(4))) unsigned*)(ws_ + WS_EPOCH) + 1u;
    const unsigned mm = (lane & 1) ? 0xffff0000u : 0x0000ffffu;
    const int q = (lane & 7) >> 1;
    msk = (v4u){q == 0 ? mm : 0u, q == 1 ? mm : 0u, q == 2 ? mm : 0u, q == 3 ? mm : 0u};
    asm volatile("" : "+s"(tag) : "s"(t64), "s"(ka0), "s"(ka1));
    asm volatile("" : "+v"(msk));
    BAR();
  };

  if (wave >= 4) {
    __builtin_amdgcn_s_waitcnt(0x0F70);
    if (tid < 280) *(volatile LAS unsigned*)(lds + L_FL + lane * 4) = (lane & 15) < 11 ? 0u : fin0;
    meet();
    const int rot = (h & 3) * 4;
    const unsigned loff = (mem * 4 + ww) * 3072 + (lane >> 3) * 384 + (lane & 7) * 16;
    constexpr long HSTR = (long)SK * DH * 2, BSTR = FIRST ? -H * HSTR : H * HSTR;
    const char* const kb0 = (const char*)kc_ + (FIRST * H + h) * HSTR;
    const char* const vb0 = (const char*)vc_ + (FIRST * H + h) * HSTR;
    unsigned due = t0 * 16u + ORG + ww * (TAU / 4), fin = fin0;
    int adj = 0, late = 0, win, dith = ww, wb = WBASE - 4;
    auto pace = [&]() {
      late = until(due); due += TAU + adj - (((TEST_PH >> ((dith - ww) >> 4)) & 1) ? TEST_D : 0); fin += adj;
      win = min((wb + (late >> WSH) + (dith & 3)) >> 2, WMAX - 1); ++dith;
      SB();
    };
    auto report = [&](unsigned prog) {
      asm volatile("" ::: "memory");
      if (lane == 0) { *(volatile LAS unsigned*)(lds + L_FL + ww * 4) = prog; *(volatile LAS unsigned*)(lds + L_FL + 48 + ww * 4) = fin + late; }
      asm volatile("" ::: "memory");
    };
    auto steer = [&](int mul) { adj = min(max(((int)(peek(lds, 11) - fin) * mul) >> 8, -FAST), TAU / 2); };
    auto fetch = [&](const char* base, int s, int j) {
      unsigned cnt;
      asm volatile("1:\n\ts_getreg_b32 %0, hwreg(HW_REG_IB_STS, 0, 4)\n\ts_cmp_gt_u32 %0, %1\n\ts_cbranch_scc1 1b" : "=&s"(cnt) : "s"(win) : "scc", "memory");
      const v4u r = ld_nt(mk_rsrc(base, (int)HSTR), loff + j * 128, ((s + rot) & 15) * STEP);
      SB();
      return r;
    };
    v4u U[NP + 4][3];
#pragma unroll
    for (int u = 0; u < NP + 4; ++u) {
      pace();
#pragma unroll
      for (int j = 0; j < 3; ++j) U[u][j] = fetch(kb0, u, j);
      if (u & 1) report(u >> 1);
      if (u >= 4) ULOG(u - 4);
    }
    waitn<0, 3>(lds, 20, 1);
    if (ww == 0) STAMP(12);
#pragma unroll 1
    for (int b = 0; b < 2; ++b) {
      v4u q[3];
#pragma unroll
      for (int j = 0; j < 3; ++j) q[j] = *(const LAS v4u*)(lds + L_QO + (b ^ FIRST) * 384 + j * 128 + (lane & 7) * 16);
      const char* const cur = kb0 + b * BSTR;
      const char* const nxt = b ? vb0 : kb0 + BSTR;
      LAS float* sc = (LAS float*)(lds + L_SC) + b * 512 + ww * 8 + (lane >> 3);
#pragma unroll 1
      for (int it = b ? 0 : NP / 4; it < 4; ++it) {
        const char* const base = it == 3 ? nxt : cur;
        steer(MUL);
        float pick = 0.f;
#pragma unroll
        for (int k = 0; k < 4; ++k) {
          if (!(k & 1)) report(PBASE + b * 8 + it * 2 + (k >> 1));
          pace();
          float d = 0.f;
#pragma unroll
          for (int j = 0; j < 3; ++j) { d = dot8(U[NP + k][j], q[j], d); asm volatile("" : "+v"(d)); SB(); U[NP + k][j] = fetch(base, 4 * it + 4 + k, j); }
          d += dpp<XOR1>(d); d += dpp<XOR2>(d); d += dpp<HMIR>(d);
          pick = (lane & 7) == k ? d : pick;
          if (b == 0) {
            float e = dot8(U[k][2], q[2], dot8(U[k][1], q[1], dot8(U[k][0], q[0], 0.f)));
            e += dpp<XOR1>(e); e += dpp<XOR2>(e); e += dpp<HMIR>(e);
            pick = (lane & 7) == 4 + k ? e : pick;
#pragma unroll
            for (int j = 0; j < 3; ++j) U[k][j] = U[4 + k][j];
          }
          asm volatile("" : "+v"(pick));
          SB();
          ULOG(b * 16 + it * 4 + k);
        }
        if (b == 0 || !(lane & 4)) sc[it * 128 + (lane & 3) * 32 - (lane & 4) * 64] = rbf(rbf(pick) * 0.07216878364870322f);
        SB();
      }
      STAMP(16 + b * 4 + ww);
    }
    f4 o[3];
    const bool useful = ((lane >> 3) & 1) == (lane >> 5);
    LAS char* part = lds + L_PART + ((ww * 2 + (lane >> 5)) * DH + (lane & 7) * 8 + ((lane >> 4) & 1) * 4) * 4;
    const LAS unsigned* pp = (const LAS unsigned*)(lds + L_PP) + ww * 8 + (lane >> 3);
    auto vstep = [&](int n, const char* base, bool more) {
      unsigned pv[4];
#pragma unroll
      for (int k = 0; k < 4; ++k) pv[k] = pp[n * 128 + k * 32];
#pragma unroll
      for (int k = 0; k < 4; ++k) {
        if (k == 2 && more) report(PBASE + 17 + 2 * n);
        const v4u a = msk & pv[k];
        SB();
        if (more) pace();
#pragma unroll
        for (int j = 0; j < 3; ++j) { o[j] = mfma(a, U[NP + k][j], o[j]); SB(); if (more) U[NP + k][j] = fetch(base, 4 * n + 4 + k, j); }
        ULOG(32 + 4 * n + k);
      }
    };
    auto vflush = [&](int b) {
      if (useful) {
#pragma unroll
        for (int j = 0; j < 3; ++j) *(LAS f4*)(part + b * (8 * DH * 4) + j * 256) = o[j];
      }
    };
#pragma unroll 1
    for (int n = 0; n < 7; ++n) {
      report(PBASE + 16 + 2 * n);
      if (n == 6) wb = WEND - 4;
      steer(n < 5 ? MUL : n == 5 ? 36 : 85);
      if ((n & 3) == 0) {
        wait1<0>(lds, 16, 1 + (n >> 2));
#pragma unroll
        for (int j = 0; j < 3; ++j) o[j] = (f4){0.f, 0.f, 0.f, 0.f};
        STAMP(24 + (n >> 2) * 4 + ww);
      }
      vstep(n, n >= 3 ? vb0 + BSTR : vb0, true);
      if (n == 3) vflush(0);
    }
    fin -= adj;
    report(PBASE + 30);
    vstep(7, vb0, false);
    vflush(1);
    post(lds, ww, PBASE + 32, lane);
    STAMP(32 + ww);
  } else {
    meet();
    if (job) {
      float v[10], sum = 0.f;
#pragma unroll
      for (int i = 0; i < 5; ++i) { v[2 * i] = __uint_as_float(xr[i] << 16); v[2 * i + 1] = __uint_as_float(xr[i] & 0xffff0000u); sum += v[2 * i] + v[2 * i + 1]; }
      const float mean = wave_sum(sum) * (1.f / D);
      float q = 0.f;
#pragma unroll
      for (int i = 0; i < 10; ++i) { const float dd = v[i] - mean; q += dd * dd; }
      const float var = rbf(wave_sum(q) * (1.f / D)), mb = rbf(mean);
      const float inv = __builtin_amdgcn_rcpf(rbf(__builtin_amdgcn_sqrtf(rbf(var + 1e-5f))));
#pragma unroll
      for (int i = 0; i < 10; ++i) {
        const unsigned t = bfbits(rbf(v[i] - mb) * inv);
        *(LAS unsigned*)(lds + L_XX + ((lane + 64 * (i >> 1)) * 2 + (i & 1)) * 8 + (wave & 1) * 4) = t | (t << 16);
      }
      post(lds, 8 + (wave & 1), 1, lane);
      waitn<0, 2>(lds, 8, 1);
      if (wave == 0) STAMP(8);
#pragma unroll 1
      for (int pass = 0;; ++pass) {
        f4 a0 = {0.f, 0.f, 0.f, 0.f}, a1 = a0;
        const LAS char* xp = lds + L_XX + (pass ? mem * 80 + (lane >> 3) : row0 + wave * 80) * 8;
#pragma unroll
        for (int i = 0; i < 10; ++i) {
          const v2u xv = *(const LAS v2u*)(xp + i * 64);
          a0 = mfma(msk & xv.x, R[i], a0); a1 = mfma(msk & xv.y, R[i], a1);
        }
        const bool more = wave == 3 && ffn && pass == 0;
        if (more) {
          const rsrc_t wr = mk_rsrc(wu_, D * F * 2);
          const unsigned uoff = (mem * 80 + (lane >> 3)) * (F * 2) + fb * 128 + (lane & 7) * 16;
#pragma unroll
          for (int i = 0; i < 10; ++i) { R[i] = ld_nt(wr, uoff, i * (8 * F * 2)); SB(); }
        }
        const f4 s = {fold(a0[0], a1[0]), fold(a0[1], a1[1]), fold(a0[2], a1[2]), fold(a0[3], a1[3])};
        if ((lane & 8) == 0) *(LAS f4*)(lds + L_WO + (wave * 2 + (lane >> 5)) * 256 + (lane & 7) * 32 + ((lane >> 4) & 1) * 16) = s;
        post(lds, 4 + wave, 1, lane);
        if (wave < 3) break;
        waitn<1, 4>(lds, 4, 1);
        const LAS float* pr = (const LAS float*)(lds + L_WO) + lane;
        const float c0 = pr[384], c1 = pr[448];
        put2(wsr, WS_P + ((h * 16 + mem + 8 * pass) * 64 + lane) * 16, tag, pass ? c0 : (pr[0] + pr[128]) + (pr[256] + c0), pass ? c1 : (pr[64] + pr[192]) + (pr[320] + c1));
        STAMP(9 + pass);
        if (!more) break;
      }
    }
    if (wave < 3) {
      const int ot = wave * 64 + lane;
      {
        wait1<4>(lds, 0, T_Q);
        float r0, r1;
        sweep2<2>(wsr, WS_P + ((h * 16 + wave * 2) * 64 + lane) * 16, 1024, tag, true, r0, r1);
        LAS u16* dst = (LAS u16*)(lds + L_QO) + ot;
        dst[0] = (u16)bfbits(r0); dst[DH] = (u16)bfbits(r1);
        post(lds, 20 + wave, 1, lane);
        if (wave == 0) STAMP(40);
      }
      {
        if (wave == 2) {
          if (ffn) {
            wait1<8>(lds, 0, T_H);
            float gt[2], up[2];
            sweep2<2>(wsr, WS_P + ((h * 16 + 6) * 64 + lane) * 16, 1024, tag, true, gt[0], gt[1]);
            sweep2<8>(wsr, WS_P + ((h * 16 + 8) * 64 + lane) * 16, 1024, tag, true, up[0], up[1]);
#pragma unroll
            for (int b = 0; b < 2; ++b) {
              const float g = rbf(gt[b]), t2 = 2.f * 0.7978845608028654f * (g + 0.044715f * g * g * g);
              *(LAS u16*)(lds + L_HB + b * 128 + lane * 2) = (u16)bfbits(rbf(g * __builtin_amdgcn_rcpf(1.f + __expf(-t2))) * rbf(up[b]));
            }
            STAMP(45);
          }
          post(lds, 18, 1, lane);
        } else
        if (wave == 0) {
          gu64* stbox = (gu64*)(ws + WS_ST) + h * 32;
#pragma unroll 1
          for (int b = 0; b < 2; ++b) {
            waitn<4, 4>(lds, 0, PBASE + b * 8 + 8);
            const f4 s0 = *(const LAS f4*)(lds + L_SC + b * 2048 + lane * 32), s1 = *(const LAS f4*)(lds + L_SC + b * 2048 + lane * 32 + 16);
            const float s[8] = {s0.x, s0.y, s0.z, s0.w, s1.x, s1.y, s1.z, s1.w};
            constexpr float L2E = 1.4426950408889634f;
            float m = s[0];
#pragma unroll
            for (int i = 1; i < 8; ++i) m = fmaxf(m, s[i]);
            m = wave_max(m);
            const float ms = m == -INFINITY ? 0.f : m;
            float l = 0.f;
#pragma unroll
            for (int i = 0; i < 8; ++i) l += __builtin_amdgcn_exp2f((s[i] - ms) * L2E);
            l = wave_sum(l);
            if (lane < 2) put(stbox + b * 16 + mem * 2 + lane, tag, __float_as_uint(lane ? l : m));
            wait1<4>(lds, 0, PBASE + b * 8 + 8 + T_ST);
            float st1[1];
            sweep<1, 16>(stbox + b * 16 + (lane & 15), 0, tag, st1);
            const float oth = dpp<XOR1>(st1[0]), mj = (lane & 1) ? oth : st1[0], lj = (lane & 1) ? st1[0] : oth;
            float M = fmaxf(mj, dpp<ROR2>(mj)); M = fmaxf(M, dpp<ROR4>(M)); M = fmaxf(M, dpp<ROR8>(M));
            float Ls = lj * __builtin_amdgcn_exp2f((mj - M) * L2E);
            Ls += dpp<ROR2>(Ls); Ls += dpp<ROR4>(Ls); Ls += dpp<ROR8>(Ls);
            const float inv = __builtin_amdgcn_rcpf(__uint_as_float(__builtin_amdgcn_readfirstlane(__float_as_uint(Ls))));
            unsigned p[8];
#pragma unroll
            for (int i = 0; i < 8; ++i) { const unsigned t = bfbits(__builtin_amdgcn_exp2f((s[i] - M) * L2E) * inv); p[i] = t | (t << 16); }
            *(LAS v4u*)(lds + L_PP + b * 2048 + lane * 32) = (v4u){p[0], p[1], p[2], p[3]};
            *(LAS v4u*)(lds + L_PP + b * 2048 + lane * 32 + 16) = (v4u){p[4], p[5], p[6], p[7]};
            post(lds, 16, b + 1, lane);
            STAMP(36 + b);
          }
        } else {
          unsigned at = t0 * 16u + CP0, plan = fin0;
          float prev = 0.f;
#pragma unroll 1
          for (int c = 0; c < NCP; ++c) {
            at = __builtin_amdgcn_readfirstlane(at);
            until(at);
            const v4u e = *(volatile LAS v4u*)(lds + L_FL + 48);
            gu64* cp = (gu64*)(ws + WS_CP) + c * 256;
            if (lane == 0 && (mem & 3) == CPMEM) put(cp + (w >> 5) * 8 + (w & 7), tag, e.x + (unsigned)(((int)(e.y - e.x) + (int)(e.z - e.x) + (int)(e.w - e.x)) >> 2));
            until(at + (c ? CPW : CPW0));
            const u64 x = __hip_atomic_load(cp + lane, RLX_AGENT);
            const bool ok = (unsigned)(x >> 32) == tag;
            const float sum = ok ? (float)(int)((unsigned)x - fin0) : 0.f, cnt = ok ? 1.f : 0.f;
            const float est = wave_sum(sum) * __builtin_amdgcn_rcpf(fmaxf(wave_sum(cnt), 1.f));
            const float over = c ? est - (float)(int)(plan - fin0) : 0.f;
            if (c == 0 || __builtin_amdgcn_readfirstlane(over > (float)LAM))
              plan = fin0 + __builtin_amdgcn_readfirstlane((int)fminf(fmaxf(est + KAP * fmaxf(c ? est - prev : 0.f, 0.f) + (float)(c ? MARG : MARG0), -8000.f), 16000.f));
            prev = est;
            post(lds, 11, plan, lane);
            at = plan - (CPL + (NCP - 1 - c) * CPD);
            at += CPD;
            STAMPV(49 + c, plan);
          }
        }
      }
      waitn<4, 4>(lds, 0, PBASE + 28);
      const LAS float* pr = (const LAS float*)(lds + L_PART) + ot;
      LAS u16* odst = (LAS u16*)(lds + L_QO) + (2 + (lane >> 5)) * DH + wave * 32 + (lane & 31);
      float s0 = 0.f, s1 = 0.f;
#pragma unroll
      for (int r = 0; r < 8; ++r) s0 += pr[r * DH];
      unsigned oout = WS_O + ((h * 8 + mem) * DH + ot) * 16;
      unsigned oin = WS_O + ((hc * 8 + (lane >> 5) * 4) * DH + (mem & 1) * 96 + wave * 32 + (lane & 31)) * 16;
      asm volatile("" : "+v"(s0), "+v"(oout), "+v"(oin), "+v"(odst), "+v"(pr));
      wait1<2>(lds, 3, PBASE + 30);
      const unsigned plan = ref_time(lds);
      waitn<0, 4>(lds, 0, PBASE + 32);
      s1 = ((pr[8 * DH] + pr[9 * DH]) + (pr[10 * DH] + pr[11 * DH])) + ((pr[12 * DH] + pr[13 * DH]) + (pr[14 * DH] + pr[15 * DH]));
      put2(wsr, oout, tag, FIRST ? s1 : s0, FIRST ? s0 : s1);
      if (wave == 0) STAMP(39);
      until(plan + T_O1G);
      float r0, r1;
      sweep2<4>(wsr, oin, DH * 16, tag, false, r0, r1);
      auto t = __builtin_amdgcn_permlane32_swap(__float_as_uint(r0), __float_as_uint(r1), false, false);
      *odst = (u16)bfbits(__uint_as_float(t[0]) + __uint_as_float(t[1]));
      if (wave == 0) STAMP(43);
      BAR();
      __builtin_amdgcn_s_waitcnt(0x0F70);
      return;
    }
  }
  __builtin_amdgcn_s_waitcnt(0x0F70);
  if (wave == 3) {
    const unsigned lds0 = (unsigned)(size_t)lds;
    const RowMap rm = row_map(lane);
    wait1<8>(lds, 18, 1);
    wait1<8>(lds, 0, PBASE + 26);
    warm_code(3);
    if (w < 160) xres = __uint_as_float((unsigned)((const GAS u16*)x_)[oi] << 16);
    asm volatile("" : "+v"(xres));
    wait1<2>(lds, 3, PBASE + 29);
    const unsigned own = peek(lds, 15), pl = peek(lds, 11);
    const unsigned due = ((int)(own - pl) > 0 ? own : pl) + WO0, tau = ffn ? TAU_WO : TAU_WO * 4 / 3;
    STAMPV(47, due);
    if (ffn) stage_rows((gptr)wd_ + ((fb * 64 + (mem & 1) * 32) * (D * 2) + (mem >> 1) * 320), lds0 + L_WD, 2, rm, due, tau);
    stage_rows((gptr)wo_ + ((hc * DH + (mem & 1) * 96) * (D * 2) + (mem >> 1) * 320), lds0 + L_WO, 6, rm, ffn ? due + 2 * TAU_WO : due, tau);
  }
  const int tt = wave - 3;
  const LAS char* ap = lds + L_QO + 768 + ((lane >> 2) & 1) * 384 + (lane >> 4) * 16;
  const LAS char* bp = lds + L_WO + ((lane >> 4) * 8 + ((lane >> 2) & 3)) * 320 + tt * 64 + (lane & 3) * 8;
  const LAS char* hp = lds + L_HB + ((lane >> 2) & 1) * 128 + (mem & 1) * 64 + (lane >> 4) * 16;
  unsigned ydst = WS_Y + (((((mem >> 1) * 5 + tt) * 4 + (lane >> 4)) * 64 + hc * 2 + (mem & 1)) * 16 + (lane & 15)) * 16;
  const bool last = wave == 3 && w < 160;
  const unsigned look = ref_time(lds) + T_YG;
  asm volatile("s_waitcnt vmcnt(10)" : "+v"(bp), "+v"(hp), "+v"(ap), "+v"(ydst) :: "memory");
  if (wave == 3) post(lds, 17, 1, lane);
  wait1<0>(lds, 17, 1);
  v4u bf[6];
#pragma unroll
  for (int i = 0; i < 4; ++i) bf[i] = tr_frag(bp + (i >> 1) * 10240 + (i & 1) * 32);
  const f4 zero = {0.f, 0.f, 0.f, 0.f};
  const v4u hf = *(const LAS v4u*)hp;
  const f4 g0 = mfma(hf, tr_frag(bp + L_WD), zero), g1 = mfma(hf, tr_frag(bp + L_WD + 32), zero);
  float gx = w < 64 ? 0.f : lane < 32 ? g0.x : g1.x;
  asm volatile("s_waitcnt vmcnt(0)" : "+v"(ap), "+v"(ydst), "+v"(gx), "+v"(bf[0]), "+v"(bf[1]), "+v"(bf[2]), "+v"(bf[3]) :: "memory");
  if (wave == 3) STAMP(48);
  BAR();
  {
    v4u a[3];
#pragma unroll
    for (int ks = 0; ks < 3; ++ks) a[ks] = *(const LAS v4u*)(ap + ks * 64);
    bf[4] = tr_frag(bp + 20480); bf[5] = tr_frag(bp + 20480 + 32);
    SB();
    f4 y0 = zero, y1 = zero;
#pragma unroll
    for (int ks = 0; ks < 3; ++ks) { y0 = mfma(a[ks], bf[2 * ks], y0); y1 = mfma(a[ks], bf[2 * ks + 1], y1); }
    put2(wsr, ydst, tag, lane < 32 ? y0.x : y1.x, gx);
    if (wave == 4) STAMPG(55);
  }
  if (last) {
    unsigned ysrc = WS_Y + (w >> 1) * (64 * 16 * 16) + (w & 1) * 128 + (lane >> 3) * 256 + (lane & 7) * 16;
    GAS u16* op = (GAS u16*)out_ + oi;
    asm volatile("" : "+v"(ysrc), "+v"(op));
    until(look);
    float s, gs;
    sweep2<8>(wsr, ysrc, 2048, tag, false, s, gs);
    s += dpp<ROR8>(s); gs += dpp<ROR8>(gs);
    s = rbf(xrow_sum(s)); gs = rbf(xrow_sum(gs));
    const float gate = rbf(gs * __builtin_amdgcn_rcpf(1.f + __expf(-gs)));
    if (lane < 8) *op = (u16)bfbits(xres + rbf(s * gate));
    if (w == 0 && lane == 0) __hip_atomic_store((GAS unsigned*)(ws + WS_EPOCH), tag, RLX_AGENT);
    STAMPG(56);
  }
  asm volatile(".Lfd_last:");
}

extern "C" void kernel_launch(void* const* d_in, const int* in_sizes, int n_in, void* d_out, int out_size, void* d_ws, size_t ws_size,
                              hipStream_t stream) {
  fused_decode<<<256, NTHREADS, 0, stream>>>((const u16*)d_in[0], (const u16*)d_in[1], (const u16*)d_in[2], (const u16*)d_in[3],
                                             (const u16*)d_in[4], (const u16*)d_in[6], (const u16*)d_in[7], (const u16*)d_in[8],
                                             (u16*)d_out, (char*)d_ws);
}
```

```cpp
#include <hip/hip_runtime.h>
#include <stdint.h>

typedef unsigned short u16;
typedef unsigned long long u64;
typedef unsigned v4u __attribute__((ext_vector_type(4)));
typedef unsigned v2u __attribute__((ext_vector_type(2)));
typedef float f4 __attribute__((ext_vector_type(4)));
typedef short s4 __attribute__((ext_vector_type(4)));
typedef __bf16 bf16x2 __attribute__((ext_vector_type(2)));
typedef __bf16 bf16x8 __attribute__((ext_vector_type(8)));
#define GAS __attribute__((address_space(1)))
#define LAS __attribute__((address_space(3)))
#define RLX_AGENT __ATOMIC_RELAXED, __HIP_MEMORY_SCOPE_AGENT
typedef const GAS char* gptr;
typedef GAS u64 gu64;
typedef __amdgpu_buffer_rsrc_t rsrc_t;

#define NP 8
#define PBASE 2
#define T_Q 2
#define T_ST 4
#define T_H 12
#define TAU 650
#define ORG 2600
#define TEST_PH 0
#define TEST_D 64
#define FIRST 1
#define WBASE 24
#define WSH 8
#define WL0 0
#define WMAX 10
#define NCP 5
#define CP0 8800
#define CPW0 4400
#define CPW 2400
#define CP1 28000
#define CP2 20400
#define CP3 13200
#define CP4 9000
#define LAM 100
#define WB0 24
#define WB2 24
#define WBN 3
#define KAP2 1.f
#define KAP3 2.f
#define KAP4 0.3f
#define MARG0 80
#define MARG 100
#define NFIRST 2
#define LEND 15
#define KMAX 0.35f
#define DEADM 600
#define GAIN 0.015f
#define RETRY 2
#define CAPW 1000
#define OWNY 6
#define OWNS 9
#define MUL 23
#define FAST 100
#define T_O1G 3000
#define T_YG 4800
#define CPMEM 1
#define WO0 600
#define TAU_WO 270

constexpr int D = 640, H = 32, SK = 4096, DH = 192, F = 1536, NTHREADS = 512;
constexpr int STEP = 32 * 3072;
constexpr unsigned SPIN_CAP = 1u << 16;

constexpr int WS_EPOCH = 0;
constexpr int WS_CP = 4096;
constexpr int WS_ST = WS_CP + 8 * 256 * 8;
constexpr int WS_P = WS_ST + H * 2 * 16 * 8;
constexpr int WS_O = WS_P + H * 16 * 64 * 16;
constexpr int WS_Y = WS_O + H * 8 * DH * 16;
constexpr int WS_END = WS_Y + 80 * 64 * 16 * 16;
constexpr int L_WO = 0;
constexpr int L_WD = 30720;
constexpr int L_XX = L_WD + 10240;
constexpr int L_PART = L_XX + 5120;
constexpr int L_QO = L_PART + 12288;
constexpr int L_HB = L_QO + 1536;
constexpr int L_SC = L_HB + 256;
constexpr int L_PP = L_SC + 4096;
constexpr int L_FL = L_PP + 4096;
constexpr int L_DG = L_FL + 160;
constexpr int L_TOTAL = 84 * 1024;

__device__ __forceinline__ float rbf(float f) { return (float)(__bf16)f; }
__device__ __forceinline__ unsigned bfbits(float f) { return (unsigned)__builtin_bit_cast(u16, (__bf16)f); }
__device__ __forceinline__ float dot2(unsigned a, unsigned b, float acc) {
  return __builtin_amdgcn_fdot2_f32_bf16(__builtin_bit_cast(bf16x2, a), __builtin_bit_cast(bf16x2, b), acc, false);
}
__device__ __forceinline__ float dot8(v4u a, v4u b, float acc) {
  return dot2(a.w, b.w, dot2(a.z, b.z, dot2(a.y, b.y, dot2(a.x, b.x, acc))));
}
__device__ __forceinline__ f4 mfma(v4u a, v4u b, f4 c) {
  return __builtin_amdgcn_mfma_f32_16x16x32_bf16(__builtin_bit_cast(bf16x8, a), __builtin_bit_cast(bf16x8, b), c, 0, 0, 0);
}
template <int CTRL> __device__ __forceinline__ float dpp(float x) {
  return __builtin_bit_cast(float, __builtin_amdgcn_mov_dpp(__builtin_bit_cast(int, x), CTRL, 0xf, 0xf, true));
}
constexpr int XOR1 = 0xB1, XOR2 = 0x4E, HMIR = 0x141, ROR2 = 0x122, ROR4 = 0x124, ROR8 = 0x128;
__device__ __forceinline__ float xrow_sum(float x) {
  auto s = __builtin_amdgcn_permlane16_swap(__float_as_uint(x), __float_as_uint(x), false, false);
  x = __uint_as_float(s[0]) + __uint_as_float(s[1]);
  auto t = __builtin_amdgcn_permlane32_swap(__float_as_uint(x), __float_as_uint(x), false, false);
  return __uint_as_float(t[0]) + __uint_as_float(t[1]);
}
__device__ __forceinline__ float xrow_max(float x) {
  auto s = __builtin_amdgcn_permlane16_swap(__float_as_uint(x), __float_as_uint(x), false, false);
  x = fmaxf(__uint_as_float(s[0]), __uint_as_float(s[1]));
  auto t = __builtin_amdgcn_permlane32_swap(__float_as_uint(x), __float_as_uint(x), false, false);
  return fmaxf(__uint_as_float(t[0]), __uint_as_float(t[1]));
}
__device__ __forceinline__ float wave_sum(float x) {
  x += dpp<XOR1>(x); x += dpp<XOR2>(x); x += dpp<HMIR>(x); x += dpp<ROR8>(x);
  return xrow_sum(x);
}
__device__ __forceinline__ float wave_max(float x) {
  x = fmaxf(x, dpp<XOR1>(x)); x = fmaxf(x, dpp<XOR2>(x)); x = fmaxf(x, dpp<HMIR>(x)); x = fmaxf(x, dpp<ROR8>(x));
  return xrow_max(x);
}
__device__ __forceinline__ float fold(float a, float b) {
  auto t = __builtin_amdgcn_permlane32_swap(__float_as_uint(a), __float_as_uint(b), false, false);
  return __uint_as_float(t[0]) + dpp<ROR8>(__uint_as_float(t[1]));
}
__device__ __forceinline__ rsrc_t mk_rsrc(const void* p, int bytes) { return __builtin_amdgcn_make_buffer_rsrc((void*)p, 0, bytes, 0x00020000); }
__device__ __forceinline__ v4u ld_nt(rsrc_t r, unsigned voff, unsigned soff) { return __builtin_amdgcn_raw_buffer_load_b128(r, voff, soff, 2); }
#define SB() __builtin_amdgcn_sched_barrier(0)
#define BAR() asm volatile("s_waitcnt lgkmcnt(0)\n\ts_barrier" ::: "memory")
__device__ __forceinline__ void put(gu64* g, unsigned tag, unsigned v) { __hip_atomic_store(g, ((u64)tag << 32) | v, RLX_AGENT); }
__device__ __forceinline__ void put2(rsrc_t r, unsigned off, unsigned tag, float a, float b) {
  __builtin_amdgcn_raw_buffer_store_b128((v4u){__float_as_uint(a), tag, __float_as_uint(b), tag}, r, off, 0, 16);
}
template <int N, int SLEEP> __device__ __forceinline__ void sweep(gu64* g, int stride, unsigned tag, float (&v)[N]) {
#pragma unroll 1
  for (unsigned spins = 0; spins < SPIN_CAP; ++spins) {
    bool ok = true;
#pragma unroll
    for (int k = 0; k < N; ++k) {
      const u64 x = __hip_atomic_load(g + k * stride, RLX_AGENT);
      v[k] = __uint_as_float((unsigned)x);
      ok &= (unsigned)(x >> 32) == tag;
    }
    if (__all(ok)) return;
    if (SLEEP) __builtin_amdgcn_s_sleep(SLEEP);
  }
#pragma unroll
  for (int k = 0; k < N; ++k) v[k] = __builtin_nanf("");
}
template <int N> __device__ __forceinline__ void sweep2(rsrc_t r, unsigned off, int stride, unsigned tag, bool lazy, float& a, float& b) {
#pragma unroll 1
  for (unsigned spins = 0; spins < SPIN_CAP; ++spins) {
    asm volatile("" ::: "memory");
    v4u x[N];
#pragma unroll
    for (int k = 0; k < N; ++k) x[k] = __builtin_amdgcn_raw_buffer_load_b128(r, off, k * stride, 16);
    unsigned lo = tag, hi = tag;
#pragma unroll
    for (int k = 0; k < N; ++k) {
      lo = min(min(lo, x[k].y), x[k].w); hi = max(max(hi, x[k].y), x[k].w);
      a = k ? a + __uint_as_float(x[k].x) : __uint_as_float(x[k].x);
      b = k ? b + __uint_as_float(x[k].z) : __uint_as_float(x[k].z);
      asm volatile("" : "+v"(a), "+v"(b), "+v"(lo), "+v"(hi));
    }
    if (__builtin_amdgcn_ballot_w64(lo != hi) == 0) return;
    if (lazy) __builtin_amdgcn_s_sleep(24);
  }
  a = b = __builtin_nanf("");
}
__device__ __forceinline__ void post(LAS char* lds, int word, unsigned v, int lane) {
  asm volatile("" ::: "memory");
  if (lane == 0) *(volatile LAS unsigned*)(lds + L_FL + word * 4) = v;
  asm volatile("" ::: "memory");
}
__device__ __forceinline__ unsigned peek(LAS char* lds, int word) {
  return __builtin_amdgcn_readfirstlane(*(volatile LAS unsigned*)(lds + L_FL + word * 4));
}
template <int SLEEP> __device__ __forceinline__ void wait1(LAS char* lds, int word, unsigned want) {
  while (peek(lds, word) < want) { if (SLEEP) __builtin_amdgcn_s_sleep(SLEEP); }
  asm volatile("" ::: "memory");
}
template <int SLEEP, int N> __device__ __forceinline__ void waitn(LAS char* lds, int word, unsigned want) {
  for (;;) {
    const v4u v = *(volatile LAS v4u*)(lds + L_FL + word * 4);
    unsigned m = min(v.x, v.y);
    if (N >= 3) m = min(m, v.z);
    if (N == 4) m = min(m, v.w);
    if (__builtin_amdgcn_readfirstlane(m) >= want) break;
    if (SLEEP) __builtin_amdgcn_s_sleep(SLEEP);
  }
  asm volatile("" ::: "memory");
}
__device__ __forceinline__ unsigned ref_time(LAS char* lds, int cap) {
  const unsigned plan = *(volatile LAS unsigned*)(lds + L_FL + 44), own = *(volatile LAS unsigned*)(lds + L_FL + 60);
  return __builtin_amdgcn_readfirstlane(plan + min(max((int)(own - plan), 0), cap));
}
__device__ __forceinline__ int until(unsigned due) {
  int late;
  asm volatile("s_branch 2f\n1:\n\ts_sleep 1\n2:\n\ts_memrealtime vcc\n\ts_waitcnt lgkmcnt(0)\n\ts_lshl_b32 %0, vcc_lo, 4\n\t"
               "s_sub_i32 %0, %0, %1\n\ts_cmp_lt_i32 %0, 0\n\ts_cbranch_scc1 1b" : "=&s"(late) : "s"(due) : "vcc", "scc", "memory");
  return late;
}
struct RowMap { unsigned o[5]; };
__device__ __forceinline__ RowMap row_map(int lane) {
  RowMap m;
#pragma unroll
  for (int i = 0; i < 5; ++i) { const unsigned ci = i * 64 + lane, row = (ci * 52429u) >> 20; m.o[i] = row * (D * 2 - 320) + ci * 16; }
  asm volatile("" : "+v"(m.o[0]), "+v"(m.o[1]), "+v"(m.o[2]), "+v"(m.o[3]), "+v"(m.o[4]));
  return m;
}
__device__ __forceinline__ void stage_rows(gptr src, unsigned lds_dst, int ng, const RowMap& m, unsigned due, unsigned tau) {
#pragma unroll 1
  for (int g = 0; g < ng; ++g) {
    unsigned keep;
    until(due); due += tau;
    asm volatile("s_mov_b32 %0, m0\n\ts_mov_b32 m0, %7\n\ts_nop 0\n\tglobal_load_lds_dwordx4 %1, %6\n\t"
                 "s_add_u32 m0, m0, 0x400\n\ts_nop 0\n\tglobal_load_lds_dwordx4 %2, %6\n\t"
                 "s_add_u32 m0, m0, 0x400\n\ts_nop 0\n\tglobal_load_lds_dwordx4 %3, %6\n\t"
                 "s_add_u32 m0, m0, 0x400\n\ts_nop 0\n\tglobal_load_lds_dwordx4 %4, %6\n\t"
                 "s_add_u32 m0, m0, 0x400\n\ts_nop 0\n\tglobal_load_lds_dwordx4 %5, %6\n\ts_mov_b32 m0, %0"
                 : "=&s"(keep) : "v"(m.o[0]), "v"(m.o[1]), "v"(m.o[2]), "v"(m.o[3]), "v"(m.o[4]), "s"(src), "s"(lds_dst) : "scc", "memory");
    src += 16 * D * 2; lds_dst += 5120;
  }
}
__device__ __forceinline__ v4u tr_frag(const LAS char* p) {
  const v2u b0 = __builtin_bit_cast(v2u, __builtin_amdgcn_ds_read_tr16_b64_v4i16((LAS s4*)p));
  const v2u b1 = __builtin_bit_cast(v2u, __builtin_amdgcn_ds_read_tr16_b64_v4i16((LAS s4*)(p + 1280)));
  return (v4u){b0.x, b0.y, b1.x, b1.y};
}

#define STAMP(i)
#define STAMPV(i, v)
#define ULOG(i)
#define STAMPG(i)

extern "C" __global__ void __launch_bounds__(512, 2)
fused_decode(const u16* x_, const u16* kc_, const u16* vc_, const u16* wq_, const u16* wo_, const u16* wg_, const u16* wu_,
             const u16* wd_, u16* out_, char* ws_) {
  unsigned ka0, ka1;
  asm volatile("s_load_dword %0, %2, 0x0\n\ts_load_dword %1, %2, 0x40" : "=&s"(ka0), "=&s"(ka1) : "s"(__builtin_amdgcn_kernarg_segment_ptr()));
  SB();
  __shared__ __attribute__((aligned(16))) char lds_raw[L_TOTAL];
  LAS char* lds = (LAS char*)lds_raw;
  GAS char* ws = (GAS char*)ws_;
  const int tid = threadIdx.x, lane = tid & 63, wave = __builtin_amdgcn_readfirstlane(tid >> 6);
  const int w = blockIdx.x, h = (w & 7) * 4 + (w >> 6), mem = (w >> 3) & 7;
  const int ww = wave - 4;
  const int hc = (h + 4) & 31;
  u64 code0;
  {
    u64 pc; unsigned back;
    asm volatile("s_getpc_b64 %0\n.Lfd_here:\n\ts_mov_b32 %1, .Lfd_here-fused_decode" : "=s"(pc), "=s"(back));
    code0 = pc - back;
  }
  auto warm_code = [&](int who) {
    u64 ex; unsigned keep;
    const unsigned c0 = (unsigned)(tid - who * 64) * 4096u + (w >> 3) * 128u;
    asm volatile("v_cmp_gt_u32 vcc, .Lfd_last-fused_decode, %2\n\ts_and_saveexec_b64 %0, vcc\n\ts_cbranch_execz 1f\n\t"
                 "s_mov_b32 %1, m0\n\ts_mov_b32 m0, %4\n\ts_nop 0\n\tglobal_load_lds_dword %2, %3\n\ts_mov_b32 m0, %1\n"
                 "1:\n\ts_mov_b64 exec, %0"
                 : "=&s"(ex), "=&s"(keep) : "v"(c0), "s"(code0), "s"((unsigned)(size_t)lds + L_FL + 128) : "vcc", "scc", "memory");
  };
  warm_code(0);
  u64 t64 = __builtin_amdgcn_s_memrealtime();
  const unsigned t0 = (unsigned)t64;
  const bool ffn = w >= 64, isq = mem < 6;
  const int fb = (w & 7) * 3 + (w >> 6) - 1;
  const int row0 = (mem & 1) * 320 + (lane >> 3), qm = (mem - 6) >> 31;
  unsigned rs8 = isq ? 8 * H * DH * 2 : 8 * F * 2;
  int jobw = wave + ((w >> 4) == 3 ? 4 : 0);
  unsigned woff = (row0 + wave * 80) * (rs8 >> 3) + (((h * 3 + (mem >> 1)) & qm) | (fb & ~qm)) * 128 + (lane & 7) * 16;
  unsigned xoff = (wave & 1) * (D * 2) + lane * 4;
  asm volatile("" : "+s"(rs8), "+s"(jobw));
  asm volatile("" : "+v"(woff), "+v"(xoff));
  SB();
  const bool job = jobw < 4;
  unsigned xr[5];
  v4u R[10];
  if (job) {
#pragma unroll
    for (int i = 0; i < 5; ++i) xr[i] = *(const GAS unsigned*)((gptr)x_ + xoff + 256 * i);
    SB();
    const rsrc_t wr = mk_rsrc(isq ? wq_ : wg_, 80 * rs8);
    unsigned so = 0;
#pragma unroll
    for (int i = 0; i < 10; ++i) { R[i] = ld_nt(wr, woff, so); so += rs8; SB(); }
  }
  STAMP(wave);
  const rsrc_t wsr = mk_rsrc(ws_, WS_END);
  const unsigned fin0 = t0 * 16u + (ORG + 63 * TAU - 16 * TEST_D * __builtin_popcount(TEST_PH));
  unsigned tag;
  v4u msk;
  const int oi = ((w >> 1) & 1) * D + (w >> 2) * 16 + (w & 1) * 8 + (lane & 7);
  float xres = 0.f;
  auto meet = [&]() {
    tag = *(const __attribute__((address_space(4))) unsigned*)(ws_ + WS_EPOCH) + 1u;
    const unsigned mm = (lane & 1) ? 0xffff0000u : 0x0000ffffu;
    const int q = (lane & 7) >> 1;
    msk = (v4u){q == 0 ? mm : 0u, q == 1 ? mm : 0u, q == 2 ? mm : 0u, q == 3 ? mm : 0u};
    asm volatile("" : "+s"(tag) : "s"(t64), "s"(ka0), "s"(ka1));
    asm volatile("" : "+v"(msk));
    BAR();
  };

  if (wave >= 4) {
    __builtin_amdgcn_s_waitcnt(0x0F70);
    if (tid < 280) *(volatile LAS unsigned*)(lds + L_FL + lane * 4) = (lane & 15) < 11 ? 0u : fin0;
    meet();
    const int rot = (h & 3) * 4;
    const unsigned loff = (mem * 4 + ww) * 3072 + (lane >> 3) * 384 + (lane & 7) * 16;
    constexpr long HSTR = (long)SK * DH * 2, BSTR = FIRST ? -H * HSTR : H * HSTR;
    const char* const kb0 = (const char*)kc_ + (FIRST * H + h) * HSTR;
    const char* const vb0 = (const char*)vc_ + (FIRST * H + h) * HSTR;
    unsigned due = t0 * 16u + ORG + ww * (TAU / 4), fin = fin0;
    int adj = 0, late = 0, win, dith = ww, wb = WB0 - 4;
    auto pace = [&]() {
      late = until(due); due += TAU + adj - (((TEST_PH >> ((dith - ww) >> 4)) & 1) ? TEST_D : 0); fin += adj;
      win = min((wb + (max(late - WL0, 0) >> WSH) + (dith & 3)) >> 2, WMAX - 1); ++dith;
      SB();
    };
    auto report = [&](unsigned prog) {
      asm volatile("" ::: "memory");
      if (lane == 0) { *(volatile LAS unsigned*)(lds + L_FL + ww * 4) = prog; *(volatile LAS unsigned*)(lds + L_FL + 48 + ww * 4) = fin + late; }
      asm volatile("" ::: "memory");
    };
    auto steer = [&](int left, int mul) {
      const v2u pl = *(volatile LAS v2u*)(lds + L_FL + 40);
      const int slope = left > LEND ? __builtin_amdgcn_readfirstlane(pl.x) : 0;
      const int gap = (int)(__builtin_amdgcn_readfirstlane(pl.y) - fin) - slope * (left - LEND);
      adj = min(max(slope + ((gap * mul) >> 8), -FAST), TAU / 2);
    };
    auto fetch = [&](const char* base, int s, int j) {
      unsigned cnt;
      asm volatile("1:\n\ts_getreg_b32 %0, hwreg(HW_REG_IB_STS, 0, 4)\n\ts_cmp_gt_u32 %0, %1\n\ts_cbranch_scc1 1b" : "=&s"(cnt) : "s"(win) : "scc", "memory");
      const v4u r = ld_nt(mk_rsrc(base, (int)HSTR), loff + j * 128, ((s + rot) & 15) * STEP);
      SB();
      return r;
    };
    v4u U[NP + 4][3];
#pragma unroll
    for (int u = 0; u < NP + 4; ++u) {
      pace();
#pragma unroll
      for (int j = 0; j < 3; ++j) U[u][j] = fetch(kb0, u, j);
      if (u & 1) report(u >> 1);
      if (u >= 4) ULOG(u - 4);
    }
    waitn<0, 3>(lds, 20, 1);
    if (ww == 0) STAMP(12);
#pragma unroll 1
    for (int b = 0; b < 2; ++b) {
      v4u q[3];
#pragma unroll
      for (int j = 0; j < 3; ++j) q[j] = *(const LAS v4u*)(lds + L_QO + (b ^ FIRST) * 384 + j * 128 + (lane & 7) * 16);
      const char* const cur = kb0 + b * BSTR;
      const char* const nxt = b ? vb0 : kb0 + BSTR;
      LAS float* sc = (LAS float*)(lds + L_SC) + b * 512 + ww * 8 + (lane >> 3);
#pragma unroll 1
      for (int it = b ? 0 : NP / 4; it < 4; ++it) {
        const char* const base = it == 3 ? nxt : cur;
        if (it == 3) wb = (b ? WB2 : WBASE) - 4;
        steer(59 - b * 16 - it * 4, MUL);
        float pick = 0.f;
#pragma unroll
        for (int k = 0; k < 4; ++k) {
          if (!(k & 1)) report(PBASE + b * 8 + it * 2 + (k >> 1));
          pace();
          float d = 0.f;
#pragma unroll
          for (int j = 0; j < 3; ++j) { d = dot8(U[NP + k][j], q[j], d); asm volatile("" : "+v"(d)); SB(); U[NP + k][j] = fetch(base, 4 * it + 4 + k, j); }
          d += dpp<XOR1>(d); d += dpp<XOR2>(d); d += dpp<HMIR>(d);
          pick = (lane & 7) == k ? d : pick;
          if (b == 0) {
            float e = dot8(U[k][2], q[2], dot8(U[k][1], q[1], dot8(U[k][0], q[0], 0.f)));
            e += dpp<XOR1>(e); e += dpp<XOR2>(e); e += dpp<HMIR>(e);
            pick = (lane & 7) == 4 + k ? e : pick;
#pragma unroll
            for (int j = 0; j < 3; ++j) U[k][j] = U[4 + k][j];
          }
          asm volatile("" : "+v"(pick));
          SB();
          ULOG(b * 16 + it * 4 + k);
        }
        if (b == 0 || !(lane & 4)) sc[it * 128 + (lane & 3) * 32 - (lane & 4) * 64] = rbf(rbf(pick) * 0.07216878364870322f);
        SB();
      }
      STAMP(16 + b * 4 + ww);
    }
    f4 o[3];
    const bool useful = ((lane >> 3) & 1) == (lane >> 5);
    LAS char* part = lds + L_PART + ((ww * 2 + (lane >> 5)) * DH + (lane & 7) * 8 + ((lane >> 4) & 1) * 4) * 4;
    const LAS unsigned* pp = (const LAS unsigned*)(lds + L_PP) + ww * 8 + (lane >> 3);
    auto vstep = [&](int n, const char* base, bool more) {
      unsigned pv[4];
#pragma unroll
      for (int k = 0; k < 4; ++k) pv[k] = pp[n * 128 + k * 32];
#pragma unroll
      for (int k = 0; k < 4; ++k) {
        if (k == 2 && more) report(PBASE + 17 + 2 * n);
        const v4u a = msk & pv[k];
        SB();
        if (more) pace();
#pragma unroll
        for (int j = 0; j < 3; ++j) { o[j] = mfma(a, U[NP + k][j], o[j]); SB(); if (more) U[NP + k][j] = fetch(base, 4 * n + 4 + k, j); }
        ULOG(32 + 4 * n + k);
      }
    };
    auto vflush = [&](int b) {
      if (useful) {
#pragma unroll
        for (int j = 0; j < 3; ++j) *(LAS f4*)(part + b * (8 * DH * 4) + j * 256) = o[j];
      }
    };
#pragma unroll 1
    for (int n = 0; n < 7; ++n) {
      report(PBASE + 16 + 2 * n);
      if (n == WBN) wb = WBASE - 4;
      steer(27 - 4 * n, n < 5 ? MUL : n == 5 ? 36 : 85);
      if ((n & 3) == 0) {
        wait1<0>(lds, 16, 1 + (n >> 2));
#pragma unroll
        for (int j = 0; j < 3; ++j) o[j] = (f4){0.f, 0.f, 0.f, 0.f};
        STAMP(24 + (n >> 2) * 4 + ww);
      }
      vstep(n, n >= 3 ? vb0 + BSTR : vb0, true);
      if (n == 3) vflush(0);
    }
    fin -= adj;
    report(PBASE + 30);
    vstep(7, vb0, false);
    vflush(1);
    post(lds, ww, PBASE + 32, lane);
    STAMP(32 + ww);
  } else {
    meet();
    if (job) {
      float v[10], sum = 0.f;
#pragma unroll
      for (int i = 0; i < 5; ++i) { v[2 * i] = __uint_as_float(xr[i] << 16); v[2 * i + 1] = __uint_as_float(xr[i] & 0xffff0000u); sum += v[2 * i] + v[2 * i + 1]; }
      const float mean = wave_sum(sum) * (1.f / D);
      float q = 0.f;
#pragma unroll
      for (int i = 0; i < 10; ++i) { const float dd = v[i] - mean; q += dd * dd; }
      const float var = rbf(wave_sum(q) * (1.f / D)), mb = rbf(mean);
      const float inv = __builtin_amdgcn_rcpf(rbf(__builtin_amdgcn_sqrtf(rbf(var + 1e-5f))));
#pragma unroll
      for (int i = 0; i < 10; ++i) {
        const unsigned t = bfbits(rbf(v[i] - mb) * inv);
        *(LAS unsigned*)(lds + L_XX + ((lane + 64 * (i >> 1)) * 2 + (i & 1)) * 8 + (wave & 1) * 4) = t | (t << 16);
      }
      post(lds, 8 + (wave & 1), 1, lane);
      waitn<0, 2>(lds, 8, 1);
      if (wave == 0) STAMP(8);
#pragma unroll 1
      for (int pass = 0;; ++pass) {
        f4 a0 = {0.f, 0.f, 0.f, 0.f}, a1 = a0;
        const LAS char* xp = lds + L_XX + (pass ? mem * 80 + (lane >> 3) : row0 + wave * 80) * 8;
#pragma unroll
        for (int i = 0; i < 10; ++i) {
          const v2u xv = *(const LAS v2u*)(xp + i * 64);
          a0 = mfma(msk & xv.x, R[i], a0); a1 = mfma(msk & xv.y, R[i], a1);
        }
        const bool more = wave == 3 && ffn && pass == 0;
        if (more) {
          const rsrc_t wr = mk_rsrc(wu_, D * F * 2);
          const unsigned uoff = (mem * 80 + (lane >> 3)) * (F * 2) + fb * 128 + (lane & 7) * 16;
#pragma unroll
          for (int i = 0; i < 10; ++i) { R[i] = ld_nt(wr, uoff, i * (8 * F * 2)); SB(); }
        }
        const f4 s = {fold(a0[0], a1[0]), fold(a0[1], a1[1]), fold(a0[2], a1[2]), fold(a0[3], a1[3])};
        if ((lane & 8) == 0) *(LAS f4*)(lds + L_WO + (wave * 2 + (lane >> 5)) * 256 + (lane & 7) * 32 + ((lane >> 4) & 1) * 16) = s;
        post(lds, 4 + wave, 1, lane);
        if (wave < 3) break;
        waitn<1, 4>(lds, 4, 1);
        const LAS float* pr = (const LAS float*)(lds + L_WO) + lane;
        const float c0 = pr[384], c1 = pr[448];
        put2(wsr, WS_P + ((h * 16 + mem + 8 * pass) * 64 + lane) * 16, tag, pass ? c0 : (pr[0] + pr[128]) + (pr[256] + c0), pass ? c1 : (pr[64] + pr[192]) + (pr[320] + c1));
        STAMP(9 + pass);
        if (!more) break;
      }
    }
    if (wave < 3) {
      const int ot = wave * 64 + lane;
      {
        wait1<4>(lds, 0, T_Q);
        float r0, r1;
        sweep2<2>(wsr, WS_P + ((h * 16 + wave * 2) * 64 + lane) * 16, 1024, tag, true, r0, r1);
        LAS u16* dst = (LAS u16*)(lds + L_QO) + ot;
        dst[0] = (u16)bfbits(r0); dst[DH] = (u16)bfbits(r1);
        post(lds, 20 + wave, 1, lane);
        if (wave == 0) STAMP(40);
      }
      {
        if (wave == 2) {
          if (ffn) {
            wait1<8>(lds, 0, T_H);
            float gt[2], up[2];
            sweep2<2>(wsr, WS_P + ((h * 16 + 6) * 64 + lane) * 16, 1024, tag, true, gt[0], gt[1]);
            sweep2<8>(wsr, WS_P + ((h * 16 + 8) * 64 + lane) * 16, 1024, tag, true, up[0], up[1]);
#pragma unroll
            for (int b = 0; b < 2; ++b) {
              const float g = rbf(gt[b]), t2 = 2.f * 0.7978845608028654f * (g + 0.044715f * g * g * g);
              *(LAS u16*)(lds + L_HB + b * 128 + lane * 2) = (u16)bfbits(rbf(g * __builtin_amdgcn_rcpf(1.f + __expf(-t2))) * rbf(up[b]));
            }
            STAMP(45);
          }
          post(lds, 18, 1, lane);
        } else
        if (wave == 0) {
          gu64* stbox = (gu64*)(ws + WS_ST) + h * 32;
#pragma unroll 1
          for (int b = 0; b < 2; ++b) {
            waitn<4, 4>(lds, 0, PBASE + b * 8 + 8);
            const f4 s0 = *(const LAS f4*)(lds + L_SC + b * 2048 + lane * 32), s1 = *(const LAS f4*)(lds + L_SC + b * 2048 + lane * 32 + 16);
            const float s[8] = {s0.x, s0.y, s0.z, s0.w, s1.x, s1.y, s1.z, s1.w};
            constexpr float L2E = 1.4426950408889634f;
            float m = s[0];
#pragma unroll
            for (int i = 1; i < 8; ++i) m = fmaxf(m, s[i]);
            m = wave_max(m);
            const float ms = m == -INFINITY ? 0.f : m;
            float l = 0.f;
#pragma unroll
            for (int i = 0; i < 8; ++i) l += __builtin_amdgcn_exp2f((s[i] - ms) * L2E);
            l = wave_sum(l);
            if (lane < 2) put(stbox + b * 16 + mem * 2 + lane, tag, __float_as_uint(lane ? l : m));
            wait1<4>(lds, 0, PBASE + b * 8 + 8 + T_ST);
            float st1[1];
            sweep<1, 16>(stbox + b * 16 + (lane & 15), 0, tag, st1);
            const float oth = dpp<XOR1>(st1[0]), mj = (lane & 1) ? oth : st1[0], lj = (lane & 1) ? st1[0] : oth;
            float M = fmaxf(mj, dpp<ROR2>(mj)); M = fmaxf(M, dpp<ROR4>(M)); M = fmaxf(M, dpp<ROR8>(M));
            float Ls = lj * __builtin_amdgcn_exp2f((mj - M) * L2E);
            Ls += dpp<ROR2>(Ls); Ls += dpp<ROR4>(Ls); Ls += dpp<ROR8>(Ls);
            const float inv = __builtin_amdgcn_rcpf(__uint_as_float(__builtin_amdgcn_readfirstlane(__float_as_uint(Ls))));
            unsigned p[8];
#pragma unroll
            for (int i = 0; i < 8; ++i) { const unsigned t = bfbits(__builtin_amdgcn_exp2f((s[i] - M) * L2E) * inv); p[i] = t | (t << 16); }
            *(LAS v4u*)(lds + L_PP + b * 2048 + lane * 32) = (v4u){p[0], p[1], p[2], p[3]};
            *(LAS v4u*)(lds + L_PP + b * 2048 + lane * 32 + 16) = (v4u){p[4], p[5], p[6], p[7]};
            post(lds, 16, b + 1, lane);
            STAMP(36 + b);
          }
        } else {
          unsigned at = t0 * 16u + CP0, plan = fin0;
          float prev = 0.f;
          int slope = 0;
          const unsigned half = (unsigned)__builtin_amdgcn_s_getreg(20 | (2 << 6) | (0 << 11));
#pragma unroll 1
          for (int c = 0; c < NCP; ++c) {
            at = __builtin_amdgcn_readfirstlane(at);
            until(at);
            const v4u e = *(volatile LAS v4u*)(lds + L_FL + 48);
            gu64* cp = (gu64*)(ws + WS_CP) + c * 256;
            if (lane == 0 && (mem & 3) == CPMEM) put(cp + (w >> 5) * 8 + (w & 7), tag, ((e.x + (unsigned)(((int)(e.y - e.x) + (int)(e.z - e.x) + (int)(e.w - e.x)) >> 2)) & ~1u) | half);
            unsigned look = at + (c ? CPW : CPW0);
            u64 x; bool ok;
#pragma unroll 1
            for (int tr = c ? RETRY : 0;; ++tr) {
              until(look);
              x = __hip_atomic_load(cp + lane, RLX_AGENT);
              ok = (unsigned)(x >> 32) == tag;
              if (tr == RETRY || __all(ok)) break;
              look += CPW;
            }
            const bool up = ok && (x & 1);
            const float sum = ok ? (float)(int)((unsigned)x - fin0) : 0.f;
            const float sa = wave_sum(sum), su = wave_sum(up ? sum : 0.f);
            const float na = (float)__builtin_popcountll(__builtin_amdgcn_ballot_w64(ok)), nu = (float)__builtin_popcountll(__builtin_amdgcn_ballot_w64(up));
            const float mean = sa * __builtin_amdgcn_rcpf(fmaxf(na, 1.f));
            const float hi = fmaxf(nu > 0.f ? su * __builtin_amdgcn_rcpf(nu) : mean, na > nu ? (sa - su) * __builtin_amdgcn_rcpf(na - nu) : mean);
            const float est = fmaxf(mean, hi - GAIN * (float)(int)(plan - at));
            const bool first = c < NFIRST;
            const unsigned before = plan;
            const float worst = wave_max(ok ? sum : -1e9f) - (float)DEADM;
            if (first || __builtin_amdgcn_readfirstlane(est - (float)(int)(plan - fin0) > (float)LAM))
              plan = fin0 + __builtin_amdgcn_readfirstlane((int)fminf(fmaxf(first ? est + (float)MARG0 : est + (c == 2 ? KAP2 : c == 3 ? KAP3 : KAP4) * fmaxf(est - prev, 0.f) + (float)MARG, -8000.f), 16000.f));
            prev = est;
            if (!first) plan += __builtin_amdgcn_readfirstlane((int)(KMAX * fminf(fmaxf(worst - (float)(int)(plan - fin0), 0.f), 4000.f)));
            if (first) {
              const v4u e2 = *(volatile LAS v4u*)(lds + L_FL + 48);
              const int own = min(min((int)(e2.x - plan), (int)(e2.y - plan)), min((int)(e2.z - plan), (int)(e2.w - plan)));
              const float left = (float)((int)(plan - look) + own - 2400) * (1.f / TAU) - (float)LEND;
              slope = (int)(-(float)own * __builtin_amdgcn_rcpf(fmaxf(left, 8.f)));
            }
            if (lane == 0) *(volatile LAS v2u*)(lds + L_FL + 40) = (v2u){(unsigned)slope, plan};
            at = (c == 3 ? before : plan) - (c == 0 ? CP1 : c == 1 ? CP2 : c == 2 ? CP3 : CP4);
            STAMPV(49 + c, plan);
          }
        }
      }
      waitn<4, 4>(lds, 0, PBASE + 28);
      const LAS float* pr = (const LAS float*)(lds + L_PART) + ot;
      LAS u16* odst = (LAS u16*)(lds + L_QO) + (2 + (lane >> 5)) * DH + wave * 32 + (lane & 31);
      float s0 = 0.f, s1 = 0.f;
#pragma unroll
      for (int r = 0; r < 8; ++r) s0 += pr[r * DH];
      unsigned oout = WS_O + ((h * 8 + mem) * DH + ot) * 16;
      unsigned oin = WS_O + ((hc * 8 + (lane >> 5) * 4) * DH + (mem & 1) * 96 + wave * 32 + (lane & 31)) * 16;
      asm volatile("" : "+v"(s0), "+v"(oout), "+v"(oin), "+v"(odst), "+v"(pr));
      wait1<2>(lds, 3, PBASE + 30);
      waitn<0, 4>(lds, 0, PBASE + 32);
      s1 = ((pr[8 * DH] + pr[9 * DH]) + (pr[10 * DH] + pr[11 * DH])) + ((pr[12 * DH] + pr[13 * DH]) + (pr[14 * DH] + pr[15 * DH]));
      put2(wsr, oout, tag, FIRST ? s1 : s0, FIRST ? s0 : s1);
      if (wave == 0) STAMP(39);
      __builtin_amdgcn_s_sleep(OWNS);
      until(peek(lds, 11) + T_O1G);
      float r0, r1;
      sweep2<4>(wsr, oin, DH * 16, tag, false, r0, r1);
      auto t = __builtin_amdgcn_permlane32_swap(__float_as_uint(r0), __float_as_uint(r1), false, false);
      *odst = (u16)bfbits(__uint_as_float(t[0]) + __uint_as_float(t[1]));
      if (wave == 0) STAMP(43);
      BAR();
      __builtin_amdgcn_s_waitcnt(0x0F70);
      return;
    }
  }
  __builtin_amdgcn_s_waitcnt(0x0F70);
  if (wave == 3) {
    const unsigned lds0 = (unsigned)(size_t)lds;
    const RowMap rm = row_map(lane);
    wait1<8>(lds, 18, 1);
    wait1<8>(lds, 0, PBASE + 26);
    warm_code(3);
    if (w < 160) xres = __uint_as_float((unsigned)((const GAS u16*)x_)[oi] << 16);
    asm volatile("" : "+v"(xres));
    wait1<2>(lds, 3, PBASE + 29);
    const unsigned due = ref_time(lds, CAPW) + WO0, tau = ffn ? TAU_WO : TAU_WO * 4 / 3;
    until(peek(lds, 15) + WO0);
    STAMPV(47, due);
    if (ffn) stage_rows((gptr)wd_ + ((fb * 64 + (mem & 1) * 32) * (D * 2) + (mem >> 1) * 320), lds0 + L_WD, 2, rm, due, tau);
    stage_rows((gptr)wo_ + ((hc * DH + (mem & 1) * 96) * (D * 2) + (mem >> 1) * 320), lds0 + L_WO, 6, rm, ffn ? due + 2 * TAU_WO : due, tau);
  }
  const int tt = wave - 3;
  const LAS char* ap = lds + L_QO + 768 + ((lane >> 2) & 1) * 384 + (lane >> 4) * 16;
  const LAS char* bp = lds + L_WO + ((lane >> 4) * 8 + ((lane >> 2) & 3)) * 320 + tt * 64 + (lane & 3) * 8;
  const LAS char* hp = lds + L_HB + ((lane >> 2) & 1) * 128 + (mem & 1) * 64 + (lane >> 4) * 16;
  unsigned ydst = WS_Y + (((((mem >> 1) * 5 + tt) * 4 + (lane >> 4)) * 64 + hc * 2 + (mem & 1)) * 16 + (lane & 15)) * 16;
  const bool last = wave == 3 && w < 160;
  asm volatile("s_waitcnt vmcnt(10)" : "+v"(bp), "+v"(hp), "+v"(ap), "+v"(ydst) :: "memory");
  if (wave == 3) post(lds, 17, 1, lane);
  wait1<0>(lds, 17, 1);
  v4u bf[6];
#pragma unroll
  for (int i = 0; i < 4; ++i) bf[i] = tr_frag(bp + (i >> 1) * 10240 + (i & 1) * 32);
  const f4 zero = {0.f, 0.f, 0.f, 0.f};
  const v4u hf = *(const LAS v4u*)hp;
  const f4 g0 = mfma(hf, tr_frag(bp + L_WD), zero), g1 = mfma(hf, tr_frag(bp + L_WD + 32), zero);
  float gx = w < 64 ? 0.f : lane < 32 ? g0.x : g1.x;
  asm volatile("s_waitcnt vmcnt(0)" : "+v"(ap), "+v"(ydst), "+v"(gx), "+v"(bf[0]), "+v"(bf[1]), "+v"(bf[2]), "+v"(bf[3]) :: "memory");
  if (wave == 3) STAMP(48);
  BAR();
  {
    v4u a[3];
#pragma unroll
    for (int ks = 0; ks < 3; ++ks) a[ks] = *(const LAS v4u*)(ap + ks * 64);
    bf[4] = tr_frag(bp + 20480); bf[5] = tr_frag(bp + 20480 + 32);
    SB();
    f4 y0 = zero, y1 = zero;
#pragma unroll
    for (int ks = 0; ks < 3; ++ks) { y0 = mfma(a[ks], bf[2 * ks], y0); y1 = mfma(a[ks], bf[2 * ks + 1], y1); }
    put2(wsr, ydst, tag, lane < 32 ? y0.x : y1.x, gx);
    if (wave == 4) STAMPG(55);
  }
  if (last) {
    unsigned ysrc = WS_Y + (w >> 1) * (64 * 16 * 16) + (w & 1) * 128 + (lane >> 3) * 256 + (lane & 7) * 16;
    GAS u16* op = (GAS u16*)out_ + oi;
    asm volatile("" : "+v"(ysrc), "+v"(op));
    __builtin_amdgcn_s_sleep(OWNY);
    until(peek(lds, 11) + T_YG);
    float s, gs;
    sweep2<8>(wsr, ysrc, 2048, tag, false, s, gs);
    s += dpp<ROR8>(s); gs += dpp<ROR8>(gs);
    s = rbf(xrow_sum(s)); gs = rbf(xrow_sum(gs));
    const float gate = rbf(gs * __builtin_amdgcn_rcpf(1.f + __expf(-gs)));
    if (lane < 8) *op = (u16)bfbits(xres + rbf(s * gate));
    if (w == 0 && lane == 0) __hip_atomic_store((GAS unsigned*)(ws + WS_EPOCH), tag, RLX_AGENT);
    STAMPG(56);
  }
  asm volatile(".Lfd_last:");
}

extern "C" void kernel_launch(void* const* d_in, const int* in_sizes, int n_in, void* d_out, int out_size, void* d_ws, size_t ws_size,
                              hipStream_t stream) {
  fused_decode<<<256, NTHREADS, 0, stream>>>((const u16*)d_in[0], (const u16*)d_in[1], (const u16*)d_in[2], (const u16*)d_in[3],
                                             (const u16*)d_in[4], (const u16*)d_in[6], (const u16*)d_in[7], (const u16*)d_in[8],
                                             (u16*)d_out, (char*)d_ws);
}
```

```cpp
#include <hip/hip_runtime.h>
#include <stdint.h>

typedef unsigned short u16;
typedef unsigned long long u64;
typedef unsigned v4u __attribute__((ext_vector_type(4)));
typedef unsigned v2u __attribute__((ext_vector_type(2)));
typedef float f4 __attribute__((ext_vector_type(4)));
typedef short s4 __attribute__((ext_vector_type(4)));
typedef __bf16 bf16x2 __attribute__((ext_vector_type(2)));
typedef __bf16 bf16x8 __attribute__((ext_vector_type(8)));
#define GAS __attribute__((address_space(1)))
#define LAS __attribute__((address_space(3)))
#define RLX_AGENT __ATOMIC_RELAXED, __HIP_MEMORY_SCOPE_AGENT
typedef const GAS char* gptr;
typedef GAS u64 gu64;
typedef __amdgpu_buffer_rsrc_t rsrc_t;

#define NP 8
#define PBASE 2
#define T_Q 2
#define T_ST 4
#define T_H 12
#define TAU 650
#define ORG 2600
#define TEST_PH 0
#define TEST_D 64
#define TOUCH 1
#define OM 1100
#define FIRST 1
#define WBASE 24
#define WSH 8
#define WL0 0
#define WMAX 10
#define NCP 5
#define CP0 8800
#define CPW0 4400
#define CPW 2400
#define CP1 28000
#define CP2 20400
#define CP3 13200
#define CP4 9000
#define LAM 100
#define WB0 24
#define WB2 24
#define WBN 3
#define KAP2 0.5f
#define KAP3 2.f
#define KAP4 0.3f
#define MARG0 80
#define MARG 100
#define NFIRST 2
#define LEND 15
#define KMAX 0.25f
#define DEADM 600
#define GAIN 0.015f
#define RETRY 2
#define CAPW 1000
#define OWNY 6
#define OWNS 9
#define MUL 23
#define FAST 100
#define T_O1G 3000
#define T_YG 4800
#define CPMEM 1
#define WO0 600
#define TAU_WO 270

constexpr int D = 640, H = 32, SK = 4096, DH = 192, F = 1536, NTHREADS = 512;
constexpr int STEP = 32 * 3072;
constexpr unsigned SPIN_CAP = 1u << 16;

constexpr int WS_EPOCH = 0;
constexpr int WS_CP = 4096;
constexpr int WS_ST = WS_CP + 8 * 256 * 8;
constexpr int WS_P = WS_ST + H * 2 * 16 * 8;
constexpr int WS_O = WS_P + H * 16 * 64 * 16;
constexpr int WS_Y = WS_O + H * 8 * DH * 16;
constexpr int WS_END = WS_Y + 80 * 64 * 16 * 16;
constexpr int L_WO = 0;
constexpr int L_WD = 30720;
constexpr int L_XX = L_WD + 10240;
constexpr int L_PART = L_XX + 5120;
constexpr int L_QO = L_PART + 12288;
constexpr int L_HB = L_QO + 1536;
constexpr int L_SC = L_HB + 256;
constexpr int L_PP = L_SC + 4096;
constexpr int L_FL = L_PP + 4096;
constexpr int L_DG = L_FL + 160;
constexpr int L_TOTAL = 84 * 1024;

__device__ __forceinline__ float rbf(float f) { return (float)(__bf16)f; }
__device__ __forceinline__ unsigned bfbits(float f) { return (unsigned)__builtin_bit_cast(u16, (__bf16)f); }
__device__ __forceinline__ float dot2(unsigned a, unsigned b, float acc) {
  return __builtin_amdgcn_fdot2_f32_bf16(__builtin_bit_cast(bf16x2, a), __builtin_bit_cast(bf16x2, b), acc, false);
}
__device__ __forceinline__ float dot8(v4u a, v4u b, float acc) {
  return dot2(a.w, b.w, dot2(a.z, b.z, dot2(a.y, b.y, dot2(a.x, b.x, acc))));
}
__device__ __forceinline__ f4 mfma(v4u a, v4u b, f4 c) {
  return __builtin_amdgcn_mfma_f32_16x16x32_bf16(__builtin_bit_cast(bf16x8, a), __builtin_bit_cast(bf16x8, b), c, 0, 0, 0);
}
template <int CTRL> __device__ __forceinline__ float dpp(float x) {
  return __builtin_bit_cast(float, __builtin_amdgcn_mov_dpp(__builtin_bit_cast(int, x), CTRL, 0xf, 0xf, true));
}
constexpr int XOR1 = 0xB1, XOR2 = 0x4E, HMIR = 0x141, ROR2 = 0x122, ROR4 = 0x124, ROR8 = 0x128;
__device__ __forceinline__ float xrow_sum(float x) {
  auto s = __builtin_amdgcn_permlane16_swap(__float_as_uint(x), __float_as_uint(x), false, false);
  x = __uint_as_float(s[0]) + __uint_as_float(s[1]);
  auto t = __builtin_amdgcn_permlane32_swap(__float_as_uint(x), __float_as_uint(x), false, false);
  return __uint_as_float(t[0]) + __uint_as_float(t[1]);
}
__device__ __forceinline__ float xrow_max(float x) {
  auto s = __builtin_amdgcn_permlane16_swap(__float_as_uint(x), __float_as_uint(x), false, false);
  x = fmaxf(__uint_as_float(s[0]), __uint_as_float(s[1]));
  auto t = __builtin_amdgcn_permlane32_swap(__float_as_uint(x), __float_as_uint(x), false, false);
  return fmaxf(__uint_as_float(t[0]), __uint_as_float(t[1]));
}
__device__ __forceinline__ float wave_sum(float x) {
  x += dpp<XOR1>(x); x += dpp<XOR2>(x); x += dpp<HMIR>(x); x += dpp<ROR8>(x);
  return xrow_sum(x);
}
__device__ __forceinline__ float wave_max(float x) {
  x = fmaxf(x, dpp<XOR1>(x)); x = fmaxf(x, dpp<XOR2>(x)); x = fmaxf(x, dpp<HMIR>(x)); x = fmaxf(x, dpp<ROR8>(x));
  return xrow_max(x);
}
__device__ __forceinline__ float fold(float a, float b) {
  auto t = __builtin_amdgcn_permlane32_swap(__float_as_uint(a), __float_as_uint(b), false, false);
  return __uint_as_float(t[0]) + dpp<ROR8>(__uint_as_float(t[1]));
}
__device__ __forceinline__ rsrc_t mk_rsrc(const void* p, int bytes) { return __builtin_amdgcn_make_buffer_rsrc((void*)p, 0, bytes, 0x00020000); }
__device__ __forceinline__ v4u ld_nt(rsrc_t r, unsigned voff, unsigned soff) { return __builtin_amdgcn_raw_buffer_load_b128(r, voff, soff, 2); }
#define SB() __builtin_amdgcn_sched_barrier(0)
#define BAR() asm volatile("s_waitcnt lgkmcnt(0)\n\ts_barrier" ::: "memory")
__device__ __forceinline__ void put(gu64* g, unsigned tag, unsigned v) { __hip_atomic_store(g, ((u64)tag << 32) | v, RLX_AGENT); }
__device__ __forceinline__ void put2(rsrc_t r, unsigned off, unsigned tag, float a, float b) {
  __builtin_amdgcn_raw_buffer_store_b128((v4u){__float_as_uint(a), tag, __float_as_uint(b), tag}, r, off, 0, 16);
}
template <int N, int SLEEP> __device__ __forceinline__ void sweep(gu64* g, int stride, unsigned tag, float (&v)[N]) {
#pragma unroll 1
  for (unsigned spins = 0; spins < SPIN_CAP; ++spins) {
    bool ok = true;
#pragma unroll
    for (int k = 0; k < N; ++k) {
      const u64 x = __hip_atomic_load(g + k * stride, RLX_AGENT);
      v[k] = __uint_as_float((unsigned)x);
      ok &= (unsigned)(x >> 32) == tag;
    }
    if (__all(ok)) return;
    if (SLEEP) __builtin_amdgcn_s_sleep(SLEEP);
  }
#pragma unroll
  for (int k = 0; k < N; ++k) v[k] = __builtin_nanf("");
}
template <int N> __device__ __forceinline__ void sweep2(rsrc_t r, unsigned off, int stride, unsigned tag, bool lazy, float& a, float& b) {
#pragma unroll 1
  for (unsigned spins = 0; spins < SPIN_CAP; ++spins) {
    asm volatile("" ::: "memory");
    v4u x[N];
#pragma unroll
    for (int k = 0; k < N; ++k) x[k] = __builtin_amdgcn_raw_buffer_load_b128(r, off, k * stride, 16);
    unsigned lo = tag, hi = tag;
#pragma unroll
    for (int k = 0; k < N; ++k) {
      lo = min(min(lo, x[k].y), x[k].w); hi = max(max(hi, x[k].y), x[k].w);
      a = k ? a + __uint_as_float(x[k].x) : __uint_as_float(x[k].x);
      b = k ? b + __uint_as_float(x[k].z) : __uint_as_float(x[k].z);
      asm volatile("" : "+v"(a), "+v"(b), "+v"(lo), "+v"(hi));
    }
    if (__builtin_amdgcn_ballot_w64(lo != hi) == 0) return;
    if (lazy) __builtin_amdgcn_s_sleep(24);
  }
  a = b = __builtin_nanf("");
}
__device__ __forceinline__ void post(LAS char* lds, int word, unsigned v, int lane) {
  asm volatile("" ::: "memory");
  if (lane == 0) *(volatile LAS unsigned*)(lds + L_FL + word * 4) = v;
  asm volatile("" ::: "memory");
}
__device__ __forceinline__ unsigned peek(LAS char* lds, int word) {
  return __builtin_amdgcn_readfirstlane(*(volatile LAS unsigned*)(lds + L_FL + word * 4));
}
template <int SLEEP> __device__ __forceinline__ void wait1(LAS char* lds, int word, unsigned want) {
  while (peek(lds, word) < want) { if (SLEEP) __builtin_amdgcn_s_sleep(SLEEP); }
  asm volatile("" ::: "memory");
}
template <int SLEEP, int N> __device__ __forceinline__ void waitn(LAS char* lds, int word, unsigned want) {
  for (;;) {
    const v4u v = *(volatile LAS v4u*)(lds + L_FL + word * 4);
    unsigned m = min(v.x, v.y);
    if (N >= 3) m = min(m, v.z);
    if (N == 4) m = min(m, v.w);
    if (__builtin_amdgcn_readfirstlane(m) >= want) break;
    if (SLEEP) __builtin_amdgcn_s_sleep(SLEEP);
  }
  asm volatile("" ::: "memory");
}
__device__ __forceinline__ unsigned ref_time(LAS char* lds, int cap) {
  const unsigned plan = *(volatile LAS unsigned*)(lds + L_FL + 44), own = *(volatile LAS unsigned*)(lds + L_FL + 60);
  return __builtin_amdgcn_readfirstlane(plan + min(max((int)(own - plan), 0), cap));
}
__device__ __forceinline__ int until(unsigned due) {
  int late;
  asm volatile("s_branch 2f\n1:\n\ts_sleep 1\n2:\n\ts_memrealtime vcc\n\ts_waitcnt lgkmcnt(0)\n\ts_lshl_b32 %0, vcc_lo, 4\n\t"
               "s_sub_i32 %0, %0, %1\n\ts_cmp_lt_i32 %0, 0\n\ts_cbranch_scc1 1b" : "=&s"(late) : "s"(due) : "vcc", "scc", "memory");
  return late;
}
struct RowMap { unsigned o[5]; };
__device__ __forceinline__ RowMap row_map(int lane) {
  RowMap m;
#pragma unroll
  for (int i = 0; i < 5; ++i) { const unsigned ci = i * 64 + lane, row = (ci * 52429u) >> 20; m.o[i] = row * (D * 2 - 320) + ci * 16; }
  asm volatile("" : "+v"(m.o[0]), "+v"(m.o[1]), "+v"(m.o[2]), "+v"(m.o[3]), "+v"(m.o[4]));
  return m;
}
__device__ __forceinline__ void stage_rows(gptr src, unsigned lds_dst, int ng, const RowMap& m, unsigned due, unsigned tau) {
#pragma unroll 1
  for (int g = 0; g < ng; ++g) {
    unsigned keep;
    until(due); due += tau;
    asm volatile("s_mov_b32 %0, m0\n\ts_mov_b32 m0, %7\n\ts_nop 0\n\tglobal_load_lds_dwordx4 %1, %6\n\t"
                 "s_add_u32 m0, m0, 0x400\n\ts_nop 0\n\tglobal_load_lds_dwordx4 %2, %6\n\t"
                 "s_add_u32 m0, m0, 0x400\n\ts_nop 0\n\tglobal_load_lds_dwordx4 %3, %6\n\t"
                 "s_add_u32 m0, m0, 0x400\n\ts_nop 0\n\tglobal_load_lds_dwordx4 %4, %6\n\t"
                 "s_add_u32 m0, m0, 0x400\n\ts_nop 0\n\tglobal_load_lds_dwordx4 %5, %6\n\ts_mov_b32 m0, %0"
                 : "=&s"(keep) : "v"(m.o[0]), "v"(m.o[1]), "v"(m.o[2]), "v"(m.o[3]), "v"(m.o[4]), "s"(src), "s"(lds_dst) : "scc", "memory");
    src += 16 * D * 2; lds_dst += 5120;
  }
}
__device__ __forceinline__ v4u tr_frag(const LAS char* p) {
  const v2u b0 = __builtin_bit_cast(v2u, __builtin_amdgcn_ds_read_tr16_b64_v4i16((LAS s4*)p));
  const v2u b1 = __builtin_bit_cast(v2u, __builtin_amdgcn_ds_read_tr16_b64_v4i16((LAS s4*)(p + 1280)));
  return (v4u){b0.x, b0.y, b1.x, b1.y};
}

#define STAMP(i)
#define STAMPV(i, v)
#define ULOG(i)
#define STAMPG(i)

extern "C" __global__ void __launch_bounds__(512, 2)
fused_decode(const u16* x_, const u16* kc_, const u16* vc_, const u16* wq_, const u16* wo_, const u16* wg_, const u16* wu_,
             const u16* wd_, u16* out_, char* ws_) {
  unsigned ka0, ka1;
  asm volatile("s_load_dword %0, %2, 0x0\n\ts_load_dword %1, %2, 0x40" : "=&s"(ka0), "=&s"(ka1) : "s"(__builtin_amdgcn_kernarg_segment_ptr()));
  SB();
  __shared__ __attribute__((aligned(16))) char lds_raw[L_TOTAL];
  LAS char* lds = (LAS char*)lds_raw;
  GAS char* ws = (GAS char*)ws_;
  const int tid = threadIdx.x, lane = tid & 63, wave = __builtin_amdgcn_readfirstlane(tid >> 6);
  const int w = blockIdx.x, h = (w & 7) * 4 + (w >> 6), mem = (w >> 3) & 7;
  const int ww = wave - 4;
  const int hc = (h + 4) & 31;
  u64 code0;
  {
    u64 pc; unsigned back;
    asm volatile("s_getpc_b64 %0\n.Lfd_here:\n\ts_mov_b32 %1, .Lfd_here-fused_decode" : "=s"(pc), "=s"(back));
    code0 = pc - back;
  }
  auto warm_code = [&](int who) {
    u64 ex; unsigned keep;
    const unsigned c0 = (unsigned)(tid - who * 64) * 4096u + (w >> 3) * 128u;
    asm volatile("v_cmp_gt_u32 vcc, .Lfd_last-fused_decode, %2\n\ts_and_saveexec_b64 %0, vcc\n\ts_cbranch_execz 1f\n\t"
                 "s_mov_b32 %1, m0\n\ts_mov_b32 m0, %4\n\ts_nop 0\n\tglobal_load_lds_dword %2, %3\n\ts_mov_b32 m0, %1\n"
                 "1:\n\ts_mov_b64 exec, %0"
                 : "=&s"(ex), "=&s"(keep) : "v"(c0), "s"(code0), "s"((unsigned)(size_t)lds + L_FL + 128) : "vcc", "scc", "memory");
  };
  warm_code(0);
  u64 t64 = __builtin_amdgcn_s_memrealtime();
  const unsigned t0 = (unsigned)t64;
  const bool ffn = w >= 64, isq = mem < 6;
  const int fb = (w & 7) * 3 + (w >> 6) - 1;
  const int row0 = (mem & 1) * 320 + (lane >> 3), qm = (mem - 6) >> 31;
  unsigned rs8 = isq ? 8 * H * DH * 2 : 8 * F * 2;
  int jobw = wave + ((w >> 4) == 3 ? 4 : 0);
  unsigned woff = (row0 + wave * 80) * (rs8 >> 3) + (((h * 3 + (mem >> 1)) & qm) | (fb & ~qm)) * 128 + (lane & 7) * 16;
  unsigned xoff = (wave & 1) * (D * 2) + lane * 4;
  asm volatile("" : "+s"(rs8), "+s"(jobw));
  asm volatile("" : "+v"(woff), "+v"(xoff));
  SB();
  const bool job = jobw < 4;
  unsigned xr[5];
  v4u R[10];
  if (job) {
#pragma unroll
    for (int i = 0; i < 5; ++i) xr[i] = *(const GAS unsigned*)((gptr)x_ + xoff + 256 * i);
    SB();
    const rsrc_t wr = mk_rsrc(isq ? wq_ : wg_, 80 * rs8);
    unsigned so = 0;
#pragma unroll
    for (int i = 0; i < 10; ++i) { R[i] = ld_nt(wr, woff, so); so += rs8; SB(); }
  }
  if (TOUCH && (w >> 3) == 7 && tid < 16) {
    const unsigned g = lane >> 2, hg = (w & 7) * 4 + g, e = lane & 1;
    const unsigned okv = (((lane >> 1) & 1) * H + hg) * (SK * DH * 2) + e * (SK * DH * 2 - 4), owo = ((hg + 4) & 31) * (DH * D * 2) + e * (DH * D * 2 - 4);
    const unsigned off = lane * (D * F * 2 / 16), ows = (e ? WS_END : 2048) + (w & 7) * 128, oo = (w & 7) * 128;
    unsigned keep;
    asm volatile("s_mov_b32 %0, m0\n\ts_mov_b32 m0, %13\n\ts_nop 0\n\tglobal_load_lds_dword %1, %6 nt\n\tglobal_load_lds_dword %3, %9 nt\n\t"
                 "global_load_lds_dword %4, %11 nt\n\tglobal_load_lds_dword %1, %7 nt\n\tglobal_load_lds_dword %2, %8 nt\n\t"
                 "global_load_lds_dword %3, %10 nt\n\tglobal_load_lds_dword %5, %12 nt\n\ts_mov_b32 m0, %0"
                 : "=&s"(keep) : "v"(okv), "v"(owo), "v"(off), "v"(ows), "v"(oo), "s"(kc_), "s"(vc_), "s"(wo_), "s"(wu_), "s"(wd_), "s"(ws_), "s"(out_),
                   "s"((unsigned)(size_t)lds + L_PP) : "memory");
  }
  STAMP(wave);
  const rsrc_t wsr = mk_rsrc(ws_, WS_END);
  unsigned fin0 = t0 * 16u + (ORG + 63 * TAU - 16 * TEST_D * __builtin_popcount(TEST_PH));
  unsigned tag;
  v4u msk;
  const int oi = ((w >> 1) & 1) * D + (w >> 2) * 16 + (w & 1) * 8 + (lane & 7);
  float xres = 0.f;
  auto meet = [&]() {
    tag = *(const __attribute__((address_space(4))) unsigned*)(ws_ + WS_EPOCH) + 1u;
    const unsigned mm = (lane & 1) ? 0xffff0000u : 0x0000ffffu;
    const int q = (lane & 7) >> 1;
    msk = (v4u){q == 0 ? mm : 0u, q == 1 ? mm : 0u, q == 2 ? mm : 0u, q == 3 ? mm : 0u};
    asm volatile("" : "+s"(tag) : "s"(t64), "s"(ka0), "s"(ka1));
    asm volatile("" : "+v"(msk));
    BAR();
    fin0 += max((int)(((unsigned)__builtin_amdgcn_s_memrealtime() - t0) * 16u) + OM - ORG, 0);
  };

  if (wave >= 4) {
    __builtin_amdgcn_s_waitcnt(0x0F70);
    if (tid < 280) *(volatile LAS unsigned*)(lds + L_FL + lane * 4) = (lane & 15) < 11 ? 0u : fin0;
    meet();
    if (ww == 0) { post(lds, 11, fin0, lane); STAMP(5); }
    const int rot = (h & 3) * 4;
    const unsigned loff = (mem * 4 + ww) * 3072 + (lane >> 3) * 384 + (lane & 7) * 16;
    constexpr long HSTR = (long)SK * DH * 2, BSTR = FIRST ? -H * HSTR : H * HSTR;
    const char* const kb0 = (const char*)kc_ + (FIRST * H + h) * HSTR;
    const char* const vb0 = (const char*)vc_ + (FIRST * H + h) * HSTR;
    unsigned due = fin0 - 63 * TAU + 16 * TEST_D * __builtin_popcount(TEST_PH) + ww * (TAU / 4), fin = fin0;
    int adj = 0, late = 0, win, dith = ww, wb = WB0 - 4;
    auto pace = [&]() {
      late = until(due); due += TAU + adj - (((TEST_PH >> ((dith - ww) >> 4)) & 1) ? TEST_D : 0); fin += adj;
      win = min((wb + (max(late - WL0, 0) >> WSH) + (dith & 3)) >> 2, WMAX - 1); ++dith;
      SB();
    };
    auto report = [&](unsigned prog) {
      asm volatile("" ::: "memory");
      if (lane == 0) { *(volatile LAS unsigned*)(lds + L_FL + ww * 4) = prog; *(volatile LAS unsigned*)(lds + L_FL + 48 + ww * 4) = fin + late; }
      asm volatile("" ::: "memory");
    };
    auto steer = [&](int left, int mul) {
      const v2u pl = *(volatile LAS v2u*)(lds + L_FL + 40);
      const int slope = left > LEND ? __builtin_amdgcn_readfirstlane(pl.x) : 0;
      const int gap = (int)(__builtin_amdgcn_readfirstlane(pl.y) - fin) - slope * (left - LEND);
      adj = min(max(slope + ((gap * mul) >> 8), -FAST), TAU / 2);
    };
    auto fetch = [&](const char* base, int s, int j) {
      unsigned cnt;
      asm volatile("1:\n\ts_getreg_b32 %0, hwreg(HW_REG_IB_STS, 0, 4)\n\ts_cmp_gt_u32 %0, %1\n\ts_cbranch_scc1 1b" : "=&s"(cnt) : "s"(win) : "scc", "memory");
      const v4u r = ld_nt(mk_rsrc(base, (int)HSTR), loff + j * 128, ((s + rot) & 15) * STEP);
      SB();
      return r;
    };
    v4u U[NP + 4][3];
#pragma unroll
    for (int u = 0; u < NP + 4; ++u) {
      pace();
#pragma unroll
      for (int j = 0; j < 3; ++j) U[u][j] = fetch(kb0, u, j);
      if (u & 1) report(u >> 1);
      if (u >= 4) ULOG(u - 4);
    }
    waitn<0, 3>(lds, 20, 1);
    if (ww == 0) STAMP(12);
#pragma unroll 1
    for (int b = 0; b < 2; ++b) {
      v4u q[3];
#pragma unroll
      for (int j = 0; j < 3; ++j) q[j] = *(const LAS v4u*)(lds + L_QO + (b ^ FIRST) * 384 + j * 128 + (lane & 7) * 16);
      const char* const cur = kb0 + b * BSTR;
      const char* const nxt = b ? vb0 : kb0 + BSTR;
      LAS float* sc = (LAS float*)(lds + L_SC) + b * 512 + ww * 8 + (lane >> 3);
#pragma unroll 1
      for (int it = b ? 0 : NP / 4; it < 4; ++it) {
        const char* const base = it == 3 ? nxt : cur;
        if (it == 3) wb = (b ? WB2 : WBASE) - 4;
        steer(59 - b * 16 - it * 4, MUL);
        float pick = 0.f;
#pragma unroll
        for (int k = 0; k < 4; ++k) {
          if (!(k & 1)) report(PBASE + b * 8 + it * 2 + (k >> 1));
          pace();
          float d = 0.f;
#pragma unroll
          for (int j = 0; j < 3; ++j) { d = dot8(U[NP + k][j], q[j], d); asm volatile("" : "+v"(d)); SB(); U[NP + k][j] = fetch(base, 4 * it + 4 + k, j); }
          d += dpp<XOR1>(d); d += dpp<XOR2>(d); d += dpp<HMIR>(d);
          pick = (lane & 7) == k ? d : pick;
          if (b == 0) {
            float e = dot8(U[k][2], q[2], dot8(U[k][1], q[1], dot8(U[k][0], q[0], 0.f)));
            e += dpp<XOR1>(e); e += dpp<XOR2>(e); e += dpp<HMIR>(e);
            pick = (lane & 7) == 4 + k ? e : pick;
#pragma unroll
            for (int j = 0; j < 3; ++j) U[k][j] = U[4 + k][j];
          }
          asm volatile("" : "+v"(pick));
          SB();
          ULOG(b * 16 + it * 4 + k);
        }
        if (b == 0 || !(lane & 4)) sc[it * 128 + (lane & 3) * 32 - (lane & 4) * 64] = rbf(rbf(pick) * 0.07216878364870322f);
        SB();
      }
      STAMP(16 + b * 4 + ww);
    }
    f4 o[3];
    const bool useful = ((lane >> 3) & 1) == (lane >> 5);
    LAS char* part = lds + L_PART + ((ww * 2 + (lane >> 5)) * DH + (lane & 7) * 8 + ((lane >> 4) & 1) * 4) * 4;
    const LAS unsigned* pp = (const LAS unsigned*)(lds + L_PP) + ww * 8 + (lane >> 3);
    auto vstep = [&](int n, const char* base, bool more) {
      unsigned pv[4];
#pragma unroll
      for (int k = 0; k < 4; ++k) pv[k] = pp[n * 128 + k * 32];
#pragma unroll
      for (int k = 0; k < 4; ++k) {
        if (k == 2 && more) report(PBASE + 17 + 2 * n);
        const v4u a = msk & pv[k];
        SB();
        if (more) pace();
#pragma unroll
        for (int j = 0; j < 3; ++j) { o[j] = mfma(a, U[NP + k][j], o[j]); SB(); if (more) U[NP + k][j] = fetch(base, 4 * n + 4 + k, j); }
        ULOG(32 + 4 * n + k);
      }
    };
    auto vflush = [&](int b) {
      if (useful) {
#pragma unroll
        for (int j = 0; j < 3; ++j) *(LAS f4*)(part + b * (8 * DH * 4) + j * 256) = o[j];
      }
    };
#pragma unroll 1
    for (int n = 0; n < 7; ++n) {
      report(PBASE + 16 + 2 * n);
      if (n == WBN) wb = WBASE - 4;
      steer(27 - 4 * n, n < 5 ? MUL : n == 5 ? 36 : 85);
      if ((n & 3) == 0) {
        wait1<0>(lds, 16, 1 + (n >> 2));
#pragma unroll
        for (int j = 0; j < 3; ++j) o[j] = (f4){0.f, 0.f, 0.f, 0.f};
        STAMP(24 + (n >> 2) * 4 + ww);
      }
      vstep(n, n >= 3 ? vb0 + BSTR : vb0, true);
      if (n == 3) vflush(0);
    }
    fin -= adj;
    report(PBASE + 30);
    vstep(7, vb0, false);
    vflush(1);
    post(lds, ww, PBASE + 32, lane);
    STAMP(32 + ww);
  } else {
    meet();
    if (job) {
      float v[10], sum = 0.f;
#pragma unroll
      for (int i = 0; i < 5; ++i) { v[2 * i] = __uint_as_float(xr[i] << 16); v[2 * i + 1] = __uint_as_float(xr[i] & 0xffff0000u); sum += v[2 * i] + v[2 * i + 1]; }
      const float mean = wave_sum(sum) * (1.f / D);
      float q = 0.f;
#pragma unroll
      for (int i = 0; i < 10; ++i) { const float dd = v[i] - mean; q += dd * dd; }
      const float var = rbf(wave_sum(q) * (1.f / D)), mb = rbf(mean);
      const float inv = __builtin_amdgcn_rcpf(rbf(__builtin_amdgcn_sqrtf(rbf(var + 1e-5f))));
#pragma unroll
      for (int i = 0; i < 10; ++i) {
        const unsigned t = bfbits(rbf(v[i] - mb) * inv);
        *(LAS unsigned*)(lds + L_XX + ((lane + 64 * (i >> 1)) * 2 + (i & 1)) * 8 + (wave & 1) * 4) = t | (t << 16);
      }
      post(lds, 8 + (wave & 1), 1, lane);
      waitn<0, 2>(lds, 8, 1);
      if (wave == 0) STAMP(8);
#pragma unroll 1
      for (int pass = 0;; ++pass) {
        f4 a0 = {0.f, 0.f, 0.f, 0.f}, a1 = a0;
        const LAS char* xp = lds + L_XX + (pass ? mem * 80 + (lane >> 3) : row0 + wave * 80) * 8;
#pragma unroll
        for (int i = 0; i < 10; ++i) {
          const v2u xv = *(const LAS v2u*)(xp + i * 64);
          a0 = mfma(msk & xv.x, R[i], a0); a1 = mfma(msk & xv.y, R[i], a1);
        }
        const bool more = wave == 3 && ffn && pass == 0;
        if (more) {
          const rsrc_t wr = mk_rsrc(wu_, D * F * 2);
          const unsigned uoff = (mem * 80 + (lane >> 3)) * (F * 2) + fb * 128 + (lane & 7) * 16;
#pragma unroll
          for (int i = 0; i < 10; ++i) { R[i] = ld_nt(wr, uoff, i * (8 * F * 2)); SB(); }
        }
        const f4 s = {fold(a0[0], a1[0]), fold(a0[1], a1[1]), fold(a0[2], a1[2]), fold(a0[3], a1[3])};
        if ((lane & 8) == 0) *(LAS f4*)(lds + L_WO + (wave * 2 + (lane >> 5)) * 256 + (lane & 7) * 32 + ((lane >> 4) & 1) * 16) = s;
        post(lds, 4 + wave, 1, lane);
        if (wave < 3) break;
        waitn<1, 4>(lds, 4, 1);
        const LAS float* pr = (const LAS float*)(lds + L_WO) + lane;
        const float c0 = pr[384], c1 = pr[448];
        put2(wsr, WS_P + ((h * 16 + mem + 8 * pass) * 64 + lane) * 16, tag, pass ? c0 : (pr[0] + pr[128]) + (pr[256] + c0), pass ? c1 : (pr[64] + pr[192]) + (pr[320] + c1));
        STAMP(9 + pass);
        if (!more) break;
      }
    }
    if (wave < 3) {
      const int ot = wave * 64 + lane;
      {
        wait1<4>(lds, 0, T_Q);
        float r0, r1;
        sweep2<2>(wsr, WS_P + ((h * 16 + wave * 2) * 64 + lane) * 16, 1024, tag, true, r0, r1);
        LAS u16* dst = (LAS u16*)(lds + L_QO) + ot;
        dst[0] = (u16)bfbits(r0); dst[DH] = (u16)bfbits(r1);
        post(lds, 20 + wave, 1, lane);
        if (wave == 0) STAMP(40);
      }
      {
        if (wave == 2) {
          if (ffn) {
            wait1<8>(lds, 0, T_H);
            float gt[2], up[2];
            sweep2<2>(wsr, WS_P + ((h * 16 + 6) * 64 + lane) * 16, 1024, tag, true, gt[0], gt[1]);
            sweep2<8>(wsr, WS_P + ((h * 16 + 8) * 64 + lane) * 16, 1024, tag, true, up[0], up[1]);
#pragma unroll
            for (int b = 0; b < 2; ++b) {
              const float g = rbf(gt[b]), t2 = 2.f * 0.7978845608028654f * (g + 0.044715f * g * g * g);
              *(LAS u16*)(lds + L_HB + b * 128 + lane * 2) = (u16)bfbits(rbf(g * __builtin_amdgcn_rcpf(1.f + __expf(-t2))) * rbf(up[b]));
            }
            STAMP(45);
          }
          post(lds, 18, 1, lane);
        } else
        if (wave == 0) {
          gu64* stbox = (gu64*)(ws + WS_ST) + h * 32;
#pragma unroll 1
          for (int b = 0; b < 2; ++b) {
            waitn<4, 4>(lds, 0, PBASE + b * 8 + 8);
            const f4 s0 = *(const LAS f4*)(lds + L_SC + b * 2048 + lane * 32), s1 = *(const LAS f4*)(lds + L_SC + b * 2048 + lane * 32 + 16);
            const float s[8] = {s0.x, s0.y, s0.z, s0.w, s1.x, s1.y, s1.z, s1.w};
            constexpr float L2E = 1.4426950408889634f;
            float m = s[0];
#pragma unroll
            for (int i = 1; i < 8; ++i) m = fmaxf(m, s[i]);
            m = wave_max(m);
            const float ms = m == -INFINITY ? 0.f : m;
            float l = 0.f;
#pragma unroll
            for (int i = 0; i < 8; ++i) l += __builtin_amdgcn_exp2f((s[i] - ms) * L2E);
            l = wave_sum(l);
            if (lane < 2) put(stbox + b * 16 + mem * 2 + lane, tag, __float_as_uint(lane ? l : m));
            wait1<4>(lds, 0, PBASE + b * 8 + 8 + T_ST);
            float st1[1];
            sweep<1, 16>(stbox + b * 16 + (lane & 15), 0, tag, st1);
            const float oth = dpp<XOR1>(st1[0]), mj = (lane & 1) ? oth : st1[0], lj = (lane & 1) ? st1[0] : oth;
            float M = fmaxf(mj, dpp<ROR2>(mj)); M = fmaxf(M, dpp<ROR4>(M)); M = fmaxf(M, dpp<ROR8>(M));
            float Ls = lj * __builtin_amdgcn_exp2f((mj - M) * L2E);
            Ls += dpp<ROR2>(Ls); Ls += dpp<ROR4>(Ls); Ls += dpp<ROR8>(Ls);
            const float inv = __builtin_amdgcn_rcpf(__uint_as_float(__builtin_amdgcn_readfirstlane(__float_as_uint(Ls))));
            unsigned p[8];
#pragma unroll
            for (int i = 0; i < 8; ++i) { const unsigned t = bfbits(__builtin_amdgcn_exp2f((s[i] - M) * L2E) * inv); p[i] = t | (t << 16); }
            *(LAS v4u*)(lds + L_PP + b * 2048 + lane * 32) = (v4u){p[0], p[1], p[2], p[3]};
            *(LAS v4u*)(lds + L_PP + b * 2048 + lane * 32 + 16) = (v4u){p[4], p[5], p[6], p[7]};
            post(lds, 16, b + 1, lane);
            STAMP(36 + b);
          }
        } else {
          unsigned at = t0 * 16u + CP0, plan = fin0;
          float prev = 0.f;
          int slope = 0;
          const unsigned half = (unsigned)__builtin_amdgcn_s_getreg(20 | (2 << 6) | (0 << 11));
#pragma unroll 1
          for (int c = 0; c < NCP; ++c) {
            at = __builtin_amdgcn_readfirstlane(at);
            until(at);
            const v4u e = *(volatile LAS v4u*)(lds + L_FL + 48);
            gu64* cp = (gu64*)(ws + WS_CP) + c * 256;
            if (lane == 0 && (mem & 3) == CPMEM) put(cp + (w >> 5) * 8 + (w & 7), tag, ((e.x + (unsigned)(((int)(e.y - e.x) + (int)(e.z - e.x) + (int)(e.w - e.x)) >> 2)) & ~1u) | half);
            unsigned look = at + (c ? CPW : CPW0);
            u64 x; bool ok;
#pragma unroll 1
            for (int tr = c ? RETRY : 0;; ++tr) {
              until(look);
              x = __hip_atomic_load(cp + lane, RLX_AGENT);
              ok = (unsigned)(x >> 32) == tag;
              if (tr == RETRY || __all(ok)) break;
              look += CPW;
            }
            const bool up = ok && (x & 1);
            const float sum = ok ? (float)(int)((unsigned)x - fin0) : 0.f;
            const float sa = wave_sum(sum), su = wave_sum(up ? sum : 0.f);
            const float na = (float)__builtin_popcountll(__builtin_amdgcn_ballot_w64(ok)), nu = (float)__builtin_popcountll(__builtin_amdgcn_ballot_w64(up));
            const float mean = sa * __builtin_amdgcn_rcpf(fmaxf(na, 1.f));
            const float hi = fmaxf(nu > 0.f ? su * __builtin_amdgcn_rcpf(nu) : mean, na > nu ? (sa - su) * __builtin_amdgcn_rcpf(na - nu) : mean);
            const float est = fmaxf(mean, hi - GAIN * (float)(int)(plan - at));
            const bool first = c < NFIRST;
            const unsigned before = plan;
            const float worst = wave_max(ok ? sum : -1e9f) - (float)DEADM;
            if (first || __builtin_amdgcn_readfirstlane(est - (float)(int)(plan - fin0) > (float)LAM))
              plan = fin0 + __builtin_amdgcn_readfirstlane((int)fminf(fmaxf(first ? est + (float)MARG0 : est + (c == 2 ? KAP2 : c == 3 ? KAP3 : KAP4) * fmaxf(est - prev, 0.f) + (float)MARG, -8000.f), 16000.f));
            prev = est;
            if (!first) plan += __builtin_amdgcn_readfirstlane((int)(KMAX * fminf(fmaxf(worst - (float)(int)(plan - fin0), 0.f), 4000.f)));
            if (first) {
              const v4u e2 = *(volatile LAS v4u*)(lds + L_FL + 48);
              const int own = min(min((int)(e2.x - plan), (int)(e2.y - plan)), min((int)(e2.z - plan), (int)(e2.w - plan)));
              const float left = (float)((int)(plan - look) + own - 2400) * (1.f / TAU) - (float)LEND;
              slope = (int)(-(float)own * __builtin_amdgcn_rcpf(fmaxf(left, 8.f)));
            }
            if (lane == 0) *(volatile LAS v2u*)(lds + L_FL + 40) = (v2u){(unsigned)slope, plan};
            at = (c == 3 ? before : plan) - (c == 0 ? CP1 : c == 1 ? CP2 : c == 2 ? CP3 : CP4);
            STAMPV(49 + c, plan);
          }
        }
      }
      waitn<4, 4>(lds, 0, PBASE + 28);
      const LAS float* pr = (const LAS float*)(lds + L_PART) + ot;
      LAS u16* odst = (LAS u16*)(lds + L_QO) + (2 + (lane >> 5)) * DH + wave * 32 + (lane & 31);
      float s0 = 0.f, s1 = 0.f;
#pragma unroll
      for (int r = 0; r < 8; ++r) s0 += pr[r * DH];
      unsigned oout = WS_O + ((h * 8 + mem) * DH + ot) * 16;
      unsigned oin = WS_O + ((hc * 8 + (lane >> 5) * 4) * DH + (mem & 1) * 96 + wave * 32 + (lane & 31)) * 16;
      asm volatile("" : "+v"(s0), "+v"(oout), "+v"(oin), "+v"(odst), "+v"(pr));
      wait1<2>(lds, 3, PBASE + 30);
      waitn<0, 4>(lds, 0, PBASE + 32);
      s1 = ((pr[8 * DH] + pr[9 * DH]) + (pr[10 * DH] + pr[11 * DH])) + ((pr[12 * DH] + pr[13 * DH]) + (pr[14 * DH] + pr[15 * DH]));
      put2(wsr, oout, tag, FIRST ? s1 : s0, FIRST ? s0 : s1);
      if (wave == 0) STAMP(39);
      __builtin_amdgcn_s_sleep(OWNS);
      until(peek(lds, 11) + T_O1G);
      float r0, r1;
      sweep2<4>(wsr, oin, DH * 16, tag, false, r0, r1);
      auto t = __builtin_amdgcn_permlane32_swap(__float_as_uint(r0), __float_as_uint(r1), false, false);
      *odst = (u16)bfbits(__uint_as_float(t[0]) + __uint_as_float(t[1]));
      if (wave == 0) STAMP(43);
      BAR();
      __builtin_amdgcn_s_waitcnt(0x0F70);
      return;
    }
  }
  __builtin_amdgcn_s_waitcnt(0x0F70);
  if (wave == 3) {
    const unsigned lds0 = (unsigned)(size_t)lds;
    const RowMap rm = row_map(lane);
    wait1<8>(lds, 18, 1);
    wait1<8>(lds, 0, PBASE + 26);
    warm_code(3);
    if (w < 160) xres = __uint_as_float((unsigned)((const GAS u16*)x_)[oi] << 16);
    asm volatile("" : "+v"(xres));
    wait1<2>(lds, 3, PBASE + 29);
    const unsigned due = ref_time(lds, CAPW) + WO0, tau = ffn ? TAU_WO : TAU_WO * 4 / 3;
    until(peek(lds, 15) + WO0);
    STAMPV(47, due);
    if (ffn) stage_rows((gptr)wd_ + ((fb * 64 + (mem & 1) * 32) * (D * 2) + (mem >> 1) * 320), lds0 + L_WD, 2, rm, due, tau);
    stage_rows((gptr)wo_ + ((hc * DH + (mem & 1) * 96) * (D * 2) + (mem >> 1) * 320), lds0 + L_WO, 6, rm, ffn ? due + 2 * TAU_WO : due, tau);
  }
  const int tt = wave - 3;
  const LAS char* ap = lds + L_QO + 768 + ((lane >> 2) & 1) * 384 + (lane >> 4) * 16;
  const LAS char* bp = lds + L_WO + ((lane >> 4) * 8 + ((lane >> 2) & 3)) * 320 + tt * 64 + (lane & 3) * 8;
  const LAS char* hp = lds + L_HB + ((lane >> 2) & 1) * 128 + (mem & 1) * 64 + (lane >> 4) * 16;
  unsigned ydst = WS_Y + (((((mem >> 1) * 5 + tt) * 4 + (lane >> 4)) * 64 + hc * 2 + (mem & 1)) * 16 + (lane & 15)) * 16;
  const bool last = wave == 3 && w < 160;
  asm volatile("s_waitcnt vmcnt(10)" : "+v"(bp), "+v"(hp), "+v"(ap), "+v"(ydst) :: "memory");
  if (wave == 3) post(lds, 17, 1, lane);
  wait1<0>(lds, 17, 1);
  v4u bf[6];
#pragma unroll
  for (int i = 0; i < 4; ++i) bf[i] = tr_frag(bp + (i >> 1) * 10240 + (i & 1) * 32);
  const f4 zero = {0.f, 0.f, 0.f, 0.f};
  const v4u hf = *(const LAS v4u*)hp;
  const f4 g0 = mfma(hf, tr_frag(bp + L_WD), zero), g1 = mfma(hf, tr_frag(bp + L_WD + 32), zero);
  float gx = w < 64 ? 0.f : lane < 32 ? g0.x : g1.x;
  asm volatile("s_waitcnt vmcnt(0)" : "+v"(ap), "+v"(ydst), "+v"(gx), "+v"(bf[0]), "+v"(bf[1]), "+v"(bf[2]), "+v"(bf[3]) :: "memory");
  if (wave == 3) STAMP(48);
  BAR();
  {
    v4u a[3];
#pragma unroll
    for (int ks = 0; ks < 3; ++ks) a[ks] = *(const LAS v4u*)(ap + ks * 64);
    bf[4] = tr_frag(bp + 20480); bf[5] = tr_frag(bp + 20480 + 32);
    SB();
    f4 y0 = zero, y1 = zero;
#pragma unroll
    for (int ks = 0; ks < 3; ++ks) { y0 = mfma(a[ks], bf[2 * ks], y0); y1 = mfma(a[ks], bf[2 * ks + 1], y1); }
    put2(wsr, ydst, tag, lane < 32 ? y0.x : y1.x, gx);
    if (wave == 4) STAMPG(55);
  }
  if (last) {
    unsigned ysrc = WS_Y + (w >> 1) * (64 * 16 * 16) + (w & 1) * 128 + (lane >> 3) * 256 + (lane & 7) * 16;
    GAS u16* op = (GAS u16*)out_ + oi;
    asm volatile("" : "+v"(ysrc), "+v"(op));
    __builtin_amdgcn_s_sleep(OWNY);
    until(peek(lds, 11) + T_YG);
    float s, gs;
    sweep2<8>(wsr, ysrc, 2048, tag, false, s, gs);
    s += dpp<ROR8>(s); gs += dpp<ROR8>(gs);
    s = rbf(xrow_sum(s)); gs = rbf(xrow_sum(gs));
    const float gate = rbf(gs * __builtin_amdgcn_rcpf(1.f + __expf(-gs)));
    if (lane < 8) *op = (u16)bfbits(xres + rbf(s * gate));
    if (w == 0 && lane == 0) __hip_atomic_store((GAS unsigned*)(ws + WS_EPOCH), tag, RLX_AGENT);
    STAMPG(56);
  }
  asm volatile(".Lfd_last:");
}

extern "C" void kernel_launch(void* const* d_in, const int* in_sizes, int n_in, void* d_out, int out_size, void* d_ws, size_t ws_size,
                              hipStream_t stream) {
  fused_decode<<<256, NTHREADS, 0, stream>>>((const u16*)d_in[0], (const u16*)d_in[1], (const u16*)d_in[2], (const u16*)d_in[3],
                                             (const u16*)d_in[4], (const u16*)d_in[6], (const u16*)d_in[7], (const u16*)d_in[8],
                                             (u16*)d_out, (char*)d_ws);
}
```

```cpp
#include <hip/hip_runtime.h>
#include <stdint.h>

typedef unsigned short u16;
typedef unsigned long long u64;
typedef unsigned v4u __attribute__((ext_vector_type(4)));
typedef unsigned v2u __attribute__((ext_vector_type(2)));
typedef float f4 __attribute__((ext_vector_type(4)));
typedef short s4 __attribute__((ext_vector_type(4)));
typedef __bf16 bf16x2 __attribute__((ext_vector_type(2)));
typedef __bf16 bf16x8 __attribute__((ext_vector_type(8)));
#define GAS __attribute__((address_space(1)))
#define LAS __attribute__((address_space(3)))
#define RLX_AGENT __ATOMIC_RELAXED, __HIP_MEMORY_SCOPE_AGENT
typedef const GAS char* gptr;
typedef GAS u64 gu64;
typedef __amdgpu_buffer_rsrc_t rsrc_t;

constexpr int D = 640, H = 32, SK = 4096, DH = 192, F = 1536, NTHREADS = 512;
constexpr int STEP = 32 * 3072;
constexpr int NP = 8;
constexpr unsigned SPIN_CAP = 1u << 16;

constexpr int P_K1 = 10, P_V1 = 26, P_OUT = 32, P_DONE = 34;
constexpr int T_Q = 2, T_ST = 4, T_H = 22;

constexpr int TAU = 650;
constexpr int ORG = 2300, OM = 1000;
constexpr int LEAD = 118;
constexpr int WBASE = 24, WSH = 8, WMAX = 10;
constexpr int NCP = 5, CP0 = 8800;
constexpr int CP1 = 28000, CP2 = 20400, CP3 = 13200, CP4 = 9000;
constexpr int CPW0 = 4400, CPW = 2400;
constexpr int LEND = 13, MUL = 23, FAST = 100, VSLOW = 58;
constexpr int WO0 = 700, TAU_WO = 340, DEC_WO = 25;
constexpr int T_OG = 3150, T_YG = 4960;

constexpr int WS_TAG = 0;
constexpr int WS_CP = 4096;
constexpr int WS_ST = WS_CP + 8 * 256 * 8;
constexpr int WS_P = WS_ST + H * 2 * 16 * 8;
constexpr int WS_O = WS_P + H * 16 * 64 * 16;
constexpr int WS_Y = WS_O + H * 8 * DH * 16;
constexpr int WS_END = WS_Y + 80 * 64 * 16 * 16;
constexpr int L_WO = 0;
constexpr int L_WD = 30720;
constexpr int L_XX = L_WD + 10240;
constexpr int L_PART = L_XX + 5120;
constexpr int L_QO = L_PART + 12288;
constexpr int L_HB = L_QO + 1536;
constexpr int L_SC = L_HB + 256;
constexpr int L_PP = L_SC + 4096;
constexpr int L_FL = L_PP + 4096;
constexpr int L_PJ = L_FL + 160;
constexpr int L_TOTAL = 84 * 1024;
constexpr int W_PROG = 0, W_PJ = 4, W_LN = 8, W_SLOPE = 10, W_PLAN = 11, W_EST = 12, W_P = 16, W_STAGED = 17, W_HID = 18, W_MOVED = 19,
              W_Q = 20, W_DUMP = 32;

__device__ __forceinline__ float rbf(float f) { return (float)(__bf16)f; }
__device__ __forceinline__ unsigned bfbits(float f) { return (unsigned)__builtin_bit_cast(u16, (__bf16)f); }
__device__ __forceinline__ float dot2(unsigned a, unsigned b, float acc) {
  return __builtin_amdgcn_fdot2_f32_bf16(__builtin_bit_cast(bf16x2, a), __builtin_bit_cast(bf16x2, b), acc, false);
}
__device__ __forceinline__ float dot8(v4u a, v4u b, float acc) {
  return dot2(a.w, b.w, dot2(a.z, b.z, dot2(a.y, b.y, dot2(a.x, b.x, acc))));
}
__device__ __forceinline__ f4 mfma(v4u a, v4u b, f4 c) {
  return __builtin_amdgcn_mfma_f32_16x16x32_bf16(__builtin_bit_cast(bf16x8, a), __builtin_bit_cast(bf16x8, b), c, 0, 0, 0);
}
template <int CTRL> __device__ __forceinline__ float dpp(float x) {
  return __builtin_bit_cast(float, __builtin_amdgcn_mov_dpp(__builtin_bit_cast(int, x), CTRL, 0xf, 0xf, true));
}
constexpr int XOR1 = 0xB1, XOR2 = 0x4E, HMIR = 0x141, ROR2 = 0x122, ROR4 = 0x124, ROR8 = 0x128;
__device__ __forceinline__ float sum8(float x) { x += dpp<XOR1>(x); x += dpp<XOR2>(x); return x + dpp<HMIR>(x); }
__device__ __forceinline__ float xrow_sum(float x) {
  auto s = __builtin_amdgcn_permlane16_swap(__float_as_uint(x), __float_as_uint(x), false, false);
  x = __uint_as_float(s[0]) + __uint_as_float(s[1]);
  auto t = __builtin_amdgcn_permlane32_swap(__float_as_uint(x), __float_as_uint(x), false, false);
  return __uint_as_float(t[0]) + __uint_as_float(t[1]);
}
__device__ __forceinline__ float wave_sum(float x) { x = sum8(x); return xrow_sum(x + dpp<ROR8>(x)); }
__device__ __forceinline__ float wave_max(float x) {
  x = fmaxf(x, dpp<XOR1>(x)); x = fmaxf(x, dpp<XOR2>(x)); x = fmaxf(x, dpp<HMIR>(x)); x = fmaxf(x, dpp<ROR8>(x));
  auto s = __builtin_amdgcn_permlane16_swap(__float_as_uint(x), __float_as_uint(x), false, false);
  x = fmaxf(__uint_as_float(s[0]), __uint_as_float(s[1]));
  auto t = __builtin_amdgcn_permlane32_swap(__float_as_uint(x), __float_as_uint(x), false, false);
  return fmaxf(__uint_as_float(t[0]), __uint_as_float(t[1]));
}
__device__ __forceinline__ float fold(float a, float b) {
  auto t = __builtin_amdgcn_permlane32_swap(__float_as_uint(a), __float_as_uint(b), false, false);
  return __uint_as_float(t[0]) + dpp<ROR8>(__uint_as_float(t[1]));
}
__device__ __forceinline__ rsrc_t mk_rsrc(const void* p, int bytes) { return __builtin_amdgcn_make_buffer_rsrc((void*)p, 0, bytes, 0x00020000); }
__device__ __forceinline__ v4u ld_nt(rsrc_t r, unsigned voff, unsigned soff) { return __builtin_amdgcn_raw_buffer_load_b128(r, voff, soff, 2); }
#define SB() __builtin_amdgcn_sched_barrier(0)
#define BAR() asm volatile("s_waitcnt lgkmcnt(0)\n\ts_barrier" ::: "memory")
#define NO_LOADS() __builtin_amdgcn_s_waitcnt(0x0F70)

__device__ __forceinline__ void put(gu64* g, unsigned tag, unsigned v) { __hip_atomic_store(g, ((u64)tag << 32) | v, RLX_AGENT); }
__device__ __forceinline__ void put2(rsrc_t r, unsigned off, unsigned tag, float a, float b) {
  __builtin_amdgcn_raw_buffer_store_b128((v4u){__float_as_uint(a), tag, __float_as_uint(b), tag}, r, off, 0, 16);
}
__device__ __forceinline__ float get(gu64* g, unsigned tag) {
#pragma unroll 1
  for (unsigned spins = 0; spins < SPIN_CAP; ++spins) {
    const u64 x = __hip_atomic_load(g, RLX_AGENT);
    if (__all((unsigned)(x >> 32) == tag)) return __uint_as_float((unsigned)x);
    __builtin_amdgcn_s_sleep(16);
  }
  return __builtin_nanf("");
}
template <int N, int SLEEP> __device__ __forceinline__ void get2(rsrc_t r, unsigned off, int stride, unsigned tag, float& a, float& b) {
#pragma unroll 1
  for (unsigned spins = 0; spins < SPIN_CAP; ++spins) {
    asm volatile("" ::: "memory");
    v4u x[N];
#pragma unroll
    for (int k = 0; k < N; ++k) x[k] = __builtin_amdgcn_raw_buffer_load_b128(r, off, k * stride, 16);
    unsigned lo = tag, hi = tag;
#pragma unroll
    for (int k = 0; k < N; ++k) {
      lo = min(min(lo, x[k].y), x[k].w); hi = max(max(hi, x[k].y), x[k].w);
      a = k ? a + __uint_as_float(x[k].x) : __uint_as_float(x[k].x);
      b = k ? b + __uint_as_float(x[k].z) : __uint_as_float(x[k].z);
      asm volatile("" : "+v"(a), "+v"(b), "+v"(lo), "+v"(hi));
    }
    if (__builtin_amdgcn_ballot_w64(lo != hi) == 0) return;
    if (SLEEP) __builtin_amdgcn_s_sleep(SLEEP);
  }
  a = b = __builtin_nanf("");
}
__device__ __forceinline__ void post(LAS char* lds, int word, unsigned v, int lane) {
  asm volatile("" ::: "memory");
  if (lane == 0) *(volatile LAS unsigned*)(lds + L_FL + word * 4) = v;
  asm volatile("" ::: "memory");
}
__device__ __forceinline__ unsigned peek(LAS char* lds, int word) {
  return __builtin_amdgcn_readfirstlane(*(volatile LAS unsigned*)(lds + L_FL + word * 4));
}
template <int SLEEP> __device__ __forceinline__ void wait1(LAS char* lds, int word, unsigned want) {
  while (peek(lds, word) < want) { if (SLEEP) __builtin_amdgcn_s_sleep(SLEEP); }
  asm volatile("" ::: "memory");
}
template <int SLEEP, int N> __device__ __forceinline__ void waitn(LAS char* lds, int word, unsigned want) {
  for (;;) {
    const v4u v = *(volatile LAS v4u*)(lds + L_FL + word * 4);
    unsigned m = min(v.x, v.y);
    if (N == 4) m = min(m, min(v.z, v.w));
    if (__builtin_amdgcn_readfirstlane(m) >= want) break;
    if (SLEEP) __builtin_amdgcn_s_sleep(SLEEP);
  }
  asm volatile("" ::: "memory");
}
__device__ __forceinline__ int until(unsigned due) {
  int late;
  asm volatile("s_branch 2f\n1:\n\ts_sleep 1\n2:\n\ts_memrealtime vcc\n\ts_waitcnt lgkmcnt(0)\n\ts_lshl_b32 %0, vcc_lo, 4\n\t"
               "s_sub_i32 %0, %0, %1\n\ts_cmp_lt_i32 %0, 0\n\ts_cbranch_scc1 1b" : "=&s"(late) : "s"(due) : "vcc", "scc", "memory");
  return late;
}
struct RowMap { unsigned o[5]; };
__device__ __forceinline__ RowMap row_map(int lane) {
  RowMap m;
#pragma unroll
  for (int i = 0; i < 5; ++i) { const unsigned ci = i * 64 + lane, row = (ci * 52429u) >> 20; m.o[i] = row * (D * 2 - 320) + ci * 16; }
  asm volatile("" : "+v"(m.o[0]), "+v"(m.o[1]), "+v"(m.o[2]), "+v"(m.o[3]), "+v"(m.o[4]));
  return m;
}
__device__ __forceinline__ void stage_rows(gptr src, unsigned lds_dst, int ng, const RowMap& m, unsigned& due, unsigned& tau, unsigned dec) {
#pragma unroll 1
  for (int g = 0; g < ng; ++g) {
    unsigned keep;
    until(due); due += tau; tau -= dec;
    asm volatile("s_mov_b32 %0, m0\n\ts_mov_b32 m0, %7\n\ts_nop 0\n\tglobal_load_lds_dwordx4 %1, %6\n\t"
                 "s_add_u32 m0, m0, 0x400\n\ts_nop 0\n\tglobal_load_lds_dwordx4 %2, %6\n\t"
                 "s_add_u32 m0, m0, 0x400\n\ts_nop 0\n\tglobal_load_lds_dwordx4 %3, %6\n\t"
                 "s_add_u32 m0, m0, 0x400\n\ts_nop 0\n\tglobal_load_lds_dwordx4 %4, %6\n\t"
                 "s_add_u32 m0, m0, 0x400\n\ts_nop 0\n\tglobal_load_lds_dwordx4 %5, %6\n\ts_mov_b32 m0, %0"
                 : "=&s"(keep) : "v"(m.o[0]), "v"(m.o[1]), "v"(m.o[2]), "v"(m.o[3]), "v"(m.o[4]), "s"(src), "s"(lds_dst) : "scc", "memory");
    src += 16 * D * 2; lds_dst += 5120;
  }
}
__device__ __forceinline__ v4u tr_frag(const LAS char* p) {
  const v2u b0 = __builtin_bit_cast(v2u, __builtin_amdgcn_ds_read_tr16_b64_v4i16((LAS s4*)p));
  const v2u b1 = __builtin_bit_cast(v2u, __builtin_amdgcn_ds_read_tr16_b64_v4i16((LAS s4*)(p + 1280)));
  return (v4u){b0.x, b0.y, b1.x, b1.y};
}

extern "C" __global__ void __launch_bounds__(512, 2)
fused_decode(const u16* x_, const u16* kc_, const u16* vc_, const u16* wq_, const u16* wo_, const u16* wg_, const u16* wu_,
             const u16* wd_, u16* out_, char* ws_) {
  unsigned ka;
  asm volatile("s_load_dword %0, %1, 0x40" : "=&s"(ka) : "s"(__builtin_amdgcn_kernarg_segment_ptr()));
  SB();
  __shared__ __attribute__((aligned(16))) char lds_raw[L_TOTAL];
  LAS char* lds = (LAS char*)lds_raw;
  GAS char* ws = (GAS char*)ws_;
  const int tid = threadIdx.x, lane = tid & 63, wave = __builtin_amdgcn_readfirstlane(tid >> 6);
  const int w = blockIdx.x, h = (w & 7) * 4 + (w >> 6), mem = (w >> 3) & 7;
  const int ww = wave - 4;
  const int hc = (h + 4) & 31;
  u64 code0;
  {
    u64 pc; unsigned back;
    asm volatile("s_getpc_b64 %0\n.Lfd_here:\n\ts_mov_b32 %1, .Lfd_here-fused_decode" : "=s"(pc), "=s"(back));
    code0 = pc - back;
  }
  auto warm_code = [&](int who) {
    u64 ex; unsigned keep;
    const unsigned c0 = (unsigned)(tid - who * 64) * 4096u + (w >> 3) * 128u;
    asm volatile("v_cmp_gt_u32 vcc, .Lfd_last-fused_decode, %2\n\ts_and_saveexec_b64 %0, vcc\n\ts_cbranch_execz 1f\n\t"
                 "s_mov_b32 %1, m0\n\ts_mov_b32 m0, %4\n\ts_nop 0\n\tglobal_load_lds_dword %2, %3\n\ts_mov_b32 m0, %1\n"
                 "1:\n\ts_mov_b64 exec, %0"
                 : "=&s"(ex), "=&s"(keep) : "v"(c0), "s"(code0), "s"((unsigned)(size_t)lds + L_FL + W_DUMP * 4) : "vcc", "scc", "memory");
  };
  warm_code(0);
  const u64 t64 = __builtin_amdgcn_s_memrealtime();
  const unsigned t0 = (unsigned)t64;
  const bool ffn = w >= 64, isq = mem < 6;
  const int fb = (w & 7) * 3 + (w >> 6) - 1;
  const int row0 = (mem & 1) * 320 + (lane >> 3), qm = (mem - 6) >> 31;
  unsigned rs8 = isq ? 8 * H * DH * 2 : 8 * F * 2;
  int jobw = wave + ((w >> 4) == 3 ? 4 : 0);
  unsigned woff = (row0 + wave * 80) * (rs8 >> 3) + (((h * 3 + (mem >> 1)) & qm) | (fb & ~qm)) * 128 + (lane & 7) * 16;
  unsigned xoff = (wave & 1) * (D * 2) + lane * 4;
  asm volatile("" : "+s"(rs8), "+s"(jobw));
  asm volatile("" : "+v"(woff), "+v"(xoff));
  SB();
  const bool job = jobw < 4;
  unsigned xr[5];
  v4u R[10];
  if (job) {
#pragma unroll
    for (int i = 0; i < 5; ++i) xr[i] = *(const GAS unsigned*)((gptr)x_ + xoff + 256 * i);
    SB();
    const rsrc_t wr = mk_rsrc(isq ? wq_ : wg_, 80 * rs8);
    unsigned so = 0;
#pragma unroll
    for (int i = 0; i < 10; ++i) { R[i] = ld_nt(wr, woff, so); so += rs8; SB(); }
  }
  if ((w >> 3) == 7 && tid < 16) {
    const unsigned g = lane >> 2, hg = (w & 7) * 4 + g, e = lane & 1;
    const unsigned okv = (((lane >> 1) & 1) * H + hg) * (SK * DH * 2) + e * (SK * DH * 2 - 4), owo = ((hg + 4) & 31) * (DH * D * 2) + e * (DH * D * 2 - 4);
    const unsigned off = lane * (D * F * 2 / 16), ows = (e ? WS_END : 2048) + (w & 7) * 128, oo = (w & 7) * 128;
    unsigned keep;
    asm volatile("s_mov_b32 %0, m0\n\ts_mov_b32 m0, %13\n\ts_nop 0\n\tglobal_load_lds_dword %1, %6 nt\n\tglobal_load_lds_dword %3, %9 nt\n\t"
                 "global_load_lds_dword %4, %11 nt\n\tglobal_load_lds_dword %1, %7 nt\n\tglobal_load_lds_dword %2, %8 nt\n\t"
                 "global_load_lds_dword %3, %10 nt\n\tglobal_load_lds_dword %5, %12 nt\n\ts_mov_b32 m0, %0"
                 : "=&s"(keep) : "v"(okv), "v"(owo), "v"(off), "v"(ows), "v"(oo), "s"(kc_), "s"(vc_), "s"(wo_), "s"(wu_), "s"(wd_), "s"(ws_), "s"(out_),
                   "s"((unsigned)(size_t)lds + L_PP) : "memory");
  }
  const rsrc_t wsr = mk_rsrc(ws_, WS_END);
  unsigned fin0 = t0 * 16u + (ORG + 63 * TAU);
  unsigned tag;
  v4u msk;
  auto meet = [&]() {
    tag = *(const __attribute__((address_space(4))) unsigned*)(ws_ + WS_TAG) + 1u;
    const unsigned mm = (lane & 1) ? 0xffff0000u : 0x0000ffffu;
    const int q = (lane & 7) >> 1;
    msk = (v4u){q == 0 ? mm : 0u, q == 1 ? mm : 0u, q == 2 ? mm : 0u, q == 3 ? mm : 0u};
    asm volatile("" : "+s"(tag) : "s"(t64), "s"(ka));
    asm volatile("" : "+v"(msk));
    BAR();
    fin0 += max((int)(((unsigned)__builtin_amdgcn_s_memrealtime() - t0) * 16u) + OM - ORG, 0);
  };

  if (wave >= 4) {
    NO_LOADS();
    if (tid < 280) *(volatile LAS unsigned*)(lds + L_FL + lane * 4) = (lane & 15) < W_PLAN ? 0u : fin0;
    meet();
    if (ww == 0) post(lds, W_PLAN, fin0, lane);
    __builtin_amdgcn_s_setprio(2);
    constexpr int HSTR = SK * DH * 2, BSTR = H * HSTR;
    const rsrc_t kr = mk_rsrc((const char*)kc_ + (size_t)h * HSTR, BSTR + HSTR), vr = mk_rsrc((const char*)vc_ + (size_t)h * HSTR, BSTR + HSTR);
    auto group_off = [&](int g) { return ((g & 4) ? 0 : BSTR) + ((g + h) & 3) * (4 * STEP); };
    unsigned loff = (mem * 4 + ww) * 3072 + (lane >> 3) * 384 + (lane & 7) * 16;
    LAS char* flw = lds + L_FL + (W_PROG + ww) * 4;
    const LAS char* qp = lds + L_QO + (lane & 7) * 16;
    LAS float* sc = (LAS float*)(lds + L_SC) + 256 + ww * 8 + (lane >> 3) + (lane & 3) * 32 - (lane & 4) * 64;
    LAS char* part = lds + L_PART + ((ww * 2 + (lane >> 5)) * DH + (lane & 7) * 8 + ((lane >> 4) & 1) * 4) * 4;
    const LAS unsigned* pp = (const LAS unsigned*)(lds + L_PP) + ww * 8 + (lane >> 3);
    asm volatile("" : "+v"(loff), "+v"(flw), "+v"(qp), "+v"(sc), "+v"(part), "+v"(pp));
    unsigned due = fin0 - 63 * TAU + ww * (TAU / 4), fin = fin0;
    int adj = 0, step = TAU, slow = 0, late = 0, win;
    auto pace = [&](int lead = 0) {
      late = until(due - lead) - lead; due += step; fin += adj;
      win = min((unsigned)(late + ((WBASE - 4 + ww) << WSH)) >> (WSH + 2), WMAX - 1u);
      if (lead && late < 0) { win = 4; late = 0; }
      SB();
    };
    auto report = [&](unsigned prog) {
      asm volatile("s_mov_b64 exec, 1\n\tds_write2_b32 %0, %1, %2 offset1:12\n\ts_mov_b64 exec, -1" :: "v"(flw), "v"(prog), "v"(fin + late) : "memory");
    };
    auto steer = [&](v2u pl, int g) {
      const int left = 61 - LEND - 4 * g, mul = g < 13 ? MUL : g == 13 ? 28 : g == 14 ? 51 : 256;
      const int sw = __builtin_amdgcn_readfirstlane(pl.x), sl = (sw & 1) * VSLOW;
      fin += 16 * (sl - slow); slow = sl;
      const int slope = left > 0 ? sw >> 1 : 0, gap = (int)(pl.y - fin) - slope * left;
      adj = __builtin_amdgcn_readfirstlane(min(max(slope + ((gap * mul) >> 8), -FAST), TAU / 2));
      step = TAU + adj + ((unsigned)(g - 7) < 4u ? slow : 0);
    };
    auto fetch = [&](rsrc_t r, int so, int j) {
      unsigned cnt;
      asm volatile("1:\n\ts_getreg_b32 %0, hwreg(HW_REG_IB_STS, 0, 4)\n\ts_cmp_gt_u32 %0, %1\n\ts_cbranch_scc1 1b" : "=&s"(cnt) : "s"(win) : "scc", "memory");
      const v4u x = ld_nt(r, loff + j * 128, so);
      SB();
      return x;
    };
    v4u U[NP + 4][3];
#pragma unroll
    for (int u = 0; u < NP + 4; ++u) {
      pace((NP + 3 - u) * LEAD);
#pragma unroll
      for (int j = 0; j < 3; ++j) U[u][j] = fetch(kr, group_off(u >> 2) + (u & 3) * STEP, j);
      if (u & 1) report(u >> 1);
    }
    v4u q[3], q2[3];
    for (;;) {
      const v4u f = *(volatile LAS v4u*)(lds + L_FL + W_Q * 4);
#pragma unroll
      for (int j = 0; j < 3; ++j) { q[j] = *(volatile LAS v4u*)(qp + 384 + j * 128); q2[j] = *(volatile LAS v4u*)(qp + j * 128); }
      if (__builtin_amdgcn_readfirstlane(f.x & f.y & f.z)) break;
    }
#pragma unroll 1
    for (int g = 3; g < 9; ++g) {
      const rsrc_t r = g == 8 ? vr : kr;
      const int so = group_off(g);
      float pick = 0.f;
      v2u pl;
#pragma unroll
      for (int k = 0; k < 4; ++k) {
        if (k == 1) pl = *(volatile LAS v2u*)(lds + L_FL + W_SLOPE * 4);
        pace();
        float d = 0.f;
#pragma unroll
        for (int j = 0; j < 3; ++j) { d = dot8(U[NP + k][j], q[j], d); asm volatile("" : "+v"(d)); SB(); U[NP + k][j] = fetch(r, so + k * STEP, j); }
        if (k == 1) steer(pl, g); else if (k != 3) report(2 * g + (k >> 1));
        d = sum8(d);
        pick = (lane & 7) == k ? d : pick;
        if (g < 5) {
          const float e = sum8(dot8(U[k][2], q[2], dot8(U[k][1], q[1], dot8(U[k][0], q[0], 0.f))));
          pick = (lane & 7) == 4 + k ? e : pick;
#pragma unroll
          for (int j = 0; j < 3; ++j) U[k][j] = U[4 + k][j];
        }
        asm volatile("" : "+v"(pick));
        SB();
      }
      if (g < 5 || !(lane & 4)) *sc = rbf(rbf(pick) * 0.07216878364870322f);
      sc += 128;
      if (g == 4) {
#pragma unroll
        for (int j = 0; j < 3; ++j) q[j] = q2[j];
      }
      SB();
    }
    f4 o[3];
#pragma unroll
    for (int j = 0; j < 3; ++j) o[j] = (f4){0.f, 0.f, 0.f, 0.f};
    auto vstep = [&](int g, bool more) {
      const int so = group_off(g);
      unsigned f = *(volatile LAS unsigned*)(lds + L_FL + W_P * 4), pv[4];
#pragma unroll
      for (int k = 0; k < 4; ++k) pv[k] = *(volatile LAS unsigned*)(pp + k * 32);
      v2u pl;
#pragma unroll
      for (int k = 0; k < 4; ++k) {
        if (k == 1 && more) pl = *(volatile LAS v2u*)(lds + L_FL + W_SLOPE * 4);
        if (more) pace();
        if (k == 0 && more && (unsigned)__builtin_amdgcn_readfirstlane(f) < (g < 13 ? 1u : 2u)) {
          wait1<0>(lds, W_P, g < 13 ? 1u : 2u);
#pragma unroll
          for (int i = 0; i < 4; ++i) pv[i] = *(volatile LAS unsigned*)(pp + i * 32);
        }
        const v4u a = msk & pv[k];
#pragma unroll
        for (int j = 0; j < 3; ++j) { o[j] = mfma(a, U[NP + k][j], o[j]); SB(); if (more) U[NP + k][j] = fetch(vr, so + k * STEP, j); }
        if (more) { if (k == 1) steer(pl, g); else if (k != 3) report(2 * g + (k >> 1)); }
        SB();
      }
      pp += 128;
    };
    auto vflush = [&](int ph) {
      if (((lane >> 3) & 1) == (lane >> 5)) {
#pragma unroll
        for (int j = 0; j < 3; ++j) *(LAS f4*)(part + ph * (8 * DH * 4) + j * 256) = o[j];
      }
    };
#pragma unroll 1
    for (int g = 9; g < 16; ++g) {
      vstep(g, true);
      if (g == 12) {
        vflush(0);
#pragma unroll
        for (int j = 0; j < 3; ++j) o[j] = (f4){0.f, 0.f, 0.f, 0.f};
      }
    }
    fin -= adj;
    report(P_OUT);
    vstep(16, false);
    vflush(1);
    report(P_DONE);
    __builtin_amdgcn_s_setprio(0);
  } else {
    meet();
    if (job) {
      float v[10], sum = 0.f;
#pragma unroll
      for (int i = 0; i < 5; ++i) { v[2 * i] = __uint_as_float(xr[i] << 16); v[2 * i + 1] = __uint_as_float(xr[i] & 0xffff0000u); sum += v[2 * i] + v[2 * i + 1]; }
      const float mean = wave_sum(sum) * (1.f / D);
      float q = 0.f;
#pragma unroll
      for (int i = 0; i < 10; ++i) { const float dd = v[i] - mean; q += dd * dd; }
      const float var = rbf(wave_sum(q) * (1.f / D)), mb = rbf(mean);
      const float inv = __builtin_amdgcn_rcpf(rbf(__builtin_amdgcn_sqrtf(rbf(var + 1e-5f))));
#pragma unroll
      for (int i = 0; i < 10; ++i) {
        const unsigned t = bfbits(rbf(v[i] - mb) * inv);
        *(LAS unsigned*)(lds + L_XX + ((lane + 64 * (i >> 1)) * 2 + (i & 1)) * 8 + (wave & 1) * 4) = t | (t << 16);
      }
      post(lds, W_LN + (wave & 1), 1, lane);
      waitn<0, 2>(lds, W_LN, 1);
#pragma unroll 1
      for (int pass = 0;; ++pass) {
        f4 a0 = {0.f, 0.f, 0.f, 0.f}, a1 = a0;
        const LAS char* xp = lds + L_XX + (pass ? mem * 80 + (lane >> 3) : row0 + wave * 80) * 8;
#pragma unroll
        for (int i = 0; i < 10; ++i) {
          const v2u xv = *(const LAS v2u*)(xp + i * 64);
          a0 = mfma(msk & xv.x, R[i], a0); a1 = mfma(msk & xv.y, R[i], a1);
        }
        const bool more = wave == 3 && ffn && pass == 0;
        if (more) {
          const rsrc_t wr = mk_rsrc(wu_, D * F * 2);
          const unsigned uoff = (mem * 80 + (lane >> 3)) * (F * 2) + fb * 128 + (lane & 7) * 16;
#pragma unroll
          for (int i = 0; i < 10; ++i) { R[i] = ld_nt(wr, uoff, i * (8 * F * 2)); SB(); }
        }
        const f4 s = {fold(a0[0], a1[0]), fold(a0[1], a1[1]), fold(a0[2], a1[2]), fold(a0[3], a1[3])};
        if ((lane & 8) == 0) *(LAS f4*)(lds + L_PJ + (wave * 2 + (lane >> 5)) * 256 + (lane & 7) * 32 + ((lane >> 4) & 1) * 16) = s;
        post(lds, W_PJ + wave, 1, lane);
        if (wave < 3) break;
        waitn<1, 4>(lds, W_PJ, 1);
        const LAS float* pr = (const LAS float*)(lds + L_PJ) + lane;
        const float c0 = pr[384], c1 = pr[448];
        put2(wsr, WS_P + ((h * 16 + mem + 8 * pass) * 64 + lane) * 16, tag,
             pass ? c0 : (pr[0] + pr[128]) + (pr[256] + c0), pass ? c1 : (pr[64] + pr[192]) + (pr[320] + c1));
        if (!more) break;
      }
    }
    if (wave < 3) {
      const int ot = wave * 64 + lane;
      {
        wait1<4>(lds, W_PROG, T_Q);
        float r0, r1;
        get2<2, 24>(wsr, WS_P + ((h * 16 + wave * 2) * 64 + lane) * 16, 1024, tag, r0, r1);
        LAS u16* dst = (LAS u16*)(lds + L_QO) + ot;
        dst[0] = (u16)bfbits(r0); dst[DH] = (u16)bfbits(r1);
        post(lds, W_Q + wave, 1, lane);
      }
      if (wave == 2) {
        if (ffn) {
          wait1<8>(lds, W_PROG, T_H);
          float gt[2], up[2];
          get2<2, 24>(wsr, WS_P + ((h * 16 + 6) * 64 + lane) * 16, 1024, tag, gt[0], gt[1]);
          get2<8, 24>(wsr, WS_P + ((h * 16 + 8) * 64 + lane) * 16, 1024, tag, up[0], up[1]);
#pragma unroll
          for (int b = 0; b < 2; ++b) {
            const float g = rbf(gt[b]), t2 = 2.f * 0.7978845608028654f * (g + 0.044715f * g * g * g);
            *(LAS u16*)(lds + L_HB + b * 128 + lane * 2) = (u16)bfbits(rbf(g * __builtin_amdgcn_rcpf(1.f + __expf(-t2))) * rbf(up[b]));
          }
        }
        post(lds, W_HID, 1, lane);
      } else if (wave == 0) {
        gu64* stbox = (gu64*)(ws + WS_ST) + h * 32;
#pragma unroll 1
        for (int ph = 0; ph < 2; ++ph) {
          waitn<4, 4>(lds, W_PROG, P_K1 + ph * 8);
          const f4 s0 = *(const LAS f4*)(lds + L_SC + ph * 2048 + lane * 32), s1 = *(const LAS f4*)(lds + L_SC + ph * 2048 + lane * 32 + 16);
          const float s[8] = {s0.x, s0.y, s0.z, s0.w, s1.x, s1.y, s1.z, s1.w};
          constexpr float L2E = 1.4426950408889634f;
          float m = s[0];
#pragma unroll
          for (int i = 1; i < 8; ++i) m = fmaxf(m, s[i]);
          m = wave_max(m);
          const float ms = m == -INFINITY ? 0.f : m;
          float l = 0.f;
#pragma unroll
          for (int i = 0; i < 8; ++i) l += __builtin_amdgcn_exp2f((s[i] - ms) * L2E);
          l = wave_sum(l);
          if (lane < 2) put(stbox + ph * 16 + mem * 2 + lane, tag, __float_as_uint(lane ? l : m));
          wait1<4>(lds, W_PROG, P_K1 + ph * 8 + T_ST);
          const float st = get(stbox + ph * 16 + (lane & 15), tag);
          const float oth = dpp<XOR1>(st), mj = (lane & 1) ? oth : st, lj = (lane & 1) ? st : oth;
          float M = fmaxf(mj, dpp<ROR2>(mj)); M = fmaxf(M, dpp<ROR4>(M)); M = fmaxf(M, dpp<ROR8>(M));
          float Ls = lj * __builtin_amdgcn_exp2f((mj - M) * L2E);
          Ls += dpp<ROR2>(Ls); Ls += dpp<ROR4>(Ls); Ls += dpp<ROR8>(Ls);
          const float inv = __builtin_amdgcn_rcpf(__uint_as_float(__builtin_amdgcn_readfirstlane(__float_as_uint(Ls))));
          unsigned p[8];
#pragma unroll
          for (int i = 0; i < 8; ++i) { const unsigned t = bfbits(__builtin_amdgcn_exp2f((s[i] - M) * L2E) * inv); p[i] = t | (t << 16); }
          *(LAS v4u*)(lds + L_PP + ph * 2048 + lane * 32) = (v4u){p[0], p[1], p[2], p[3]};
          *(LAS v4u*)(lds + L_PP + ph * 2048 + lane * 32 + 16) = (v4u){p[4], p[5], p[6], p[7]};
          post(lds, W_P, ph + 1, lane);
        }
      } else {
        unsigned at = t0 * 16u + CP0, plan = fin0;
        float prev = 0.f;
        int slope = 0, vs = 0;
        const unsigned half = (unsigned)__builtin_amdgcn_s_getreg(20 | (2 << 6) | (0 << 11));
#pragma unroll 1
        for (int c = 0; c < NCP; ++c) {
          at = __builtin_amdgcn_readfirstlane(at);
          until(at);
          const v4u e = *(volatile LAS v4u*)(lds + L_FL + W_EST * 4);
          gu64* cp = (gu64*)(ws + WS_CP) + c * 256;
          if (lane == 0 && (mem & 3) == 1)
            put(cp + (w >> 5) * 8 + (w & 7), tag,
                ((e.x + (unsigned)(((int)(e.y - e.x) + (int)(e.z - e.x) + (int)(e.w - e.x)) >> 2)) & ~3u) | half | (peek(lds, W_P) ? 2u : 0u));
          unsigned look = at + (c ? CPW : CPW0);
          u64 x; bool ok;
#pragma unroll 1
          for (int tr = c ? 2 : 0;; ++tr) {
            until(look);
            x = __hip_atomic_load(cp + lane, RLX_AGENT);
            ok = (unsigned)(x >> 32) == tag;
            if (tr == 2 || __all(ok)) break;
            look += CPW;
          }
          if (w == 0 && c == NCP - 1 && __all(ok && (x & 2))) {
            if (lane == 0) __hip_atomic_store((GAS unsigned*)(ws + WS_TAG), tag, RLX_AGENT);
            post(lds, W_MOVED, 1, lane);
          }
          const bool up = ok && (x & 1);
          const float sum = ok ? (float)(int)((unsigned)x - fin0) : 0.f;
          const float sa = wave_sum(sum), su = wave_sum(up ? sum : 0.f);
          const float na = (float)__builtin_popcountll(__builtin_amdgcn_ballot_w64(ok)), nu = (float)__builtin_popcountll(__builtin_amdgcn_ballot_w64(up));
          const float mean = sa * __builtin_amdgcn_rcpf(fmaxf(na, 1.f));
          const float hi = fmaxf(nu > 0.f ? su * __builtin_amdgcn_rcpf(nu) : mean, na > nu ? (sa - su) * __builtin_amdgcn_rcpf(na - nu) : mean);
          const float est = fmaxf(mean, hi - 0.015f * (float)(int)(plan - at));
          const bool first = c < 2;
          const unsigned before = plan;
          const float worst = wave_max(ok ? sum : -1e9f) - 600.f;
          if (first || __builtin_amdgcn_readfirstlane(est - (float)(int)(plan - fin0) > 100.f))
            plan = fin0 + __builtin_amdgcn_readfirstlane((int)fminf(fmaxf(
                first ? est + 80.f : est + (c == 2 ? 0.5f : c == 3 ? 2.f : 0.3f) * fminf(fmaxf(est - prev, 0.f), 600.f) + 100.f, -8000.f), 16000.f));
          const bool hint = c == 1 && __builtin_amdgcn_readfirstlane(est - prev > 580.f);
          prev = est;
          if (!first) plan += __builtin_amdgcn_readfirstlane((int)(0.25f * fminf(fmaxf(worst - (float)(int)(plan - fin0), 0.f), 4000.f)));
          if (first) {
            const v4u e2 = *(volatile LAS v4u*)(lds + L_FL + W_EST * 4);
            const int own = min(min((int)(e2.x - plan), (int)(e2.y - plan)), min((int)(e2.z - plan), (int)(e2.w - plan)));
            const float left = (float)((int)(plan - look) + own - 2400) * (1.f / TAU) - (float)LEND;
            slope = (int)(-(float)own * __builtin_amdgcn_rcpf(fmaxf(left, 8.f)));
          }
          at = (c == 3 ? before : plan) - (c == 0 ? CP1 : c == 1 ? CP2 : c == 2 ? CP3 : CP4);
          if (hint) { vs = 1; plan += 16 * VSLOW; prev += (float)(16 * VSLOW); }
          if (lane == 0) *(volatile LAS v2u*)(lds + L_FL + W_SLOPE * 4) = (v2u){(unsigned)(slope * 2 + vs), plan};
        }
      }
      waitn<4, 4>(lds, W_PROG, P_V1 + 4);
      const LAS float* pr = (const LAS float*)(lds + L_PART) + ot;
      LAS u16* odst = (LAS u16*)(lds + L_QO) + (2 + (lane >> 5)) * DH + wave * 32 + (lane & 31);
      float s0 = 0.f;
#pragma unroll
      for (int r = 0; r < 8; ++r) s0 += pr[r * DH];
      unsigned oout = WS_O + ((h * 8 + mem) * DH + ot) * 16;
      unsigned oin = WS_O + ((hc * 8 + (lane >> 5) * 4) * DH + (mem & 1) * 96 + wave * 32 + (lane & 31)) * 16;
      asm volatile("" : "+v"(s0), "+v"(oout), "+v"(oin), "+v"(odst), "+v"(pr));
      wait1<2>(lds, W_PROG + 3, P_OUT);
      waitn<0, 4>(lds, W_PROG, P_DONE);
      const float s1 = ((pr[8 * DH] + pr[9 * DH]) + (pr[10 * DH] + pr[11 * DH])) + ((pr[12 * DH] + pr[13 * DH]) + (pr[14 * DH] + pr[15 * DH]));
      put2(wsr, oout, tag, s1, s0);
      __builtin_amdgcn_s_sleep(9);
      until(peek(lds, W_PLAN) + T_OG);
      float r0, r1;
      get2<4, 0>(wsr, oin, DH * 16, tag, r0, r1);
      auto t = __builtin_amdgcn_permlane32_swap(__float_as_uint(r0), __float_as_uint(r1), false, false);
      *odst = (u16)bfbits(__uint_as_float(t[0]) + __uint_as_float(t[1]));
      BAR();
      NO_LOADS();
      return;
    }
  }
  NO_LOADS();
  const int oline = (w >> 6) * 8 + (w & 7), ob = oline >= 10, ocol = (oline - 10 * ob) * 64 + mem * 8;
  const int oi = ob * D + ocol + (lane & 7);
  float xres = 0.f;
  if (wave == 3) {
    const unsigned lds0 = (unsigned)(size_t)lds;
    const RowMap rm = row_map(lane);
    wait1<8>(lds, W_HID, 1);
    wait1<8>(lds, W_PROG, P_V1 + 2);
    warm_code(3);
    if (oline < 20) xres = __uint_as_float((unsigned)((const GAS u16*)x_)[oi] << 16);
    asm volatile("" : "+v"(xres));
    wait1<2>(lds, W_PROG + 3, P_OUT - 1);
    const unsigned plan = *(volatile LAS unsigned*)(lds + L_FL + W_PLAN * 4), own = *(volatile LAS unsigned*)(lds + L_FL + (W_EST + 3) * 4);
    unsigned due = __builtin_amdgcn_readfirstlane(plan + min(max((int)(own - plan), 0), 1000)) + WO0, tau = ffn ? TAU_WO : TAU_WO * 4 / 3;
    const unsigned dec = ffn ? DEC_WO : DEC_WO * 2;
    until(__builtin_amdgcn_readfirstlane(own) + WO0);
    if (ffn) stage_rows((gptr)wd_ + ((fb * 64 + (mem & 1) * 32) * (D * 2) + (mem >> 1) * 320), lds0 + L_WD, 2, rm, due, tau, dec);
    stage_rows((gptr)wo_ + ((hc * DH + (mem & 1) * 96) * (D * 2) + (mem >> 1) * 320), lds0 + L_WO, 6, rm, due, tau, dec);
  }
  const int tt = wave - 3;
  const LAS char* ap = lds + L_QO + 768 + ((lane >> 2) & 1) * 384 + (lane >> 4) * 16;
  const LAS char* bp = lds + L_WO + ((lane >> 4) * 8 + ((lane >> 2) & 3)) * 320 + tt * 64 + (lane & 3) * 8;
  const LAS char* hp = lds + L_HB + ((lane >> 2) & 1) * 128 + (mem & 1) * 64 + (lane >> 4) * 16;
  unsigned ydst = WS_Y + (((((mem >> 1) * 5 + tt) * 4 + (lane >> 4)) * 64 + hc * 2 + (mem & 1)) * 16 + (lane & 15)) * 16;
  asm volatile("s_waitcnt vmcnt(10)" : "+v"(bp), "+v"(hp), "+v"(ap), "+v"(ydst) :: "memory");
  if (wave == 3) post(lds, W_STAGED, 1, lane);
  wait1<0>(lds, W_STAGED, 1);
  v4u bf[6];
#pragma unroll
  for (int i = 0; i < 4; ++i) bf[i] = tr_frag(bp + (i >> 1) * 10240 + (i & 1) * 32);
  const f4 zero = {0.f, 0.f, 0.f, 0.f};
  const v4u hf = *(const LAS v4u*)hp;
  const f4 g0 = mfma(hf, tr_frag(bp + L_WD), zero), g1 = mfma(hf, tr_frag(bp + L_WD + 32), zero);
  float gx = !ffn ? 0.f : lane < 32 ? g0.x : g1.x;
  asm volatile("s_waitcnt vmcnt(0)" : "+v"(ap), "+v"(ydst), "+v"(gx), "+v"(bf[0]), "+v"(bf[1]), "+v"(bf[2]), "+v"(bf[3]) :: "memory");
  BAR();
  {
    v4u a[3];
#pragma unroll
    for (int ks = 0; ks < 3; ++ks) a[ks] = *(const LAS v4u*)(ap + ks * 64);
    bf[4] = tr_frag(bp + 20480); bf[5] = tr_frag(bp + 20480 + 32);
    SB();
    f4 y0 = zero, y1 = zero;
#pragma unroll
    for (int ks = 0; ks < 3; ++ks) { y0 = mfma(a[ks], bf[2 * ks], y0); y1 = mfma(a[ks], bf[2 * ks + 1], y1); }
    put2(wsr, ydst, tag, lane < 32 ? y0.x : y1.x, gx);
  }
  if (wave == 3 && oline < 20) {
    unsigned ysrc = WS_Y + ((ocol >> 4) * 2 + ob) * (64 * 16 * 16) + (mem & 1) * 128 + (lane >> 3) * 256 + (lane & 7) * 16;
    GAS u16* op = (GAS u16*)out_ + oi;
    asm volatile("" : "+v"(ysrc), "+v"(op));
    const unsigned moved = peek(lds, W_MOVED);
    __builtin_amdgcn_s_sleep(6);
    until(peek(lds, W_PLAN) + T_YG);
    float s, gs;
    get2<8, 0>(wsr, ysrc, 2048, tag, s, gs);
    s += dpp<ROR8>(s); gs += dpp<ROR8>(gs);
    s = rbf(xrow_sum(s)); gs = rbf(xrow_sum(gs));
    const float gate = rbf(gs * __builtin_amdgcn_rcpf(1.f + __expf(-gs)));
    if (lane < 8) *op = (u16)bfbits(xres + rbf(s * gate));
    if (w == 0 && !moved && lane == 0) __hip_atomic_store((GAS unsigned*)(ws + WS_TAG), tag, RLX_AGENT);
  }
  asm volatile(".Lfd_last:");
}

extern "C" void kernel_launch(void* const* d_in, const int* in_sizes, int n_in, void* d_out, int out_size, void* d_ws, size_t ws_size,
                              hipStream_t stream) {
  fused_decode<<<256, NTHREADS, 0, stream>>>((const u16*)d_in[0], (const u16*)d_in[1], (const u16*)d_in[2], (const u16*)d_in[3],
                                             (const u16*)d_in[4], (const u16*)d_in[6], (const u16*)d_in[7], (const u16*)d_in[8],
                                             (u16*)d_out, (char*)d_ws);
}
```

```cpp
#include <hip/hip_runtime.h>
#include <stdint.h>

typedef unsigned short u16;
typedef unsigned long long u64;
typedef unsigned v4u __attribute__((ext_vector_type(4)));
typedef unsigned v2u __attribute__((ext_vector_type(2)));
typedef float f4 __attribute__((ext_vector_type(4)));
typedef short s4 __attribute__((ext_vector_type(4)));
typedef __bf16 bf16x2 __attribute__((ext_vector_type(2)));
typedef __bf16 bf16x8 __attribute__((ext_vector_type(8)));
#define GAS __attribute__((address_space(1)))
#define LAS __attribute__((address_space(3)))
#define RLX_AGENT __ATOMIC_RELAXED, __HIP_MEMORY_SCOPE_AGENT
typedef const GAS char* gptr;
typedef GAS u64 gu64;
typedef __amdgpu_buffer_rsrc_t rsrc_t;

constexpr int D = 640, H = 32, SK = 4096, DH = 192, F = 1536, NTHREADS = 512;
constexpr int STEP = 32 * 3072;
constexpr int NP = 8;
constexpr unsigned SPIN_CAP = 1u << 16;

constexpr int P_K1 = 10, P_V1 = 26, P_OUT = 32, P_DONE = 34;
constexpr int T_Q = 2, T_ST = 4, T_H = 22;

constexpr int TAU = 650;
constexpr int ORG = 2700, OM = 1400;
constexpr int WBASE = 24, WSH = 8, WMAX = 10;
constexpr int NCP = 5, CP0 = 8800;
constexpr int CP1 = 28000, CP2 = 20400, CP3 = 13200, CP4 = 9000;
constexpr int CPW0 = 4400, CPW = 2400;
constexpr int LEND = 13, MUL = 23, FAST = 100, VSLOW = 58;
constexpr int WO0 = 700, TAU_WO = 340, DEC_WO = 25;
constexpr int T_OG = 3150, T_YG = 4960;

constexpr int WS_TAG = 0;
constexpr int WS_CP = 4096;
constexpr int WS_ST = WS_CP + 8 * 256 * 8;
constexpr int WS_P = WS_ST + H * 2 * 16 * 8;
constexpr int WS_O = WS_P + H * 16 * 64 * 16;
constexpr int WS_Y = WS_O + H * 8 * DH * 16;
constexpr int WS_END = WS_Y + 80 * 64 * 16 * 16;
constexpr int L_WO = 0;
constexpr int L_WD = 30720;
constexpr int L_XX = L_WD + 10240;
constexpr int L_PART = L_XX + 5120;
constexpr int L_QO = L_PART + 12288;
constexpr int L_HB = L_QO + 1536;
constexpr int L_SC = L_HB + 256;
constexpr int L_PP = L_SC + 4096;
constexpr int L_FL = L_PP + 4096;
constexpr int L_PJ = L_FL + 160;
constexpr int L_TOTAL = 84 * 1024;
constexpr int W_PROG = 0, W_PJ = 4, W_LN = 8, W_SLOPE = 10, W_PLAN = 11, W_EST = 12, W_P = 16, W_STAGED = 17, W_HID = 18, W_MOVED = 19,
              W_Q = 20, W_DUMP = 32;

__device__ __forceinline__ float rbf(float f) { return (float)(__bf16)f; }
__device__ __forceinline__ unsigned bfbits(float f) { return (unsigned)__builtin_bit_cast(u16, (__bf16)f); }
__device__ __forceinline__ float dot2(unsigned a, unsigned b, float acc) {
  return __builtin_amdgcn_fdot2_f32_bf16(__builtin_bit_cast(bf16x2, a), __builtin_bit_cast(bf16x2, b), acc, false);
}
__device__ __forceinline__ float dot8(v4u a, v4u b, float acc) {
  return dot2(a.w, b.w, dot2(a.z, b.z, dot2(a.y, b.y, dot2(a.x, b.x, acc))));
}
__device__ __forceinline__ f4 mfma(v4u a, v4u b, f4 c) {
  return __builtin_amdgcn_mfma_f32_16x16x32_bf16(__builtin_bit_cast(bf16x8, a), __builtin_bit_cast(bf16x8, b), c, 0, 0, 0);
}
template <int CTRL> __device__ __forceinline__ float dpp(float x) {
  return __builtin_bit_cast(float, __builtin_amdgcn_mov_dpp(__builtin_bit_cast(int, x), CTRL, 0xf, 0xf, true));
}
constexpr int XOR1 = 0xB1, XOR2 = 0x4E, HMIR = 0x141, ROR2 = 0x122, ROR4 = 0x124, ROR8 = 0x128;
__device__ __forceinline__ float sum8(float x) { x += dpp<XOR1>(x); x += dpp<XOR2>(x); return x + dpp<HMIR>(x); }
__device__ __forceinline__ float xrow_sum(float x) {
  auto s = __builtin_amdgcn_permlane16_swap(__float_as_uint(x), __float_as_uint(x), false, false);
  x = __uint_as_float(s[0]) + __uint_as_float(s[1]);
  auto t = __builtin_amdgcn_permlane32_swap(__float_as_uint(x), __float_as_uint(x), false, false);
  return __uint_as_float(t[0]) + __uint_as_float(t[1]);
}
__device__ __forceinline__ float wave_sum(float x) { x = sum8(x); return xrow_sum(x + dpp<ROR8>(x)); }
__device__ __forceinline__ float wave_max(float x) {
  x = fmaxf(x, dpp<XOR1>(x)); x = fmaxf(x, dpp<XOR2>(x)); x = fmaxf(x, dpp<HMIR>(x)); x = fmaxf(x, dpp<ROR8>(x));
  auto s = __builtin_amdgcn_permlane16_swap(__float_as_uint(x), __float_as_uint(x), false, false);
  x = fmaxf(__uint_as_float(s[0]), __uint_as_float(s[1]));
  auto t = __builtin_amdgcn_permlane32_swap(__float_as_uint(x), __float_as_uint(x), false, false);
  return fmaxf(__uint_as_float(t[0]), __uint_as_float(t[1]));
}
__device__ __forceinline__ float fold(float a, float b) {
  auto t = __builtin_amdgcn_permlane32_swap(__float_as_uint(a), __float_as_uint(b), false, false);
  return __uint_as_float(t[0]) + dpp<ROR8>(__uint_as_float(t[1]));
}
__device__ __forceinline__ rsrc_t mk_rsrc(const void* p, int bytes) { return __builtin_amdgcn_make_buffer_rsrc((void*)p, 0, bytes, 0x00020000); }
__device__ __forceinline__ v4u ld_nt(rsrc_t r, unsigned voff, unsigned soff) { return __builtin_amdgcn_raw_buffer_load_b128(r, voff, soff, 2); }
#define SB() __builtin_amdgcn_sched_barrier(0)
#define BAR() asm volatile("s_waitcnt lgkmcnt(0)\n\ts_barrier" ::: "memory")
#define NO_LOADS() __builtin_amdgcn_s_waitcnt(0x0F70)

__device__ __forceinline__ void put(gu64* g, unsigned tag, unsigned v) { __hip_atomic_store(g, ((u64)tag << 32) | v, RLX_AGENT); }
__device__ __forceinline__ void put2(rsrc_t r, unsigned off, unsigned tag, float a, float b) {
  __builtin_amdgcn_raw_buffer_store_b128((v4u){__float_as_uint(a), tag, __float_as_uint(b), tag}, r, off, 0, 16);
}
__device__ __forceinline__ float get(gu64* g, unsigned tag) {
#pragma unroll 1
  for (unsigned spins = 0; spins < SPIN_CAP; ++spins) {
    const u64 x = __hip_atomic_load(g, RLX_AGENT);
    if (__all((unsigned)(x >> 32) == tag)) return __uint_as_float((unsigned)x);
    __builtin_amdgcn_s_sleep(16);
  }
  return __builtin_nanf("");
}
template <int N, int SLEEP> __device__ __forceinline__ void get2(rsrc_t r, unsigned off, int stride, unsigned tag, float& a, float& b) {
#pragma unroll 1
  for (unsigned spins = 0; spins < SPIN_CAP; ++spins) {
    asm volatile("" ::: "memory");
    v4u x[N];
#pragma unroll
    for (int k = 0; k < N; ++k) x[k] = __builtin_amdgcn_raw_buffer_load_b128(r, off, k * stride, 16);
    unsigned lo = tag, hi = tag;
#pragma unroll
    for (int k = 0; k < N; ++k) {
      lo = min(min(lo, x[k].y), x[k].w); hi = max(max(hi, x[k].y), x[k].w);
      a = k ? a + __uint_as_float(x[k].x) : __uint_as_float(x[k].x);
      b = k ? b + __uint_as_float(x[k].z) : __uint_as_float(x[k].z);
      asm volatile("" : "+v"(a), "+v"(b), "+v"(lo), "+v"(hi));
    }
    if (__builtin_amdgcn_ballot_w64(lo != hi) == 0) return;
    if (SLEEP) __builtin_amdgcn_s_sleep(SLEEP);
  }
  a = b = __builtin_nanf("");
}
__device__ __forceinline__ void post(LAS char* lds, int word, unsigned v, int lane) {
  asm volatile("" ::: "memory");
  if (lane == 0) *(volatile LAS unsigned*)(lds + L_FL + word * 4) = v;
  asm volatile("" ::: "memory");
}
__device__ __forceinline__ unsigned peek(LAS char* lds, int word) {
  return __builtin_amdgcn_readfirstlane(*(volatile LAS unsigned*)(lds + L_FL + word * 4));
}
template <int SLEEP> __device__ __forceinline__ void wait1(LAS char* lds, int word, unsigned want) {
  while (peek(lds, word) < want) { if (SLEEP) __builtin_amdgcn_s_sleep(SLEEP); }
  asm volatile("" ::: "memory");
}
template <int SLEEP, int N> __device__ __forceinline__ void waitn(LAS char* lds, int word, unsigned want) {
  for (;;) {
    const v4u v = *(volatile LAS v4u*)(lds + L_FL + word * 4);
    unsigned m = min(v.x, v.y);
    if (N == 4) m = min(m, min(v.z, v.w));
    if (__builtin_amdgcn_readfirstlane(m) >= want) break;
    if (SLEEP) __builtin_amdgcn_s_sleep(SLEEP);
  }
  asm volatile("" ::: "memory");
}
__device__ __forceinline__ int until(unsigned due) {
  int late;
  asm volatile("s_branch 2f\n1:\n\ts_sleep 1\n2:\n\ts_memrealtime vcc\n\ts_waitcnt lgkmcnt(0)\n\ts_lshl_b32 %0, vcc_lo, 4\n\t"
               "s_sub_i32 %0, %0, %1\n\ts_cmp_lt_i32 %0, 0\n\ts_cbranch_scc1 1b" : "=&s"(late) : "s"(due) : "vcc", "scc", "memory");
  return late;
}
struct RowMap { unsigned o[5]; };
__device__ __forceinline__ RowMap row_map(int lane) {
  RowMap m;
#pragma unroll
  for (int i = 0; i < 5; ++i) { const unsigned ci = i * 64 + lane, row = (ci * 52429u) >> 20; m.o[i] = row * (D * 2 - 320) + ci * 16; }
  asm volatile("" : "+v"(m.o[0]), "+v"(m.o[1]), "+v"(m.o[2]), "+v"(m.o[3]), "+v"(m.o[4]));
  return m;
}
__device__ __forceinline__ void stage_rows(gptr src, unsigned lds_dst, int ng, const RowMap& m, unsigned& due, unsigned& tau, unsigned dec) {
#pragma unroll 1
  for (int g = 0; g < ng; ++g) {
    unsigned keep;
    until(due); due += tau; tau -= dec;
    asm volatile("s_mov_b32 %0, m0\n\ts_mov_b32 m0, %7\n\ts_nop 0\n\tglobal_load_lds_dwordx4 %1, %6\n\t"
                 "s_add_u32 m0, m0, 0x400\n\ts_nop 0\n\tglobal_load_lds_dwordx4 %2, %6\n\t"
                 "s_add_u32 m0, m0, 0x400\n\ts_nop 0\n\tglobal_load_lds_dwordx4 %3, %6\n\t"
                 "s_add_u32 m0, m0, 0x400\n\ts_nop 0\n\tglobal_load_lds_dwordx4 %4, %6\n\t"
                 "s_add_u32 m0, m0, 0x400\n\ts_nop 0\n\tglobal_load_lds_dwordx4 %5, %6\n\ts_mov_b32 m0, %0"
                 : "=&s"(keep) : "v"(m.o[0]), "v"(m.o[1]), "v"(m.o[2]), "v"(m.o[3]), "v"(m.o[4]), "s"(src), "s"(lds_dst) : "scc", "memory");
    src += 16 * D * 2; lds_dst += 5120;
  }
}
__device__ __forceinline__ v4u tr_frag(const LAS char* p) {
  const v2u b0 = __builtin_bit_cast(v2u, __builtin_amdgcn_ds_read_tr16_b64_v4i16((LAS s4*)p));
  const v2u b1 = __builtin_bit_cast(v2u, __builtin_amdgcn_ds_read_tr16_b64_v4i16((LAS s4*)(p + 1280)));
  return (v4u){b0.x, b0.y, b1.x, b1.y};
}

extern "C" __global__ void __launch_bounds__(512, 2)
fused_decode(const u16* x_, const u16* kc_, const u16* vc_, const u16* wq_, const u16* wo_, const u16* wg_, const u16* wu_,
             const u16* wd_, u16* out_, char* ws_) {
  unsigned ka0, ka1;
  asm volatile("s_load_dword %0, %2, 0x0\n\ts_load_dword %1, %2, 0x40" : "=&s"(ka0), "=&s"(ka1) : "s"(__builtin_amdgcn_kernarg_segment_ptr()));
  SB();
  __shared__ __attribute__((aligned(16))) char lds_raw[L_TOTAL];
  LAS char* lds = (LAS char*)lds_raw;
  GAS char* ws = (GAS char*)ws_;
  const int tid = threadIdx.x, lane = tid & 63, wave = __builtin_amdgcn_readfirstlane(tid >> 6);
  const int w = blockIdx.x, h = (w & 7) * 4 + (w >> 6), mem = (w >> 3) & 7;
  const int ww = wave - 4;
  const int hc = (h + 4) & 31;
  u64 code0;
  {
    u64 pc; unsigned back;
    asm volatile("s_getpc_b64 %0\n.Lfd_here:\n\ts_mov_b32 %1, .Lfd_here-fused_decode" : "=s"(pc), "=s"(back));
    code0 = pc - back;
  }
  auto warm_code = [&](int who) {
    u64 ex; unsigned keep;
    const unsigned c0 = (unsigned)(tid - who * 64) * 4096u + (w >> 3) * 128u;
    asm volatile("v_cmp_gt_u32 vcc, .Lfd_last-fused_decode, %2\n\ts_and_saveexec_b64 %0, vcc\n\ts_cbranch_execz 1f\n\t"
                 "s_mov_b32 %1, m0\n\ts_mov_b32 m0, %4\n\ts_nop 0\n\tglobal_load_lds_dword %2, %3\n\ts_mov_b32 m0, %1\n"
                 "1:\n\ts_mov_b64 exec, %0"
                 : "=&s"(ex), "=&s"(keep) : "v"(c0), "s"(code0), "s"((unsigned)(size_t)lds + L_FL + W_DUMP * 4) : "vcc", "scc", "memory");
  };
  warm_code(0);
  const u64 t64 = __builtin_amdgcn_s_memrealtime();
  const unsigned t0 = (unsigned)t64;
  const bool ffn = w >= 64, isq = mem < 6;
  const int fb = (w & 7) * 3 + (w >> 6) - 1;
  const int row0 = (mem & 1) * 320 + (lane >> 3), qm = (mem - 6) >> 31;
  unsigned rs8 = isq ? 8 * H * DH * 2 : 8 * F * 2;
  int jobw = wave + ((w >> 4) == 3 ? 4 : 0);
  unsigned woff = (row0 + wave * 80) * (rs8 >> 3) + (((h * 3 + (mem >> 1)) & qm) | (fb & ~qm)) * 128 + (lane & 7) * 16;
  unsigned xoff = (wave & 1) * (D * 2) + lane * 4;
  asm volatile("" : "+s"(rs8), "+s"(jobw));
  asm volatile("" : "+v"(woff), "+v"(xoff));
  SB();
  const bool job = jobw < 4;
  unsigned xr[5];
  v4u R[10];
  if (job) {
#pragma unroll
    for (int i = 0; i < 5; ++i) xr[i] = *(const GAS unsigned*)((gptr)x_ + xoff + 256 * i);
    SB();
    const rsrc_t wr = mk_rsrc(isq ? wq_ : wg_, 80 * rs8);
    unsigned so = 0;
#pragma unroll
    for (int i = 0; i < 10; ++i) { R[i] = ld_nt(wr, woff, so); so += rs8; SB(); }
  }
  if ((w >> 3) == 7 && tid < 16) {
    const unsigned g = lane >> 2, hg = (w & 7) * 4 + g, e = lane & 1;
    const unsigned okv = (((lane >> 1) & 1) * H + hg) * (SK * DH * 2) + e * (SK * DH * 2 - 4), owo = ((hg + 4) & 31) * (DH * D * 2) + e * (DH * D * 2 - 4);
    const unsigned off = lane * (D * F * 2 / 16), ows = (e ? WS_END : 2048) + (w & 7) * 128, oo = (w & 7) * 128;
    unsigned keep;
    asm volatile("s_mov_b32 %0, m0\n\ts_mov_b32 m0, %13\n\ts_nop 0\n\tglobal_load_lds_dword %1, %6 nt\n\tglobal_load_lds_dword %3, %9 nt\n\t"
                 "global_load_lds_dword %4, %11 nt\n\tglobal_load_lds_dword %1, %7 nt\n\tglobal_load_lds_dword %2, %8 nt\n\t"
                 "global_load_lds_dword %3, %10 nt\n\tglobal_load_lds_dword %5, %12 nt\n\ts_mov_b32 m0, %0"
                 : "=&s"(keep) : "v"(okv), "v"(owo), "v"(off), "v"(ows), "v"(oo), "s"(kc_), "s"(vc_), "s"(wo_), "s"(wu_), "s"(wd_), "s"(ws_), "s"(out_),
                   "s"((unsigned)(size_t)lds + L_PP) : "memory");
  }
  const rsrc_t wsr = mk_rsrc(ws_, WS_END);
  unsigned fin0 = t0 * 16u + (ORG + 63 * TAU);
  unsigned tag;
  v4u msk;
  auto meet = [&]() {
    tag = *(const __attribute__((address_space(4))) unsigned*)(ws_ + WS_TAG) + 1u;
    const unsigned mm = (lane & 1) ? 0xffff0000u : 0x0000ffffu;
    const int q = (lane & 7) >> 1;
    msk = (v4u){q == 0 ? mm : 0u, q == 1 ? mm : 0u, q == 2 ? mm : 0u, q == 3 ? mm : 0u};
    asm volatile("" : "+s"(tag) : "s"(t64), "s"(ka0), "s"(ka1));
    asm volatile("" : "+v"(msk));
    BAR();
    fin0 += max((int)(((unsigned)__builtin_amdgcn_s_memrealtime() - t0) * 16u) + OM - ORG, 0);
  };

  if (wave >= 4) {
    NO_LOADS();
    if (tid < 280) *(volatile LAS unsigned*)(lds + L_FL + lane * 4) = (lane & 15) < W_PLAN ? 0u : fin0;
    meet();
    if (ww == 0) post(lds, W_PLAN, fin0, lane);
    __builtin_amdgcn_s_setprio(2);
    constexpr int HSTR = SK * DH * 2, BSTR = H * HSTR;
    const rsrc_t kr = mk_rsrc((const char*)kc_ + (size_t)h * HSTR, BSTR + HSTR), vr = mk_rsrc((const char*)vc_ + (size_t)h * HSTR, BSTR + HSTR);
    auto group_off = [&](int g) { return ((g & 4) ? 0 : BSTR) + ((g + h) & 3) * (4 * STEP); };
    unsigned loff = (mem * 4 + ww) * 3072 + (lane >> 3) * 384 + (lane & 7) * 16;
    LAS char* flw = lds + L_FL + (W_PROG + ww) * 4;
    const LAS char* qp = lds + L_QO + (lane & 7) * 16;
    LAS float* sc = (LAS float*)(lds + L_SC) + 256 + ww * 8 + (lane >> 3) + (lane & 3) * 32 - (lane & 4) * 64;
    LAS char* part = lds + L_PART + ((ww * 2 + (lane >> 5)) * DH + (lane & 7) * 8 + ((lane >> 4) & 1) * 4) * 4;
    const LAS unsigned* pp = (const LAS unsigned*)(lds + L_PP) + ww * 8 + (lane >> 3);
    asm volatile("" : "+v"(loff), "+v"(flw), "+v"(qp), "+v"(sc), "+v"(part), "+v"(pp));
    unsigned due = fin0 - 63 * TAU + ww * (TAU / 4), fin = fin0;
    int adj = 0, step = TAU, slow = 0, late = 0, win;
    auto pace = [&]() {
      late = until(due); due += step; fin += adj;
      win = min((unsigned)(late + ((WBASE - 4 + ww) << WSH)) >> (WSH + 2), WMAX - 1u);
      SB();
    };
    auto report = [&](unsigned prog) {
      asm volatile("s_mov_b64 exec, 1\n\tds_write2_b32 %0, %1, %2 offset1:12\n\ts_mov_b64 exec, -1" :: "v"(flw), "v"(prog), "v"(fin + late) : "memory");
    };
    auto steer = [&](v2u pl, int g) {
      const int left = 61 - LEND - 4 * g, mul = g < 13 ? MUL : g == 13 ? 28 : g == 14 ? 51 : 256;
      const int sw = __builtin_amdgcn_readfirstlane(pl.x), sl = (sw & 1) * VSLOW;
      fin += 16 * (sl - slow); slow = sl;
      const int slope = left > 0 ? sw >> 1 : 0, gap = (int)(pl.y - fin) - slope * left;
      adj = __builtin_amdgcn_readfirstlane(min(max(slope + ((gap * mul) >> 8), -FAST), TAU / 2));
      step = TAU + adj + ((unsigned)(g - 7) < 4u ? slow : 0);
    };
    auto fetch = [&](rsrc_t r, int so, int j) {
      unsigned cnt;
      asm volatile("1:\n\ts_getreg_b32 %0, hwreg(HW_REG_IB_STS, 0, 4)\n\ts_cmp_gt_u32 %0, %1\n\ts_cbranch_scc1 1b" : "=&s"(cnt) : "s"(win) : "scc", "memory");
      const v4u x = ld_nt(r, loff + j * 128, so);
      SB();
      return x;
    };
    v4u U[NP + 4][3];
#pragma unroll
    for (int u = 0; u < NP + 4; ++u) {
      pace();
#pragma unroll
      for (int j = 0; j < 3; ++j) U[u][j] = fetch(kr, group_off(u >> 2) + (u & 3) * STEP, j);
      if (u & 1) report(u >> 1);
    }
    v4u q[3], q2[3];
    for (;;) {
      const v4u f = *(volatile LAS v4u*)(lds + L_FL + W_Q * 4);
#pragma unroll
      for (int j = 0; j < 3; ++j) { q[j] = *(volatile LAS v4u*)(qp + 384 + j * 128); q2[j] = *(volatile LAS v4u*)(qp + j * 128); }
      if (__builtin_amdgcn_readfirstlane(f.x & f.y & f.z)) break;
    }
#pragma unroll 1
    for (int g = 3; g < 9; ++g) {
      const rsrc_t r = g == 8 ? vr : kr;
      const int so = group_off(g);
      float pick = 0.f;
      v2u pl;
#pragma unroll
      for (int k = 0; k < 4; ++k) {
        if (k == 1) pl = *(volatile LAS v2u*)(lds + L_FL + W_SLOPE * 4);
        pace();
        float d = 0.f;
#pragma unroll
        for (int j = 0; j < 3; ++j) { d = dot8(U[NP + k][j], q[j], d); asm volatile("" : "+v"(d)); SB(); U[NP + k][j] = fetch(r, so + k * STEP, j); }
        if (k == 1) steer(pl, g); else if (k != 3) report(2 * g + (k >> 1));
        d = sum8(d);
        pick = (lane & 7) == k ? d : pick;
        if (g < 5) {
          const float e = sum8(dot8(U[k][2], q[2], dot8(U[k][1], q[1], dot8(U[k][0], q[0], 0.f))));
          pick = (lane & 7) == 4 + k ? e : pick;
#pragma unroll
          for (int j = 0; j < 3; ++j) U[k][j] = U[4 + k][j];
        }
        asm volatile("" : "+v"(pick));
        SB();
      }
      if (g < 5 || !(lane & 4)) *sc = rbf(rbf(pick) * 0.07216878364870322f);
      sc += 128;
      if (g == 4) {
#pragma unroll
        for (int j = 0; j < 3; ++j) q[j] = q2[j];
      }
      SB();
    }
    f4 o[3];
#pragma unroll
    for (int j = 0; j < 3; ++j) o[j] = (f4){0.f, 0.f, 0.f, 0.f};
    auto vstep = [&](int g, bool more) {
      const int so = group_off(g);
      unsigned f = *(volatile LAS unsigned*)(lds + L_FL + W_P * 4), pv[4];
#pragma unroll
      for (int k = 0; k < 4; ++k) pv[k] = *(volatile LAS unsigned*)(pp + k * 32);
      v2u pl;
#pragma unroll
      for (int k = 0; k < 4; ++k) {
        if (k == 1 && more) pl = *(volatile LAS v2u*)(lds + L_FL + W_SLOPE * 4);
        if (more) pace();
        if (k == 0 && more && (unsigned)__builtin_amdgcn_readfirstlane(f) < (g < 13 ? 1u : 2u)) {
          wait1<0>(lds, W_P, g < 13 ? 1u : 2u);
#pragma unroll
          for (int i = 0; i < 4; ++i) pv[i] = *(volatile LAS unsigned*)(pp + i * 32);
        }
        const v4u a = msk & pv[k];
#pragma unroll
        for (int j = 0; j < 3; ++j) { o[j] = mfma(a, U[NP + k][j], o[j]); SB(); if (more) U[NP + k][j] = fetch(vr, so + k * STEP, j); }
        if (more) { if (k == 1) steer(pl, g); else if (k != 3) report(2 * g + (k >> 1)); }
        SB();
      }
      pp += 128;
    };
    auto vflush = [&](int ph) {
      if (((lane >> 3) & 1) == (lane >> 5)) {
#pragma unroll
        for (int j = 0; j < 3; ++j) *(LAS f4*)(part + ph * (8 * DH * 4) + j * 256) = o[j];
      }
    };
#pragma unroll 1
    for (int g = 9; g < 16; ++g) {
      vstep(g, true);
      if (g == 12) {
        vflush(0);
#pragma unroll
        for (int j = 0; j < 3; ++j) o[j] = (f4){0.f, 0.f, 0.f, 0.f};
      }
    }
    fin -= adj;
    report(P_OUT);
    vstep(16, false);
    vflush(1);
    report(P_DONE);
    __builtin_amdgcn_s_setprio(0);
  } else {
    meet();
    if (job) {
      float v[10], sum = 0.f;
#pragma unroll
      for (int i = 0; i < 5; ++i) { v[2 * i] = __uint_as_float(xr[i] << 16); v[2 * i + 1] = __uint_as_float(xr[i] & 0xffff0000u); sum += v[2 * i] + v[2 * i + 1]; }
      const float mean = wave_sum(sum) * (1.f / D);
      float q = 0.f;
#pragma unroll
      for (int i = 0; i < 10; ++i) { const float dd = v[i] - mean; q += dd * dd; }
      const float var = rbf(wave_sum(q) * (1.f / D)), mb = rbf(mean);
      const float inv = __builtin_amdgcn_rcpf(rbf(__builtin_amdgcn_sqrtf(rbf(var + 1e-5f))));
#pragma unroll
      for (int i = 0; i < 10; ++i) {
        const unsigned t = bfbits(rbf(v[i] - mb) * inv);
        *(LAS unsigned*)(lds + L_XX + ((lane + 64 * (i >> 1)) * 2 + (i & 1)) * 8 + (wave & 1) * 4) = t | (t << 16);
      }
      post(lds, W_LN + (wave & 1), 1, lane);
      waitn<0, 2>(lds, W_LN, 1);
#pragma unroll 1
      for (int pass = 0;; ++pass) {
        f4 a0 = {0.f, 0.f, 0.f, 0.f}, a1 = a0;
        const LAS char* xp = lds + L_XX + (pass ? mem * 80 + (lane >> 3) : row0 + wave * 80) * 8;
#pragma unroll
        for (int i = 0; i < 10; ++i) {
          const v2u xv = *(const LAS v2u*)(xp + i * 64);
          a0 = mfma(msk & xv.x, R[i], a0); a1 = mfma(msk & xv.y, R[i], a1);
        }
        const bool more = wave == 3 && ffn && pass == 0;
        if (more) {
          const rsrc_t wr = mk_rsrc(wu_, D * F * 2);
          const unsigned uoff = (mem * 80 + (lane >> 3)) * (F * 2) + fb * 128 + (lane & 7) * 16;
#pragma unroll
          for (int i = 0; i < 10; ++i) { R[i] = ld_nt(wr, uoff, i * (8 * F * 2)); SB(); }
        }
        const f4 s = {fold(a0[0], a1[0]), fold(a0[1], a1[1]), fold(a0[2], a1[2]), fold(a0[3], a1[3])};
        if ((lane & 8) == 0) *(LAS f4*)(lds + L_PJ + (wave * 2 + (lane >> 5)) * 256 + (lane & 7) * 32 + ((lane >> 4) & 1) * 16) = s;
        post(lds, W_PJ + wave, 1, lane);
        if (wave < 3) break;
        waitn<1, 4>(lds, W_PJ, 1);
        const LAS float* pr = (const LAS float*)(lds + L_PJ) + lane;
        const float c0 = pr[384], c1 = pr[448];
        put2(wsr, WS_P + ((h * 16 + mem + 8 * pass) * 64 + lane) * 16, tag,
             pass ? c0 : (pr[0] + pr[128]) + (pr[256] + c0), pass ? c1 : (pr[64] + pr[192]) + (pr[320] + c1));
        if (!more) break;
      }
    }
    if (wave < 3) {
      const int ot = wave * 64 + lane;
      {
        wait1<4>(lds, W_PROG, T_Q);
        float r0, r1;
        get2<2, 24>(wsr, WS_P + ((h * 16 + wave * 2) * 64 + lane) * 16, 1024, tag, r0, r1);
        LAS u16* dst = (LAS u16*)(lds + L_QO) + ot;
        dst[0] = (u16)bfbits(r0); dst[DH] = (u16)bfbits(r1);
        post(lds, W_Q + wave, 1, lane);
      }
      if (wave == 2) {
        if (ffn) {
          wait1<8>(lds, W_PROG, T_H);
          float gt[2], up[2];
          get2<2, 24>(wsr, WS_P + ((h * 16 + 6) * 64 + lane) * 16, 1024, tag, gt[0], gt[1]);
          get2<8, 24>(wsr, WS_P + ((h * 16 + 8) * 64 + lane) * 16, 1024, tag, up[0], up[1]);
#pragma unroll
          for (int b = 0; b < 2; ++b) {
            const float g = rbf(gt[b]), t2 = 2.f * 0.7978845608028654f * (g + 0.044715f * g * g * g);
            *(LAS u16*)(lds + L_HB + b * 128 + lane * 2) = (u16)bfbits(rbf(g * __builtin_amdgcn_rcpf(1.f + __expf(-t2))) * rbf(up[b]));
          }
        }
        post(lds, W_HID, 1, lane);
      } else if (wave == 0) {
        gu64* stbox = (gu64*)(ws + WS_ST) + h * 32;
#pragma unroll 1
        for (int ph = 0; ph < 2; ++ph) {
          waitn<4, 4>(lds, W_PROG, P_K1 + ph * 8);
          const f4 s0 = *(const LAS f4*)(lds + L_SC + ph * 2048 + lane * 32), s1 = *(const LAS f4*)(lds + L_SC + ph * 2048 + lane * 32 + 16);
          const float s[8] = {s0.x, s0.y, s0.z, s0.w, s1.x, s1.y, s1.z, s1.w};
          constexpr float L2E = 1.4426950408889634f;
          float m = s[0];
#pragma unroll
          for (int i = 1; i < 8; ++i) m = fmaxf(m, s[i]);
          m = wave_max(m);
          const float ms = m == -INFINITY ? 0.f : m;
          float l = 0.f;
#pragma unroll
          for (int i = 0; i < 8; ++i) l += __builtin_amdgcn_exp2f((s[i] - ms) * L2E);
          l = wave_sum(l);
          if (lane < 2) put(stbox + ph * 16 + mem * 2 + lane, tag, __float_as_uint(lane ? l : m));
          wait1<4>(lds, W_PROG, P_K1 + ph * 8 + T_ST);
          const float st = get(stbox + ph * 16 + (lane & 15), tag);
          const float oth = dpp<XOR1>(st), mj = (lane & 1) ? oth : st, lj = (lane & 1) ? st : oth;
          float M = fmaxf(mj, dpp<ROR2>(mj)); M = fmaxf(M, dpp<ROR4>(M)); M = fmaxf(M, dpp<ROR8>(M));
          float Ls = lj * __builtin_amdgcn_exp2f((mj - M) * L2E);
          Ls += dpp<ROR2>(Ls); Ls += dpp<ROR4>(Ls); Ls += dpp<ROR8>(Ls);
          const float inv = __builtin_amdgcn_rcpf(__uint_as_float(__builtin_amdgcn_readfirstlane(__float_as_uint(Ls))));
          unsigned p[8];
#pragma unroll
          for (int i = 0; i < 8; ++i) { const unsigned t = bfbits(__builtin_amdgcn_exp2f((s[i] - M) * L2E) * inv); p[i] = t | (t << 16); }
          *(LAS v4u*)(lds + L_PP + ph * 2048 + lane * 32) = (v4u){p[0], p[1], p[2], p[3]};
          *(LAS v4u*)(lds + L_PP + ph * 2048 + lane * 32 + 16) = (v4u){p[4], p[5], p[6], p[7]};
          post(lds, W_P, ph + 1, lane);
        }
      } else {
        unsigned at = t0 * 16u + CP0, plan = fin0;
        float prev = 0.f;
        int slope = 0, vs = 0;
        const unsigned half = (unsigned)__builtin_amdgcn_s_getreg(20 | (2 << 6) | (0 << 11));
#pragma unroll 1
        for (int c = 0; c < NCP; ++c) {
          at = __builtin_amdgcn_readfirstlane(at);
          until(at);
          const v4u e = *(volatile LAS v4u*)(lds + L_FL + W_EST * 4);
          gu64* cp = (gu64*)(ws + WS_CP) + c * 256;
          if (lane == 0 && (mem & 3) == 1)
            put(cp + (w >> 5) * 8 + (w & 7), tag,
                ((e.x + (unsigned)(((int)(e.y - e.x) + (int)(e.z - e.x) + (int)(e.w - e.x)) >> 2)) & ~3u) | half | (peek(lds, W_P) ? 2u : 0u));
          unsigned look = at + (c ? CPW : CPW0);
          u64 x; bool ok;
#pragma unroll 1
          for (int tr = c ? 2 : 0;; ++tr) {
            until(look);
            x = __hip_atomic_load(cp + lane, RLX_AGENT);
            ok = (unsigned)(x >> 32) == tag;
            if (tr == 2 || __all(ok)) break;
            look += CPW;
          }
          if (w == 0 && c == NCP - 1 && __all(ok && (x & 2))) {
            if (lane == 0) __hip_atomic_store((GAS unsigned*)(ws + WS_TAG), tag, RLX_AGENT);
            post(lds, W_MOVED, 1, lane);
          }
          const bool up = ok && (x & 1);
          const float sum = ok ? (float)(int)((unsigned)x - fin0) : 0.f;
          const float sa = wave_sum(sum), su = wave_sum(up ? sum : 0.f);
          const float na = (float)__builtin_popcountll(__builtin_amdgcn_ballot_w64(ok)), nu = (float)__builtin_popcountll(__builtin_amdgcn_ballot_w64(up));
          const float mean = sa * __builtin_amdgcn_rcpf(fmaxf(na, 1.f));
          const float hi = fmaxf(nu > 0.f ? su * __builtin_amdgcn_rcpf(nu) : mean, na > nu ? (sa - su) * __builtin_amdgcn_rcpf(na - nu) : mean);
          const float est = fmaxf(mean, hi - 0.015f * (float)(int)(plan - at));
          const bool first = c < 2;
          const unsigned before = plan;
          const float worst = wave_max(ok ? sum : -1e9f) - 600.f;
          if (first || __builtin_amdgcn_readfirstlane(est - (float)(int)(plan - fin0) > 100.f))
            plan = fin0 + __builtin_amdgcn_readfirstlane((int)fminf(fmaxf(
                first ? est + 80.f : est + (c == 2 ? 0.5f : c == 3 ? 2.f : 0.3f) * fminf(fmaxf(est - prev, 0.f), 600.f) + 100.f, -8000.f), 16000.f));
          const bool hint = c == 1 && __builtin_amdgcn_readfirstlane(est - prev > 560.f);
          prev = est;
          if (!first) plan += __builtin_amdgcn_readfirstlane((int)(0.25f * fminf(fmaxf(worst - (float)(int)(plan - fin0), 0.f), 4000.f)));
          if (first) {
            const v4u e2 = *(volatile LAS v4u*)(lds + L_FL + W_EST * 4);
            const int own = min(min((int)(e2.x - plan), (int)(e2.y - plan)), min((int)(e2.z - plan), (int)(e2.w - plan)));
            const float left = (float)((int)(plan - look) + own - 2400) * (1.f / TAU) - (float)LEND;
            slope = (int)(-(float)own * __builtin_amdgcn_rcpf(fmaxf(left, 8.f)));
          }
          at = (c == 3 ? before : plan) - (c == 0 ? CP1 : c == 1 ? CP2 : c == 2 ? CP3 : CP4);
          if (hint) { vs = 1; plan += 16 * VSLOW; prev += (float)(16 * VSLOW); }
          if (lane == 0) *(volatile LAS v2u*)(lds + L_FL + W_SLOPE * 4) = (v2u){(unsigned)(slope * 2 + vs), plan};
        }
      }
      waitn<4, 4>(lds, W_PROG, P_V1 + 4);
      const LAS float* pr = (const LAS float*)(lds + L_PART) + ot;
      LAS u16* odst = (LAS u16*)(lds + L_QO) + (2 + (lane >> 5)) * DH + wave * 32 + (lane & 31);
      float s0 = 0.f;
#pragma unroll
      for (int r = 0; r < 8; ++r) s0 += pr[r * DH];
      unsigned oout = WS_O + ((h * 8 + mem) * DH + ot) * 16;
      unsigned oin = WS_O + ((hc * 8 + (lane >> 5) * 4) * DH + (mem & 1) * 96 + wave * 32 + (lane & 31)) * 16;
      asm volatile("" : "+v"(s0), "+v"(oout), "+v"(oin), "+v"(odst), "+v"(pr));
      wait1<2>(lds, W_PROG + 3, P_OUT);
      waitn<0, 4>(lds, W_PROG, P_DONE);
      const float s1 = ((pr[8 * DH] + pr[9 * DH]) + (pr[10 * DH] + pr[11 * DH])) + ((pr[12 * DH] + pr[13 * DH]) + (pr[14 * DH] + pr[15 * DH]));
      put2(wsr, oout, tag, s1, s0);
      __builtin_amdgcn_s_sleep(9);
      until(peek(lds, W_PLAN) + T_OG);
      float r0, r1;
      get2<4, 0>(wsr, oin, DH * 16, tag, r0, r1);
      auto t = __builtin_amdgcn_permlane32_swap(__float_as_uint(r0), __float_as_uint(r1), false, false);
      *odst = (u16)bfbits(__uint_as_float(t[0]) + __uint_as_float(t[1]));
      BAR();
      NO_LOADS();
      return;
    }
  }
  NO_LOADS();
  const int oline = (w >> 6) * 8 + (w & 7), ob = oline >= 10, ocol = (oline - 10 * ob) * 64 + mem * 8;
  const int oi = ob * D + ocol + (lane & 7);
  float xres = 0.f;
  if (wave == 3) {
    const unsigned lds0 = (unsigned)(size_t)lds;
    const RowMap rm = row_map(lane);
    wait1<8>(lds, W_HID, 1);
    wait1<8>(lds, W_PROG, P_V1 + 2);
    warm_code(3);
    if (oline < 20) xres = __uint_as_float((unsigned)((const GAS u16*)x_)[oi] << 16);
    asm volatile("" : "+v"(xres));
    wait1<2>(lds, W_PROG + 3, P_OUT - 1);
    const unsigned plan = *(volatile LAS unsigned*)(lds + L_FL + W_PLAN * 4), own = *(volatile LAS unsigned*)(lds + L_FL + (W_EST + 3) * 4);
    unsigned due = __builtin_amdgcn_readfirstlane(plan + min(max((int)(own - plan), 0), 1000)) + WO0, tau = ffn ? TAU_WO : TAU_WO * 4 / 3;
    const unsigned dec = ffn ? DEC_WO : DEC_WO * 2;
    until(__builtin_amdgcn_readfirstlane(own) + WO0);
    if (ffn) stage_rows((gptr)wd_ + ((fb * 64 + (mem & 1) * 32) * (D * 2) + (mem >> 1) * 320), lds0 + L_WD, 2, rm, due, tau, dec);
    stage_rows((gptr)wo_ + ((hc * DH + (mem & 1) * 96) * (D * 2) + (mem >> 1) * 320), lds0 + L_WO, 6, rm, due, tau, dec);
  }
  const int tt = wave - 3;
  const LAS char* ap = lds + L_QO + 768 + ((lane >> 2) & 1) * 384 + (lane >> 4) * 16;
  const LAS char* bp = lds + L_WO + ((lane >> 4) * 8 + ((lane >> 2) & 3)) * 320 + tt * 64 + (lane & 3) * 8;
  const LAS char* hp = lds + L_HB + ((lane >> 2) & 1) * 128 + (mem & 1) * 64 + (lane >> 4) * 16;
  unsigned ydst = WS_Y + (((((mem >> 1) * 5 + tt) * 4 + (lane >> 4)) * 64 + hc * 2 + (mem & 1)) * 16 + (lane & 15)) * 16;
  asm volatile("s_waitcnt vmcnt(10)" : "+v"(bp), "+v"(hp), "+v"(ap), "+v"(ydst) :: "memory");
  if (wave == 3) post(lds, W_STAGED, 1, lane);
  wait1<0>(lds, W_STAGED, 1);
  v4u bf[6];
#pragma unroll
  for (int i = 0; i < 4; ++i) bf[i] = tr_frag(bp + (i >> 1) * 10240 + (i & 1) * 32);
  const f4 zero = {0.f, 0.f, 0.f, 0.f};
  const v4u hf = *(const LAS v4u*)hp;
  const f4 g0 = mfma(hf, tr_frag(bp + L_WD), zero), g1 = mfma(hf, tr_frag(bp + L_WD + 32), zero);
  float gx = !ffn ? 0.f : lane < 32 ? g0.x : g1.x;
  asm volatile("s_waitcnt vmcnt(0)" : "+v"(ap), "+v"(ydst), "+v"(gx), "+v"(bf[0]), "+v"(bf[1]), "+v"(bf[2]), "+v"(bf[3]) :: "memory");
  BAR();
  {
    v4u a[3];
#pragma unroll
    for (int ks = 0; ks < 3; ++ks) a[ks] = *(const LAS v4u*)(ap + ks * 64);
    bf[4] = tr_frag(bp + 20480); bf[5] = tr_frag(bp + 20480 + 32);
    SB();
    f4 y0 = zero, y1 = zero;
#pragma unroll
    for (int ks = 0; ks < 3; ++ks) { y0 = mfma(a[ks], bf[2 * ks], y0); y1 = mfma(a[ks], bf[2 * ks + 1], y1); }
    put2(wsr, ydst, tag, lane < 32 ? y0.x : y1.x, gx);
  }
  if (wave == 3 && oline < 20) {
    unsigned ysrc = WS_Y + ((ocol >> 4) * 2 + ob) * (64 * 16 * 16) + (mem & 1) * 128 + (lane >> 3) * 256 + (lane & 7) * 16;
    GAS u16* op = (GAS u16*)out_ + oi;
    asm volatile("" : "+v"(ysrc), "+v"(op));
    const unsigned moved = peek(lds, W_MOVED);
    __builtin_amdgcn_s_sleep(6);
    until(peek(lds, W_PLAN) + T_YG);
    float s, gs;
    get2<8, 0>(wsr, ysrc, 2048, tag, s, gs);
    s += dpp<ROR8>(s); gs += dpp<ROR8>(gs);
    s = rbf(xrow_sum(s)); gs = rbf(xrow_sum(gs));
    const float gate = rbf(gs * __builtin_amdgcn_rcpf(1.f + __expf(-gs)));
    if (lane < 8) *op = (u16)bfbits(xres + rbf(s * gate));
    if (w == 0 && !moved && lane == 0) __hip_atomic_store((GAS unsigned*)(ws + WS_TAG), tag, RLX_AGENT);
  }
  asm volatile(".Lfd_last:");
}

extern "C" void kernel_launch(void* const* d_in, const int* in_sizes, int n_in, void* d_out, int out_size, void* d_ws, size_t ws_size,
                              hipStream_t stream) {
  fused_decode<<<256, NTHREADS, 0, stream>>>((const u16*)d_in[0], (const u16*)d_in[1], (const u16*)d_in[2], (const u16*)d_in[3],
                                             (const u16*)d_in[4], (const u16*)d_in[6], (const u16*)d_in[7], (const u16*)d_in[8],
                                             (u16*)d_out, (char*)d_ws);
}
```

```cpp
#include <hip/hip_runtime.h>
#include <stdint.h>

typedef unsigned short u16;
typedef unsigned long long u64;
typedef unsigned v4u __attribute__((ext_vector_type(4)));
typedef unsigned v2u __attribute__((ext_vector_type(2)));
typedef float f4 __attribute__((ext_vector_type(4)));
typedef short s4 __attribute__((ext_vector_type(4)));
typedef __bf16 bf16x2 __attribute__((ext_vector_type(2)));
typedef __bf16 bf16x8 __attribute__((ext_vector_type(8)));
#define GAS __attribute__((address_space(1)))
#define LAS __attribute__((address_space(3)))
#define RLX_AGENT __ATOMIC_RELAXED, __HIP_MEMORY_SCOPE_AGENT
typedef const GAS char* gptr;
typedef GAS u64 gu64;
typedef __amdgpu_buffer_rsrc_t rsrc_t;

constexpr int D = 640, H = 32, SK = 4096, DH = 192, F = 1536, NTHREADS = 512;
constexpr int STEP = 32 * 3072;
constexpr int NP = 8;
constexpr unsigned SPIN_CAP = 1u << 16;

constexpr int P_K1 = 10, P_V1 = 26, P_OUT = 32, P_DONE = 34;
constexpr int T_Q = 2, T_ST = 4, T_H = 22;

constexpr int TAU = 650;
constexpr int ORG = 2300, OM = 1000;
constexpr int LEAD = 118;
constexpr int WBASE = 24, WSH = 8, WMAX = 10;
constexpr int NCP = 5, CP0 = 8800;
constexpr int CP1 = 28000, CP2 = 21200, CP3 = 16100, CP4 = 10400;
constexpr int CPW0 = 4400, CPW = 2400, CPW3 = 2000;
constexpr int LEND = 13, MUL = 23, FAST = 100, VSLOW = 58;
constexpr int WO0 = 650, TAU_WO = 410, DEC_WO = 57;
constexpr int T_OG = 3150, T_YG = 4930;

constexpr int WS_TAG = 0;
constexpr int WS_CP = 4096;
constexpr int WS_ST = WS_CP + 8 * 256 * 8;
constexpr int WS_P = WS_ST + H * 2 * 16 * 8;
constexpr int WS_O = WS_P + H * 16 * 64 * 16;
constexpr int WS_Y = WS_O + H * 8 * DH * 16;
constexpr int WS_END = WS_Y + 80 * 64 * 16 * 16;
constexpr int L_WO = 0;
constexpr int L_WD = 30720;
constexpr int L_XX = L_WD + 10240;
constexpr int L_PART = L_XX + 5120;
constexpr int L_QO = L_PART + 12288;
constexpr int L_HB = L_QO + 1536;
constexpr int L_SC = L_HB + 256;
constexpr int L_PP = L_SC + 4096;
constexpr int L_FL = L_PP + 4096;
constexpr int L_PJ = L_FL + 160;
constexpr int L_TOTAL = 84 * 1024;
constexpr int W_PROG = 0, W_PJ = 4, W_LN = 8, W_SLOPE = 10, W_PLAN = 11, W_EST = 12, W_P = 16, W_STAGED = 17, W_HID = 18, W_MOVED = 19,
              W_Q = 20, W_GROW = 24, W_DUMP = 32;

__device__ __forceinline__ float rbf(float f) { return (float)(__bf16)f; }
__device__ __forceinline__ unsigned bfbits(float f) { return (unsigned)__builtin_bit_cast(u16, (__bf16)f); }
__device__ __forceinline__ float dot2(unsigned a, unsigned b, float acc) {
  return __builtin_amdgcn_fdot2_f32_bf16(__builtin_bit_cast(bf16x2, a), __builtin_bit_cast(bf16x2, b), acc, false);
}
__device__ __forceinline__ float dot8(v4u a, v4u b, float acc) {
  return dot2(a.w, b.w, dot2(a.z, b.z, dot2(a.y, b.y, dot2(a.x, b.x, acc))));
}
__device__ __forceinline__ f4 mfma(v4u a, v4u b, f4 c) {
  return __builtin_amdgcn_mfma_f32_16x16x32_bf16(__builtin_bit_cast(bf16x8, a), __builtin_bit_cast(bf16x8, b), c, 0, 0, 0);
}
template <int CTRL> __device__ __forceinline__ float dpp(float x) {
  return __builtin_bit_cast(float, __builtin_amdgcn_mov_dpp(__builtin_bit_cast(int, x), CTRL, 0xf, 0xf, true));
}
constexpr int XOR1 = 0xB1, XOR2 = 0x4E, HMIR = 0x141, ROR2 = 0x122, ROR4 = 0x124, ROR8 = 0x128;
__device__ __forceinline__ float sum8(float x) { x += dpp<XOR1>(x); x += dpp<XOR2>(x); return x + dpp<HMIR>(x); }
__device__ __forceinline__ float xrow_sum(float x) {
  auto s = __builtin_amdgcn_permlane16_swap(__float_as_uint(x), __float_as_uint(x), false, false);
  x = __uint_as_float(s[0]) + __uint_as_float(s[1]);
  auto t = __builtin_amdgcn_permlane32_swap(__float_as_uint(x), __float_as_uint(x), false, false);
  return __uint_as_float(t[0]) + __uint_as_float(t[1]);
}
__device__ __forceinline__ float wave_sum(float x) { x = sum8(x); return xrow_sum(x + dpp<ROR8>(x)); }
__device__ __forceinline__ float wave_max(float x) {
  x = fmaxf(x, dpp<XOR1>(x)); x = fmaxf(x, dpp<XOR2>(x)); x = fmaxf(x, dpp<HMIR>(x)); x = fmaxf(x, dpp<ROR8>(x));
  auto s = __builtin_amdgcn_permlane16_swap(__float_as_uint(x), __float_as_uint(x), false, false);
  x = fmaxf(__uint_as_float(s[0]), __uint_as_float(s[1]));
  auto t = __builtin_amdgcn_permlane32_swap(__float_as_uint(x), __float_as_uint(x), false, false);
  return fmaxf(__uint_as_float(t[0]), __uint_as_float(t[1]));
}
__device__ __forceinline__ float fold(float a, float b) {
  auto t = __builtin_amdgcn_permlane32_swap(__float_as_uint(a), __float_as_uint(b), false, false);
  return __uint_as_float(t[0]) + dpp<ROR8>(__uint_as_float(t[1]));
}
__device__ __forceinline__ rsrc_t mk_rsrc(const void* p, int bytes) { return __builtin_amdgcn_make_buffer_rsrc((void*)p, 0, bytes, 0x00020000); }
__device__ __forceinline__ v4u ld_nt(rsrc_t r, unsigned voff, unsigned soff) { return __builtin_amdgcn_raw_buffer_load_b128(r, voff, soff, 2); }
#define SB() __builtin_amdgcn_sched_barrier(0)
#define BAR() asm volatile("s_waitcnt lgkmcnt(0)\n\ts_barrier" ::: "memory")
#define NO_LOADS() __builtin_amdgcn_s_waitcnt(0x0F70)

__device__ __forceinline__ void put(gu64* g, unsigned tag, unsigned v) { __hip_atomic_store(g, ((u64)tag << 32) | v, RLX_AGENT); }
__device__ __forceinline__ void put2(rsrc_t r, unsigned off, unsigned tag, float a, float b) {
  __builtin_amdgcn_raw_buffer_store_b128((v4u){__float_as_uint(a), tag, __float_as_uint(b), tag}, r, off, 0, 16);
}
__device__ __forceinline__ float get(gu64* g, unsigned tag) {
#pragma unroll 1
  for (unsigned spins = 0; spins < SPIN_CAP; ++spins) {
    const u64 x = __hip_atomic_load(g, RLX_AGENT);
    if (__all((unsigned)(x >> 32) == tag)) return __uint_as_float((unsigned)x);
    __builtin_amdgcn_s_sleep(16);
  }
  return __builtin_nanf("");
}
template <int N, int SLEEP> __device__ __forceinline__ void get2(rsrc_t r, unsigned off, int stride, unsigned tag, float& a, float& b) {
#pragma unroll 1
  for (unsigned spins = 0; spins < SPIN_CAP; ++spins) {
    asm volatile("" ::: "memory");
    v4u x[N];
#pragma unroll
    for (int k = 0; k < N; ++k) x[k] = __builtin_amdgcn_raw_buffer_load_b128(r, off, k * stride, 16);
    unsigned lo = tag, hi = tag;
#pragma unroll
    for (int k = 0; k < N; ++k) {
      lo = min(min(lo, x[k].y), x[k].w); hi = max(max(hi, x[k].y), x[k].w);
      a = k ? a + __uint_as_float(x[k].x) : __uint_as_float(x[k].x);
      b = k ? b + __uint_as_float(x[k].z) : __uint_as_float(x[k].z);
      asm volatile("" : "+v"(a), "+v"(b), "+v"(lo), "+v"(hi));
    }
    if (__builtin_amdgcn_ballot_w64(lo != hi) == 0) return;
    if (SLEEP) __builtin_amdgcn_s_sleep(SLEEP);
  }
  a = b = __builtin_nanf("");
}
__device__ __forceinline__ void post(LAS char* lds, int word, unsigned v, int lane) {
  asm volatile("" ::: "memory");
  if (lane == 0) *(volatile LAS unsigned*)(lds + L_FL + word * 4) = v;
  asm volatile("" ::: "memory");
}
__device__ __forceinline__ unsigned peek(LAS char* lds, int word) {
  return __builtin_amdgcn_readfirstlane(*(volatile LAS unsigned*)(lds + L_FL + word * 4));
}
template <int SLEEP> __device__ __forceinline__ void wait1(LAS char* lds, int word, unsigned want) {
  while (peek(lds, word) < want) { if (SLEEP) __builtin_amdgcn_s_sleep(SLEEP); }
  asm volatile("" ::: "memory");
}
template <int SLEEP, int N> __device__ __forceinline__ void waitn(LAS char* lds, int word, unsigned want) {
  for (;;) {
    const v4u v = *(volatile LAS v4u*)(lds + L_FL + word * 4);
    unsigned m = min(v.x, v.y);
    if (N == 4) m = min(m, min(v.z, v.w));
    if (__builtin_amdgcn_readfirstlane(m) >= want) break;
    if (SLEEP) __builtin_amdgcn_s_sleep(SLEEP);
  }
  asm volatile("" ::: "memory");
}
__device__ __forceinline__ int until(unsigned due) {
  int late;
  asm volatile("s_branch 2f\n1:\n\ts_sleep 1\n2:\n\ts_memrealtime vcc\n\ts_waitcnt lgkmcnt(0)\n\ts_lshl_b32 %0, vcc_lo, 4\n\t"
               "s_sub_i32 %0, %0, %1\n\ts_cmp_lt_i32 %0, 0\n\ts_cbranch_scc1 1b" : "=&s"(late) : "s"(due) : "vcc", "scc", "memory");
  return late;
}
struct RowMap { unsigned o[5]; };
__device__ __forceinline__ RowMap row_map(int lane) {
  RowMap m;
#pragma unroll
  for (int i = 0; i < 5; ++i) { const unsigned ci = i * 64 + lane, row = (ci * 52429u) >> 20; m.o[i] = row * (D * 2 - 320) + ci * 16; }
  asm volatile("" : "+v"(m.o[0]), "+v"(m.o[1]), "+v"(m.o[2]), "+v"(m.o[3]), "+v"(m.o[4]));
  return m;
}
__device__ __forceinline__ void stage_rows(gptr src, unsigned lds_dst, int ng, const RowMap& m, unsigned& due, unsigned& tau, unsigned dec) {
#pragma unroll 1
  for (int g = 0; g < ng; ++g) {
    unsigned keep;
    until(due); due += tau; tau -= dec;
    asm volatile("s_mov_b32 %0, m0\n\ts_mov_b32 m0, %7\n\ts_nop 0\n\tglobal_load_lds_dwordx4 %1, %6\n\t"
                 "s_add_u32 m0, m0, 0x400\n\ts_nop 0\n\tglobal_load_lds_dwordx4 %2, %6\n\t"
                 "s_add_u32 m0, m0, 0x400\n\ts_nop 0\n\tglobal_load_lds_dwordx4 %3, %6\n\t"
                 "s_add_u32 m0, m0, 0x400\n\ts_nop 0\n\tglobal_load_lds_dwordx4 %4, %6\n\t"
                 "s_add_u32 m0, m0, 0x400\n\ts_nop 0\n\tglobal_load_lds_dwordx4 %5, %6\n\ts_mov_b32 m0, %0"
                 : "=&s"(keep) : "v"(m.o[0]), "v"(m.o[1]), "v"(m.o[2]), "v"(m.o[3]), "v"(m.o[4]), "s"(src), "s"(lds_dst) : "scc", "memory");
    src += 16 * D * 2; lds_dst += 5120;
  }
}
__device__ __forceinline__ v4u tr_frag(const LAS char* p) {
  const v2u b0 = __builtin_bit_cast(v2u, __builtin_amdgcn_ds_read_tr16_b64_v4i16((LAS s4*)p));
  const v2u b1 = __builtin_bit_cast(v2u, __builtin_amdgcn_ds_read_tr16_b64_v4i16((LAS s4*)(p + 1280)));
  return (v4u){b0.x, b0.y, b1.x, b1.y};
}

extern "C" __global__ void __launch_bounds__(512, 2)
fused_decode(const u16* x_, const u16* kc_, const u16* vc_, const u16* wq_, const u16* wo_, const u16* wg_, const u16* wu_,
             const u16* wd_, u16* out_, char* ws_) {
  unsigned ka;
  asm volatile("s_load_dword %0, %1, 0x40" : "=&s"(ka) : "s"(__builtin_amdgcn_kernarg_segment_ptr()));
  SB();
  __shared__ __attribute__((aligned(16))) char lds_raw[L_TOTAL];
  LAS char* lds = (LAS char*)lds_raw;
  GAS char* ws = (GAS char*)ws_;
  const int tid = threadIdx.x, lane = tid & 63, wave = __builtin_amdgcn_readfirstlane(tid >> 6);
  const int w = blockIdx.x, h = (w & 7) * 4 + (w >> 6), mem = (w >> 3) & 7;
  const int ww = wave - 4;
  const int hc = (h + 4) & 31;
  u64 code0;
  {
    u64 pc; unsigned back;
    asm volatile("s_getpc_b64 %0\n.Lfd_here:\n\ts_mov_b32 %1, .Lfd_here-fused_decode" : "=s"(pc), "=s"(back));
    code0 = pc - back;
  }
  auto warm_code = [&](int who) {
    u64 ex; unsigned keep;
    const unsigned c0 = (unsigned)(tid - who * 64) * 4096u + (w >> 3) * 128u;
    asm volatile("v_cmp_gt_u32 vcc, .Lfd_last-fused_decode, %2\n\ts_and_saveexec_b64 %0, vcc\n\ts_cbranch_execz 1f\n\t"
                 "s_mov_b32 %1, m0\n\ts_mov_b32 m0, %4\n\ts_nop 0\n\tglobal_load_lds_dword %2, %3\n\ts_mov_b32 m0, %1\n"
                 "1:\n\ts_mov_b64 exec, %0"
                 : "=&s"(ex), "=&s"(keep) : "v"(c0), "s"(code0), "s"((unsigned)(size_t)lds + L_FL + W_DUMP * 4) : "vcc", "scc", "memory");
  };
  warm_code(0);
  const u64 t64 = __builtin_amdgcn_s_memrealtime();
  const unsigned t0 = (unsigned)t64;
  const bool ffn = w >= 64, isq = mem < 6;
  const int fb = (w & 7) * 3 + (w >> 6) - 1;
  const int row0 = (mem & 1) * 320 + (lane >> 3), qm = (mem - 6) >> 31;
  unsigned rs8 = isq ? 8 * H * DH * 2 : 8 * F * 2;
  int jobw = wave + ((w >> 4) == 3 ? 4 : 0);
  unsigned woff = (row0 + wave * 80) * (rs8 >> 3) + (((h * 3 + (mem >> 1)) & qm) | (fb & ~qm)) * 128 + (lane & 7) * 16;
  unsigned xoff = (wave & 1) * (D * 2) + lane * 4;
  asm volatile("" : "+s"(rs8), "+s"(jobw));
  asm volatile("" : "+v"(woff), "+v"(xoff));
  SB();
  const bool job = jobw < 4;
  unsigned xr[5];
  v4u R[10];
  if (job) {
#pragma unroll
    for (int i = 0; i < 5; ++i) xr[i] = *(const GAS unsigned*)((gptr)x_ + xoff + 256 * i);
    SB();
    const rsrc_t wr = mk_rsrc(isq ? wq_ : wg_, 80 * rs8);
    unsigned so = 0;
#pragma unroll
    for (int i = 0; i < 10; ++i) { R[i] = ld_nt(wr, woff, so); so += rs8; SB(); }
  }
  if ((w >> 3) == 7 && tid < 16) {
    const unsigned g = lane >> 2, hg = (w & 7) * 4 + g, e = lane & 1;
    const unsigned okv = (((lane >> 1) & 1) * H + hg) * (SK * DH * 2) + e * (SK * DH * 2 - 4), owo = ((hg + 4) & 31) * (DH * D * 2) + e * (DH * D * 2 - 4);
    const unsigned off = lane * (D * F * 2 / 16), ows = (e ? WS_END : 2048) + (w & 7) * 128, oo = (w & 7) * 128;
    unsigned keep;
    asm volatile("s_mov_b32 %0, m0\n\ts_mov_b32 m0, %13\n\ts_nop 0\n\tglobal_load_lds_dword %1, %6 nt\n\tglobal_load_lds_dword %3, %9 nt\n\t"
                 "global_load_lds_dword %4, %11 nt\n\tglobal_load_lds_dword %1, %7 nt\n\tglobal_load_lds_dword %2, %8 nt\n\t"
                 "global_load_lds_dword %3, %10 nt\n\tglobal_load_lds_dword %5, %12 nt\n\ts_mov_b32 m0, %0"
                 : "=&s"(keep) : "v"(okv), "v"(owo), "v"(off), "v"(ows), "v"(oo), "s"(kc_), "s"(vc_), "s"(wo_), "s"(wu_), "s"(wd_), "s"(ws_), "s"(out_),
                   "s"((unsigned)(size_t)lds + L_PP) : "memory");
  }
  const rsrc_t wsr = mk_rsrc(ws_, WS_END);
  unsigned fin0 = t0 * 16u + (ORG + 63 * TAU);
  unsigned tag;
  v4u msk;
  auto meet = [&]() {
    tag = *(const __attribute__((address_space(4))) unsigned*)(ws_ + WS_TAG) + 1u;
    const unsigned mm = (lane & 1) ? 0xffff0000u : 0x0000ffffu;
    const int q = (lane & 7) >> 1;
    msk = (v4u){q == 0 ? mm : 0u, q == 1 ? mm : 0u, q == 2 ? mm : 0u, q == 3 ? mm : 0u};
    asm volatile("" : "+s"(tag) : "s"(t64), "s"(ka));
    asm volatile("" : "+v"(msk));
    BAR();
    fin0 += max((int)(((unsigned)__builtin_amdgcn_s_memrealtime() - t0) * 16u) + OM - ORG, 0);
  };

  if (wave >= 4) {
    NO_LOADS();
    if (tid < 284) *(volatile LAS unsigned*)(lds + L_FL + lane * 4) = lane < W_PLAN || lane >= W_P ? 0u : fin0;
    meet();
    if (ww == 0) post(lds, W_PLAN, fin0, lane);
    __builtin_amdgcn_s_setprio(2);
    constexpr int HSTR = SK * DH * 2, BSTR = H * HSTR;
    const rsrc_t kr = mk_rsrc((const char*)kc_ + (size_t)h * HSTR, BSTR + HSTR), vr = mk_rsrc((const char*)vc_ + (size_t)h * HSTR, BSTR + HSTR);
    auto group_off = [&](int g) { return ((g & 4) ? 0 : BSTR) + ((g + h) & 3) * (4 * STEP); };
    unsigned loff = (mem * 4 + ww) * 3072 + (lane >> 3) * 384 + (lane & 7) * 16;
    LAS char* flw = lds + L_FL + (W_PROG + ww) * 4;
    const LAS char* qp = lds + L_QO + (lane & 7) * 16;
    LAS float* sc = (LAS float*)(lds + L_SC) + 256 + ww * 8 + (lane >> 3) + (lane & 3) * 32 - (lane & 4) * 64;
    LAS char* part = lds + L_PART + ((ww * 2 + (lane >> 5)) * DH + (lane & 7) * 8 + ((lane >> 4) & 1) * 4) * 4;
    const LAS unsigned* pp = (const LAS unsigned*)(lds + L_PP) + ww * 8 + (lane >> 3);
    asm volatile("" : "+v"(loff), "+v"(flw), "+v"(qp), "+v"(sc), "+v"(part), "+v"(pp));
    unsigned due = fin0 - 63 * TAU + ww * (TAU / 4), fin = fin0;
    int adj = 0, step = TAU, slow = 0, late = 0, win;
    auto pace = [&](int lead = 0) {
      late = until(due - lead) - lead; due += step; fin += adj;
      win = min((unsigned)(late + ((WBASE - 4 + ww) << WSH)) >> (WSH + 2), WMAX - 1u);
      if (lead && late < 0) { win = 4; late = 0; }
      SB();
    };
    auto report = [&](unsigned prog) {
      asm volatile("s_mov_b64 exec, 1\n\tds_write2_b32 %0, %1, %2 offset1:12\n\ts_mov_b64 exec, -1" :: "v"(flw), "v"(prog), "v"(fin + late) : "memory");
    };
    auto gauge = [&]() {
      if (late >= 300) return late + 2 * TAU;
      asm volatile("s_waitcnt vmcnt(3)" ::: "memory");
      return (int)((unsigned)__builtin_amdgcn_s_memrealtime() * 16u - due) + 2 * step;
    };
    int back0 = 0;
    auto steer = [&](v2u pl, int g) {
      const int left = 61 - LEND - 4 * g, mul = g < 13 ? MUL : g == 13 ? 28 : g == 14 ? 51 : 256;
      const int sw = __builtin_amdgcn_readfirstlane(pl.x), sl = (sw & 1) * VSLOW;
      fin += 16 * (sl - slow); slow = sl;
      const int slope = left > 0 ? sw >> 1 : 0, gap = (int)(pl.y - fin) - slope * left;
      adj = __builtin_amdgcn_readfirstlane(min(max(slope + ((gap * mul) >> 8), -FAST), TAU / 2));
      step = TAU + adj + ((unsigned)(g - 7) < 4u ? slow : 0);
    };
    auto fetch = [&](rsrc_t r, int so, int j) {
      unsigned cnt;
      asm volatile("1:\n\ts_getreg_b32 %0, hwreg(HW_REG_IB_STS, 0, 4)\n\ts_cmp_gt_u32 %0, %1\n\ts_cbranch_scc1 1b" : "=&s"(cnt) : "s"(win) : "scc", "memory");
      const v4u x = ld_nt(r, loff + j * 128, so);
      SB();
      return x;
    };
    v4u U[NP + 4][3];
#pragma unroll
    for (int u = 0; u < NP + 4; ++u) {
      pace((NP + 3 - u) * LEAD);
#pragma unroll
      for (int j = 0; j < 3; ++j) U[u][j] = fetch(kr, group_off(u >> 2) + (u & 3) * STEP, j);
      if (u & 1) report(u >> 1);
    }
    v4u q[3], q2[3];
    for (;;) {
      const v4u f = *(volatile LAS v4u*)(lds + L_FL + W_Q * 4);
#pragma unroll
      for (int j = 0; j < 3; ++j) { q[j] = *(volatile LAS v4u*)(qp + 384 + j * 128); q2[j] = *(volatile LAS v4u*)(qp + j * 128); }
      if (__builtin_amdgcn_readfirstlane(f.x & f.y & f.z)) break;
    }
#pragma unroll 1
    for (int g = 3; g < 9; ++g) {
      const rsrc_t r = g == 8 ? vr : kr;
      const int so = group_off(g);
      float pick = 0.f;
      v2u pl;
#pragma unroll
      for (int k = 0; k < 4; ++k) {
        if (k == 1) pl = *(volatile LAS v2u*)(lds + L_FL + W_SLOPE * 4);
        pace();
        float d = 0.f;
#pragma unroll
        for (int j = 0; j < 3; ++j) { d = dot8(U[NP + k][j], q[j], d); asm volatile("" : "+v"(d)); SB(); U[NP + k][j] = fetch(r, so + k * STEP, j); }
        if (k == 1) steer(pl, g); else if (k != 3) report(2 * g + (k >> 1));
        d = sum8(d);
        pick = (lane & 7) == k ? d : pick;
        if (g < 5) {
          const float e = sum8(dot8(U[k][2], q[2], dot8(U[k][1], q[1], dot8(U[k][0], q[0], 0.f))));
          pick = (lane & 7) == 4 + k ? e : pick;
#pragma unroll
          for (int j = 0; j < 3; ++j) U[k][j] = U[4 + k][j];
        }
        asm volatile("" : "+v"(pick));
        SB();
        if (k == 0 && g == 8) back0 = gauge();
      }
      if (g < 5 || !(lane & 4)) *sc = rbf(rbf(pick) * 0.07216878364870322f);
      sc += 128;
      if (g == 4) {
#pragma unroll
        for (int j = 0; j < 3; ++j) q[j] = q2[j];
      }
      SB();
    }
    f4 o[3];
#pragma unroll
    for (int j = 0; j < 3; ++j) o[j] = (f4){0.f, 0.f, 0.f, 0.f};
    auto vstep = [&](int g, bool more) {
      const int so = group_off(g);
      unsigned f = *(volatile LAS unsigned*)(lds + L_FL + W_P * 4), pv[4];
#pragma unroll
      for (int k = 0; k < 4; ++k) pv[k] = *(volatile LAS unsigned*)(pp + k * 32);
      v2u pl;
#pragma unroll
      for (int k = 0; k < 4; ++k) {
        if (k == 1 && more) pl = *(volatile LAS v2u*)(lds + L_FL + W_SLOPE * 4);
        if (more) pace();
        if (k == 0 && more && (unsigned)__builtin_amdgcn_readfirstlane(f) < (g < 13 ? 1u : 2u)) {
          wait1<0>(lds, W_P, g < 13 ? 1u : 2u);
#pragma unroll
          for (int i = 0; i < 4; ++i) pv[i] = *(volatile LAS unsigned*)(pp + i * 32);
        }
        const v4u a = msk & pv[k];
#pragma unroll
        for (int j = 0; j < 3; ++j) { o[j] = mfma(a, U[NP + k][j], o[j]); SB(); if (more) U[NP + k][j] = fetch(vr, so + k * STEP, j); }
        if (more) {
          if (k == 1) steer(pl, g); else if (k != 3) report(2 * g + (k >> 1));
          if (k == 0 && g == 9) post(lds, W_GROW + ww, gauge() - back0, lane);
        }
        SB();
      }
      pp += 128;
    };
    auto vflush = [&](int ph) {
      if (((lane >> 3) & 1) == (lane >> 5)) {
#pragma unroll
        for (int j = 0; j < 3; ++j) *(LAS f4*)(part + ph * (8 * DH * 4) + j * 256) = o[j];
      }
    };
#pragma unroll 1
    for (int g = 9; g < 16; ++g) {
      vstep(g, true);
      if (g == 12) {
        vflush(0);
#pragma unroll
        for (int j = 0; j < 3; ++j) o[j] = (f4){0.f, 0.f, 0.f, 0.f};
      }
    }
    fin -= adj;
    report(P_OUT);
    vstep(16, false);
    vflush(1);
    report(P_DONE);
    __builtin_amdgcn_s_setprio(0);
  } else {
    meet();
    if (job) {
      float v[10], sum = 0.f;
#pragma unroll
      for (int i = 0; i < 5; ++i) { v[2 * i] = __uint_as_float(xr[i] << 16); v[2 * i + 1] = __uint_as_float(xr[i] & 0xffff0000u); sum += v[2 * i] + v[2 * i + 1]; }
      const float mean = wave_sum(sum) * (1.f / D);
      float q = 0.f;
#pragma unroll
      for (int i = 0; i < 10; ++i) { const float dd = v[i] - mean; q += dd * dd; }
      const float var = rbf(wave_sum(q) * (1.f / D)), mb = rbf(mean);
      const float inv = __builtin_amdgcn_rcpf(rbf(__builtin_amdgcn_sqrtf(rbf(var + 1e-5f))));
#pragma unroll
      for (int i = 0; i < 10; ++i) {
        const unsigned t = bfbits(rbf(v[i] - mb) * inv);
        *(LAS unsigned*)(lds + L_XX + ((lane + 64 * (i >> 1)) * 2 + (i & 1)) * 8 + (wave & 1) * 4) = t | (t << 16);
      }
      post(lds, W_LN + (wave & 1), 1, lane);
      waitn<0, 2>(lds, W_LN, 1);
#pragma unroll 1
      for (int pass = 0;; ++pass) {
        f4 a0 = {0.f, 0.f, 0.f, 0.f}, a1 = a0;
        const LAS char* xp = lds + L_XX + (pass ? mem * 80 + (lane >> 3) : row0 + wave * 80) * 8;
#pragma unroll
        for (int i = 0; i < 10; ++i) {
          const v2u xv = *(const LAS v2u*)(xp + i * 64);
          a0 = mfma(msk & xv.x, R[i], a0); a1 = mfma(msk & xv.y, R[i], a1);
        }
        const bool more = wave == 3 && ffn && pass == 0;
        if (more) {
          const rsrc_t wr = mk_rsrc(wu_, D * F * 2);
          const unsigned uoff = (mem * 80 + (lane >> 3)) * (F * 2) + fb * 128 + (lane & 7) * 16;
#pragma unroll
          for (int i = 0; i < 10; ++i) { R[i] = ld_nt(wr, uoff, i * (8 * F * 2)); SB(); }
        }
        const f4 s = {fold(a0[0], a1[0]), fold(a0[1], a1[1]), fold(a0[2], a1[2]), fold(a0[3], a1[3])};
        if ((lane & 8) == 0) *(LAS f4*)(lds + L_PJ + (wave * 2 + (lane >> 5)) * 256 + (lane & 7) * 32 + ((lane >> 4) & 1) * 16) = s;
        post(lds, W_PJ + wave, 1, lane);
        if (wave < 3) break;
        waitn<1, 4>(lds, W_PJ, 1);
        const LAS float* pr = (const LAS float*)(lds + L_PJ) + lane;
        const float c0 = pr[384], c1 = pr[448];
        put2(wsr, WS_P + ((h * 16 + mem + 8 * pass) * 64 + lane) * 16, tag,
             pass ? c0 : (pr[0] + pr[128]) + (pr[256] + c0), pass ? c1 : (pr[64] + pr[192]) + (pr[320] + c1));
        if (!more) break;
      }
    }
    if (wave < 3) {
      const int ot = wave * 64 + lane;
      {
        wait1<4>(lds, W_PROG, T_Q);
        float r0, r1;
        get2<2, 24>(wsr, WS_P + ((h * 16 + wave * 2) * 64 + lane) * 16, 1024, tag, r0, r1);
        LAS u16* dst = (LAS u16*)(lds + L_QO) + ot;
        dst[0] = (u16)bfbits(r0); dst[DH] = (u16)bfbits(r1);
        post(lds, W_Q + wave, 1, lane);
      }
      if (wave == 2) {
        if (ffn) {
          wait1<8>(lds, W_PROG, T_H);
          float gt[2], up[2];
          get2<2, 24>(wsr, WS_P + ((h * 16 + 6) * 64 + lane) * 16, 1024, tag, gt[0], gt[1]);
          get2<8, 24>(wsr, WS_P + ((h * 16 + 8) * 64 + lane) * 16, 1024, tag, up[0], up[1]);
#pragma unroll
          for (int b = 0; b < 2; ++b) {
            const float g = rbf(gt[b]), t2 = 2.f * 0.7978845608028654f * (g + 0.044715f * g * g * g);
            *(LAS u16*)(lds + L_HB + b * 128 + lane * 2) = (u16)bfbits(rbf(g * __builtin_amdgcn_rcpf(1.f + __expf(-t2))) * rbf(up[b]));
          }
        }
        post(lds, W_HID, 1, lane);
      } else if (wave == 0) {
        gu64* stbox = (gu64*)(ws + WS_ST) + h * 32;
#pragma unroll 1
        for (int ph = 0; ph < 2; ++ph) {
          waitn<4, 4>(lds, W_PROG, P_K1 + ph * 8);
          const f4 s0 = *(const LAS f4*)(lds + L_SC + ph * 2048 + lane * 32), s1 = *(const LAS f4*)(lds + L_SC + ph * 2048 + lane * 32 + 16);
          const float s[8] = {s0.x, s0.y, s0.z, s0.w, s1.x, s1.y, s1.z, s1.w};
          constexpr float L2E = 1.4426950408889634f;
          float m = s[0];
#pragma unroll
          for (int i = 1; i < 8; ++i) m = fmaxf(m, s[i]);
          m = wave_max(m);
          const float ms = m == -INFINITY ? 0.f : m;
          float l = 0.f;
#pragma unroll
          for (int i = 0; i < 8; ++i) l += __builtin_amdgcn_exp2f((s[i] - ms) * L2E);
          l = wave_sum(l);
          if (lane < 2) put(stbox + ph * 16 + mem * 2 + lane, tag, __float_as_uint(lane ? l : m));
          wait1<4>(lds, W_PROG, P_K1 + ph * 8 + T_ST);
          const float st = get(stbox + ph * 16 + (lane & 15), tag);
          const float oth = dpp<XOR1>(st), mj = (lane & 1) ? oth : st, lj = (lane & 1) ? st : oth;
          float M = fmaxf(mj, dpp<ROR2>(mj)); M = fmaxf(M, dpp<ROR4>(M)); M = fmaxf(M, dpp<ROR8>(M));
          float Ls = lj * __builtin_amdgcn_exp2f((mj - M) * L2E);
          Ls += dpp<ROR2>(Ls); Ls += dpp<ROR4>(Ls); Ls += dpp<ROR8>(Ls);
          const float inv = __builtin_amdgcn_rcpf(__uint_as_float(__builtin_amdgcn_readfirstlane(__float_as_uint(Ls))));
          unsigned p[8];
#pragma unroll
          for (int i = 0; i < 8; ++i) { const unsigned t = bfbits(__builtin_amdgcn_exp2f((s[i] - M) * L2E) * inv); p[i] = t | (t << 16); }
          *(LAS v4u*)(lds + L_PP + ph * 2048 + lane * 32) = (v4u){p[0], p[1], p[2], p[3]};
          *(LAS v4u*)(lds + L_PP + ph * 2048 + lane * 32 + 16) = (v4u){p[4], p[5], p[6], p[7]};
          post(lds, W_P, ph + 1, lane);
        }
      } else {
        unsigned at = t0 * 16u + CP0, plan = fin0;
        float prev = 0.f;
        int slope = 0, vs = 0;
        const unsigned half = (unsigned)__builtin_amdgcn_s_getreg(20 | (2 << 6) | (0 << 11));
#pragma unroll 1
        for (int c = 0; c < NCP; ++c) {
          at = __builtin_amdgcn_readfirstlane(at);
          until(at);
          const v4u e = *(volatile LAS v4u*)(lds + L_FL + W_EST * 4), bg = *(volatile LAS v4u*)(lds + L_FL + W_GROW * 4);
          const unsigned mine = e.x + (unsigned)(((int)(e.y - e.x) + (int)(e.z - e.x) + (int)(e.w - e.x)) >> 2);
          const int grow = min(max((int)(bg.x + bg.y + bg.z + bg.w) >> 5, -128), 127) + 128;
          gu64* cp = (gu64*)(ws + WS_CP) + c * 256;
          if (lane == 0 && (mem & 3) == 1) put(cp + (w >> 5) * 8 + (w & 7), tag, ((mine >> 2) << 10) | (grow << 2) | half | (peek(lds, W_P) ? 2u : 0u));
          unsigned look = at + (c == 3 ? CPW3 : c ? CPW : CPW0);
          u64 x; bool ok;
#pragma unroll 1
          for (int tr = c ? 2 : 0;; ++tr) {
            until(look);
            x = __hip_atomic_load(cp + lane, RLX_AGENT);
            ok = (unsigned)(x >> 32) == tag;
            if (tr == 2 || __all(ok)) break;
            look += CPW;
          }
          if (w == 0 && c == NCP - 1 && __all(ok && (x & 2))) {
            if (lane == 0) __hip_atomic_store((GAS unsigned*)(ws + WS_TAG), tag, RLX_AGENT);
            post(lds, W_MOVED, 1, lane);
          }
          const bool up = ok && (x & 1);
          const float sum = ok ? (float)(((int)((unsigned)x - ((fin0 >> 2) << 10)) >> 10) * 4) : 0.f;
          const float sa = wave_sum(sum), su = wave_sum(up ? sum : 0.f);
          const float na = (float)__builtin_popcountll(__builtin_amdgcn_ballot_w64(ok)), nu = (float)__builtin_popcountll(__builtin_amdgcn_ballot_w64(up));
          const float mean = sa * __builtin_amdgcn_rcpf(fmaxf(na, 1.f));
          const float hi = fmaxf(nu > 0.f ? su * __builtin_amdgcn_rcpf(nu) : mean, na > nu ? (sa - su) * __builtin_amdgcn_rcpf(na - nu) : mean);
          const float est = fmaxf(mean, hi - 0.015f * (float)(int)(plan - at));
          const bool first = c < 2;
          const unsigned before = plan;
          const float worst = wave_max(ok ? sum : -1e9f) - 600.f;
          if (first || __builtin_amdgcn_readfirstlane(est - (float)(int)(plan - fin0) > 100.f))
            plan = fin0 + __builtin_amdgcn_readfirstlane((int)fminf(fmaxf(
                first ? est + 80.f : est + (c == 3 ? 1.f : 0.5f) * fminf(fmaxf(est - prev, 0.f), 600.f) + 100.f, -8000.f), 16000.f));
          const bool hint = c == 1 && __builtin_amdgcn_readfirstlane(est - prev > 580.f);
          prev = est;
          if (!first) plan += __builtin_amdgcn_readfirstlane((int)(0.25f * fminf(fmaxf(worst - (float)(int)(plan - fin0), 0.f), 4000.f)));
          if (first) {
            const v4u e2 = *(volatile LAS v4u*)(lds + L_FL + W_EST * 4);
            const int own = min(min((int)(e2.x - plan), (int)(e2.y - plan)), min((int)(e2.z - plan), (int)(e2.w - plan)));
            const float left = (float)((int)(plan - look) + own - 2400) * (1.f / TAU) - (float)LEND;
            slope = (int)(-(float)own * __builtin_amdgcn_rcpf(fmaxf(left, 8.f)));
          }
          at = (c == 3 ? before : plan) - (c == 0 ? CP1 : c == 1 ? CP2 : c == 2 ? CP3 : CP4);
          if (c == 3 && !vs && __builtin_amdgcn_readfirstlane(wave_sum(ok ? (float)((int)(((unsigned)x >> 2) & 255u) - 128) : 0.f) > 7.5f * na)
              && (int)(before + 1000u - plan) > 0) plan = before + 1000u;
          if (hint) { vs = 1; plan += 16 * VSLOW; prev += (float)(16 * VSLOW); }
          if (lane == 0) *(volatile LAS v2u*)(lds + L_FL + W_SLOPE * 4) = (v2u){(unsigned)(slope * 2 + vs), plan};
        }
      }
      waitn<4, 4>(lds, W_PROG, P_V1 + 4);
      const LAS float* pr = (const LAS float*)(lds + L_PART) + ot;
      LAS u16* odst = (LAS u16*)(lds + L_QO) + (2 + (lane >> 5)) * DH + wave * 32 + (lane & 31);
      float s0 = 0.f;
#pragma unroll
      for (int r = 0; r < 8; ++r) s0 += pr[r * DH];
      unsigned oout = WS_O + ((h * 8 + mem) * DH + ot) * 16;
      unsigned oin = WS_O + ((hc * 8 + (lane >> 5) * 4) * DH + (mem & 1) * 96 + wave * 32 + (lane & 31)) * 16;
      asm volatile("" : "+v"(s0), "+v"(oout), "+v"(oin), "+v"(odst), "+v"(pr));
      wait1<2>(lds, W_PROG + 3, P_OUT);
      waitn<0, 4>(lds, W_PROG, P_DONE);
      const float s1 = ((pr[8 * DH] + pr[9 * DH]) + (pr[10 * DH] + pr[11 * DH])) + ((pr[12 * DH] + pr[13 * DH]) + (pr[14 * DH] + pr[15 * DH]));
      put2(wsr, oout, tag, s1, s0);
      __builtin_amdgcn_s_sleep(9);
      until(peek(lds, W_PLAN) + T_OG);
      float r0, r1;
      get2<4, 0>(wsr, oin, DH * 16, tag, r0, r1);
      auto t = __builtin_amdgcn_permlane32_swap(__float_as_uint(r0), __float_as_uint(r1), false, false);
      *odst = (u16)bfbits(__uint_as_float(t[0]) + __uint_as_float(t[1]));
      BAR();
      NO_LOADS();
      return;
    }
  }
  NO_LOADS();
  const int oline = (w >> 6) * 8 + (w & 7), ob = oline >= 10, ocol = (oline - 10 * ob) * 64 + mem * 8;
  const int oi = ob * D + ocol + (lane & 7);
  float xres = 0.f;
  if (wave == 3) {
    const unsigned lds0 = (unsigned)(size_t)lds;
    const RowMap rm = row_map(lane);
    wait1<8>(lds, W_HID, 1);
    wait1<8>(lds, W_PROG, P_V1 + 2);
    warm_code(3);
    if (oline < 20) xres = __uint_as_float((unsigned)((const GAS u16*)x_)[oi] << 16);
    asm volatile("" : "+v"(xres));
    wait1<2>(lds, W_PROG + 3, P_OUT - 1);
    const unsigned plan = *(volatile LAS unsigned*)(lds + L_FL + W_PLAN * 4), own = *(volatile LAS unsigned*)(lds + L_FL + (W_EST + 3) * 4);
    unsigned due = __builtin_amdgcn_readfirstlane(plan + min(max((int)(own - plan), 0), 1000)) + WO0, tau = ffn ? TAU_WO : TAU_WO * 4 / 3;
    const unsigned dec = ffn ? DEC_WO : DEC_WO * 2;
    until(__builtin_amdgcn_readfirstlane(own) + WO0);
    if (ffn) stage_rows((gptr)wd_ + ((fb * 64 + (mem & 1) * 32) * (D * 2) + (mem >> 1) * 320), lds0 + L_WD, 2, rm, due, tau, dec);
    stage_rows((gptr)wo_ + ((hc * DH + (mem & 1) * 96) * (D * 2) + (mem >> 1) * 320), lds0 + L_WO, 6, rm, due, tau, dec);
  }
  const int tt = wave - 3;
  const LAS char* ap = lds + L_QO + 768 + ((lane >> 2) & 1) * 384 + (lane >> 4) * 16;
  const LAS char* bp = lds + L_WO + ((lane >> 4) * 8 + ((lane >> 2) & 3)) * 320 + tt * 64 + (lane & 3) * 8;
  const LAS char* hp = lds + L_HB + ((lane >> 2) & 1) * 128 + (mem & 1) * 64 + (lane >> 4) * 16;
  unsigned ydst = WS_Y + (((((mem >> 1) * 5 + tt) * 4 + (lane >> 4)) * 64 + hc * 2 + (mem & 1)) * 16 + (lane & 15)) * 16;
  asm volatile("s_waitcnt vmcnt(10)" : "+v"(bp), "+v"(hp), "+v"(ap), "+v"(ydst) :: "memory");
  if (wave == 3) post(lds, W_STAGED, 1, lane);
  wait1<0>(lds, W_STAGED, 1);
  v4u bf[6];
#pragma unroll
  for (int i = 0; i < 4; ++i) bf[i] = tr_frag(bp + (i >> 1) * 10240 + (i & 1) * 32);
  const f4 zero = {0.f, 0.f, 0.f, 0.f};
  const v4u hf = *(const LAS v4u*)hp;
  const f4 g0 = mfma(hf, tr_frag(bp + L_WD), zero), g1 = mfma(hf, tr_frag(bp + L_WD + 32), zero);
  float gx = !ffn ? 0.f : lane < 32 ? g0.x : g1.x;
  asm volatile("s_waitcnt vmcnt(0)" : "+v"(ap), "+v"(ydst), "+v"(gx), "+v"(bf[0]), "+v"(bf[1]), "+v"(bf[2]), "+v"(bf[3]) :: "memory");
  BAR();
  {
    v4u a[3];
#pragma unroll
    for (int ks = 0; ks < 3; ++ks) a[ks] = *(const LAS v4u*)(ap + ks * 64);
    bf[4] = tr_frag(bp + 20480); bf[5] = tr_frag(bp + 20480 + 32);
    SB();
    f4 y0 = zero, y1 = zero;
#pragma unroll
    for (int ks = 0; ks < 3; ++ks) { y0 = mfma(a[ks], bf[2 * ks], y0); y1 = mfma(a[ks], bf[2 * ks + 1], y1); }
    put2(wsr, ydst, tag, lane < 32 ? y0.x : y1.x, gx);
  }
  if (wave == 3 && oline < 20) {
    unsigned ysrc = WS_Y + ((ocol >> 4) * 2 + ob) * (64 * 16 * 16) + (mem & 1) * 128 + (lane >> 3) * 256 + (lane & 7) * 16;
    GAS u16* op = (GAS u16*)out_ + oi;
    asm volatile("" : "+v"(ysrc), "+v"(op));
    const unsigned moved = peek(lds, W_MOVED);
    __builtin_amdgcn_s_sleep(6);
    until(peek(lds, W_PLAN) + T_YG);
    float s, gs;
    get2<8, 0>(wsr, ysrc, 2048, tag, s, gs);
    s += dpp<ROR8>(s); gs += dpp<ROR8>(gs);
    s = rbf(xrow_sum(s)); gs = rbf(xrow_sum(gs));
    const float gate = rbf(gs * __builtin_amdgcn_rcpf(1.f + __expf(-gs)));
    if (lane < 8) *op = (u16)bfbits(xres + rbf(s * gate));
    if (w == 0 && !moved && lane == 0) __hip_atomic_store((GAS unsigned*)(ws + WS_TAG), tag, RLX_AGENT);
  }
  asm volatile(".Lfd_last:");
}

extern "C" void kernel_launch(void* const* d_in, const int* in_sizes, int n_in, void* d_out, int out_size, void* d_ws, size_t ws_size,
                              hipStream_t stream) {
  fused_decode<<<256, NTHREADS, 0, stream>>>((const u16*)d_in[0], (const u16*)d_in[1], (const u16*)d_in[2], (const u16*)d_in[3],
                                             (const u16*)d_in[4], (const u16*)d_in[6], (const u16*)d_in[7], (const u16*)d_in[8],
                                             (u16*)d_out, (char*)d_ws);
}
```

```cpp
#include <hip/hip_runtime.h>
#include <stdint.h>

typedef unsigned short u16;
typedef unsigned long long u64;
typedef unsigned v4u __attribute__((ext_vector_type(4)));
typedef unsigned v2u __attribute__((ext_vector_type(2)));
typedef float f4 __attribute__((ext_vector_type(4)));
typedef short s4 __attribute__((ext_vector_type(4)));
typedef __bf16 bf16x2 __attribute__((ext_vector_type(2)));
typedef __bf16 bf16x8 __attribute__((ext_vector_type(8)));
#define GAS __attribute__((address_space(1)))
#define LAS __attribute__((address_space(3)))
#define RLX_AGENT __ATOMIC_RELAXED, __HIP_MEMORY_SCOPE_AGENT
typedef const GAS char* gptr;
typedef GAS u64 gu64;
typedef __amdgpu_buffer_rsrc_t rsrc_t;

constexpr int D = 640, H = 32, SK = 4096, DH = 192, F = 1536, NTHREADS = 512;
constexpr int STEP = 32 * 3072;
constexpr int NP = 8;
constexpr unsigned SPIN_CAP = 1u << 16;

constexpr int P_K1 = 10, P_V1 = 26, P_OUT = 32, P_DONE = 34;
constexpr int T_Q = 2, T_ST = 4, T_H = 22;

constexpr int TAU = 650;
constexpr int ORG = 2200, OM = 1300;
constexpr int LEAD = 118;
constexpr int WBASE = 24, WSH = 8, WMAX = 10;
constexpr int NCP = 5;
constexpr int CP0 = 34400, CP1 = 28000, CP2 = 21200, CP3 = 16100, CP4 = 10400;
constexpr int CPW0 = 4400, CPW = 2400, CPW3 = 2000;
constexpr int LEND = 13, MUL = 23, FAST = 100, VSLOW = 58;
constexpr int WO0 = 650, TAU_WO = 410, DEC_WO = 57;
constexpr int T_OG = 3150, T_YG = 4930;

constexpr int WS_TAG = 0;
constexpr int WS_CP = 4096;
constexpr int WS_ST = WS_CP + 8 * 256 * 8;
constexpr int WS_P = WS_ST + H * 2 * 16 * 8;
constexpr int WS_O = WS_P + H * 16 * 64 * 16;
constexpr int WS_Y = WS_O + H * 8 * DH * 16;
constexpr int WS_END = WS_Y + 80 * 64 * 16 * 16;
constexpr int L_WO = 0;
constexpr int L_WD = 30720;
constexpr int L_XX = L_WD + 10240;
constexpr int L_PART = L_XX + 5120;
constexpr int L_QO = L_PART + 12288;
constexpr int L_HB = L_QO + 1536;
constexpr int L_SC = L_HB + 256;
constexpr int L_PP = L_SC + 4096;
constexpr int L_FL = L_PP + 4096;
constexpr int L_PJ = L_FL + 160;
constexpr int L_TOTAL = 84 * 1024;
constexpr int W_PROG = 0, W_PJ = 4, W_LN = 8, W_SLOPE = 10, W_PLAN = 11, W_EST = 12, W_P = 16, W_STAGED = 17, W_HID = 18, W_MOVED = 19,
              W_Q = 20, W_GROW = 24, W_DUMP = 32;

__device__ __forceinline__ float rbf(float f) { return (float)(__bf16)f; }
__device__ __forceinline__ unsigned bfbits(float f) { return (unsigned)__builtin_bit_cast(u16, (__bf16)f); }
__device__ __forceinline__ float dot2(unsigned a, unsigned b, float acc) {
  return __builtin_amdgcn_fdot2_f32_bf16(__builtin_bit_cast(bf16x2, a), __builtin_bit_cast(bf16x2, b), acc, false);
}
__device__ __forceinline__ float dot8(v4u a, v4u b, float acc) {
  return dot2(a.w, b.w, dot2(a.z, b.z, dot2(a.y, b.y, dot2(a.x, b.x, acc))));
}
__device__ __forceinline__ f4 mfma(v4u a, v4u b, f4 c) {
  return __builtin_amdgcn_mfma_f32_16x16x32_bf16(__builtin_bit_cast(bf16x8, a), __builtin_bit_cast(bf16x8, b), c, 0, 0, 0);
}
template <int CTRL> __device__ __forceinline__ float dpp(float x) {
  return __builtin_bit_cast(float, __builtin_amdgcn_mov_dpp(__builtin_bit_cast(int, x), CTRL, 0xf, 0xf, true));
}
constexpr int XOR1 = 0xB1, XOR2 = 0x4E, HMIR = 0x141, ROR2 = 0x122, ROR4 = 0x124, ROR8 = 0x128;
__device__ __forceinline__ float sum8(float x) { x += dpp<XOR1>(x); x += dpp<XOR2>(x); return x + dpp<HMIR>(x); }
__device__ __forceinline__ float xrow_sum(float x) {
  auto s = __builtin_amdgcn_permlane16_swap(__float_as_uint(x), __float_as_uint(x), false, false);
  x = __uint_as_float(s[0]) + __uint_as_float(s[1]);
  auto t = __builtin_amdgcn_permlane32_swap(__float_as_uint(x), __float_as_uint(x), false, false);
  return __uint_as_float(t[0]) + __uint_as_float(t[1]);
}
__device__ __forceinline__ float wave_sum(float x) { x = sum8(x); return xrow_sum(x + dpp<ROR8>(x)); }
__device__ __forceinline__ float wave_max(float x) {
  x = fmaxf(x, dpp<XOR1>(x)); x = fmaxf(x, dpp<XOR2>(x)); x = fmaxf(x, dpp<HMIR>(x)); x = fmaxf(x, dpp<ROR8>(x));
  auto s = __builtin_amdgcn_permlane16_swap(__float_as_uint(x), __float_as_uint(x), false, false);
  x = fmaxf(__uint_as_float(s[0]), __uint_as_float(s[1]));
  auto t = __builtin_amdgcn_permlane32_swap(__float_as_uint(x), __float_as_uint(x), false, false);
  return fmaxf(__uint_as_float(t[0]), __uint_as_float(t[1]));
}
__device__ __forceinline__ float fold(float a, float b) {
  auto t = __builtin_amdgcn_permlane32_swap(__float_as_uint(a), __float_as_uint(b), false, false);
  return __uint_as_float(t[0]) + dpp<ROR8>(__uint_as_float(t[1]));
}
__device__ __forceinline__ rsrc_t mk_rsrc(const void* p, int bytes) { return __builtin_amdgcn_make_buffer_rsrc((void*)p, 0, bytes, 0x00020000); }
__device__ __forceinline__ v4u ld_nt(rsrc_t r, unsigned voff, unsigned soff) { return __builtin_amdgcn_raw_buffer_load_b128(r, voff, soff, 2); }
#define SB() __builtin_amdgcn_sched_barrier(0)
#define BAR() asm volatile("s_waitcnt lgkmcnt(0)\n\ts_barrier" ::: "memory")
#define NO_LOADS() __builtin_amdgcn_s_waitcnt(0x0F70)

__device__ __forceinline__ void put(gu64* g, unsigned tag, unsigned v) { __hip_atomic_store(g, ((u64)tag << 32) | v, RLX_AGENT); }
__device__ __forceinline__ void put2(rsrc_t r, unsigned off, unsigned tag, float a, float b) {
  __builtin_amdgcn_raw_buffer_store_b128((v4u){__float_as_uint(a), tag, __float_as_uint(b), tag}, r, off, 0, 16);
}
__device__ __forceinline__ float get(gu64* g, unsigned tag) {
#pragma unroll 1
  for (unsigned spins = 0; spins < SPIN_CAP; ++spins) {
    const u64 x = __hip_atomic_load(g, RLX_AGENT);
    if (__all((unsigned)(x >> 32) == tag)) return __uint_as_float((unsigned)x);
    __builtin_amdgcn_s_sleep(16);
  }
  return __builtin_nanf("");
}
template <int N, int SLEEP> __device__ __forceinline__ void get2(rsrc_t r, unsigned off, int stride, unsigned tag, float& a, float& b) {
#pragma unroll 1
  for (unsigned spins = 0; spins < SPIN_CAP; ++spins) {
    asm volatile("" ::: "memory");
    v4u x[N];
#pragma unroll
    for (int k = 0; k < N; ++k) x[k] = __builtin_amdgcn_raw_buffer_load_b128(r, off, k * stride, 16);
    unsigned lo = tag, hi = tag;
#pragma unroll
    for (int k = 0; k < N; ++k) {
      lo = min(min(lo, x[k].y), x[k].w); hi = max(max(hi, x[k].y), x[k].w);
      a = k ? a + __uint_as_float(x[k].x) : __uint_as_float(x[k].x);
      b = k ? b + __uint_as_float(x[k].z) : __uint_as_float(x[k].z);
      asm volatile("" : "+v"(a), "+v"(b), "+v"(lo), "+v"(hi));
    }
    if (__builtin_amdgcn_ballot_w64(lo != hi) == 0) return;
    if (SLEEP) __builtin_amdgcn_s_sleep(SLEEP);
  }
  a = b = __builtin_nanf("");
}
__device__ __forceinline__ void post(LAS char* lds, int word, unsigned v, int lane) {
  asm volatile("" ::: "memory");
  if (lane == 0) *(volatile LAS unsigned*)(lds + L_FL + word * 4) = v;
  asm volatile("" ::: "memory");
}
__device__ __forceinline__ unsigned peek(LAS char* lds, int word) {
  return __builtin_amdgcn_readfirstlane(*(volatile LAS unsigned*)(lds + L_FL + word * 4));
}
template <int SLEEP> __device__ __forceinline__ void wait1(LAS char* lds, int word, unsigned want) {
  while (peek(lds, word) < want) { if (SLEEP) __builtin_amdgcn_s_sleep(SLEEP); }
  asm volatile("" ::: "memory");
}
template <int SLEEP, int N> __device__ __forceinline__ void waitn(LAS char* lds, int word, unsigned want) {
  for (;;) {
    const v4u v = *(volatile LAS v4u*)(lds + L_FL + word * 4);
    unsigned m = min(v.x, v.y);
    if (N == 4) m = min(m, min(v.z, v.w));
    if (__builtin_amdgcn_readfirstlane(m) >= want) break;
    if (SLEEP) __builtin_amdgcn_s_sleep(SLEEP);
  }
  asm volatile("" ::: "memory");
}
__device__ __forceinline__ int until(unsigned due) {
  int late;
  asm volatile("s_branch 2f\n1:\n\ts_sleep 1\n2:\n\ts_memrealtime vcc\n\ts_waitcnt lgkmcnt(0)\n\ts_lshl_b32 %0, vcc_lo, 4\n\t"
               "s_sub_i32 %0, %0, %1\n\ts_cmp_lt_i32 %0, 0\n\ts_cbranch_scc1 1b" : "=&s"(late) : "s"(due) : "vcc", "scc", "memory");
  return late;
}
struct RowMap { unsigned o[5]; };
__device__ __forceinline__ RowMap row_map(int lane) {
  RowMap m;
#pragma unroll
  for (int i = 0; i < 5; ++i) { const unsigned ci = i * 64 + lane, row = (ci * 52429u) >> 20; m.o[i] = row * (D * 2 - 320) + ci * 16; }
  asm volatile("" : "+v"(m.o[0]), "+v"(m.o[1]), "+v"(m.o[2]), "+v"(m.o[3]), "+v"(m.o[4]));
  return m;
}
__device__ __forceinline__ void stage_rows(gptr src, unsigned lds_dst, int ng, const RowMap& m, unsigned& due, unsigned& tau, unsigned dec) {
#pragma unroll 1
  for (int g = 0; g < ng; ++g) {
    unsigned keep;
    until(due); due += tau; tau -= dec;
    asm volatile("s_mov_b32 %0, m0\n\ts_mov_b32 m0, %7\n\ts_nop 0\n\tglobal_load_lds_dwordx4 %1, %6\n\t"
                 "s_add_u32 m0, m0, 0x400\n\ts_nop 0\n\tglobal_load_lds_dwordx4 %2, %6\n\t"
                 "s_add_u32 m0, m0, 0x400\n\ts_nop 0\n\tglobal_load_lds_dwordx4 %3, %6\n\t"
                 "s_add_u32 m0, m0, 0x400\n\ts_nop 0\n\tglobal_load_lds_dwordx4 %4, %6\n\t"
                 "s_add_u32 m0, m0, 0x400\n\ts_nop 0\n\tglobal_load_lds_dwordx4 %5, %6\n\ts_mov_b32 m0, %0"
                 : "=&s"(keep) : "v"(m.o[0]), "v"(m.o[1]), "v"(m.o[2]), "v"(m.o[3]), "v"(m.o[4]), "s"(src), "s"(lds_dst) : "scc", "memory");
    src += 16 * D * 2; lds_dst += 5120;
  }
}
__device__ __forceinline__ v4u tr_frag(const LAS char* p) {
  const v2u b0 = __builtin_bit_cast(v2u, __builtin_amdgcn_ds_read_tr16_b64_v4i16((LAS s4*)p));
  const v2u b1 = __builtin_bit_cast(v2u, __builtin_amdgcn_ds_read_tr16_b64_v4i16((LAS s4*)(p + 1280)));
  return (v4u){b0.x, b0.y, b1.x, b1.y};
}

extern "C" __global__ void __launch_bounds__(512, 2)
fused_decode(const u16* x_, const u16* kc_, const u16* vc_, const u16* wq_, const u16* wo_, const u16* wg_, const u16* wu_,
             const u16* wd_, u16* out_, char* ws_) {
  unsigned ka;
  asm volatile("s_load_dword %0, %1, 0x40" : "=&s"(ka) : "s"(__builtin_amdgcn_kernarg_segment_ptr()));
  SB();
  __shared__ __attribute__((aligned(16))) char lds_raw[L_TOTAL];
  LAS char* lds = (LAS char*)lds_raw;
  GAS char* ws = (GAS char*)ws_;
  const int tid = threadIdx.x, lane = tid & 63, wave = __builtin_amdgcn_readfirstlane(tid >> 6);
  const int w = blockIdx.x, h = (w & 7) * 4 + (w >> 6), mem = (w >> 3) & 7;
  const int ww = wave - 4;
  const int hc = (h + 4) & 31;
  u64 code0;
  {
    u64 pc; unsigned back;
    asm volatile("s_getpc_b64 %0\n.Lfd_here:\n\ts_mov_b32 %1, .Lfd_here-fused_decode" : "=s"(pc), "=s"(back));
    code0 = pc - back;
  }
  auto warm_code = [&](int who) {
    u64 ex; unsigned keep;
    const unsigned c0 = (unsigned)(tid - who * 64) * 4096u + (w >> 3) * 128u;
    asm volatile("v_cmp_gt_u32 vcc, .Lfd_last-fused_decode, %2\n\ts_and_saveexec_b64 %0, vcc\n\ts_cbranch_execz 1f\n\t"
                 "s_mov_b32 %1, m0\n\ts_mov_b32 m0, %4\n\ts_nop 0\n\tglobal_load_lds_dword %2, %3\n\ts_mov_b32 m0, %1\n"
                 "1:\n\ts_mov_b64 exec, %0"
                 : "=&s"(ex), "=&s"(keep) : "v"(c0), "s"(code0), "s"((unsigned)(size_t)lds + L_FL + W_DUMP * 4) : "vcc", "scc", "memory");
  };
  warm_code(0);
  const bool ffn = w >= 64, isq = mem < 6;
  const int fb = (w & 7) * 3 + (w >> 6) - 1;
  const int row0 = (mem & 1) * 320 + (lane >> 3), qm = (mem - 6) >> 31;
  unsigned rs8 = isq ? 8 * H * DH * 2 : 8 * F * 2;
  int jobw = wave + ((w >> 4) == 3 ? 4 : 0);
  unsigned woff = (row0 + wave * 80) * (rs8 >> 3) + (((h * 3 + (mem >> 1)) & qm) | (fb & ~qm)) * 128 + (lane & 7) * 16;
  unsigned xoff = (wave & 1) * (D * 2) + lane * 4;
  asm volatile("" : "+s"(rs8), "+s"(jobw));
  asm volatile("" : "+v"(woff), "+v"(xoff));
  SB();
  const bool job = jobw < 4;
  unsigned xr[5];
  v4u R[10];
  if (job) {
#pragma unroll
    for (int i = 0; i < 5; ++i) xr[i] = *(const GAS unsigned*)((gptr)x_ + xoff + 256 * i);
    SB();
    const rsrc_t wr = mk_rsrc(isq ? wq_ : wg_, 80 * rs8);
    unsigned so = 0;
#pragma unroll
    for (int i = 0; i < 10; ++i) { R[i] = ld_nt(wr, woff, so); so += rs8; SB(); }
  }
  if ((w >> 3) == 7 && tid < 16) {
    const unsigned g = lane >> 2, hg = (w & 7) * 4 + g, e = lane & 1;
    const unsigned okv = (((lane >> 1) & 1) * H + hg) * (SK * DH * 2) + e * (SK * DH * 2 - 4), owo = ((hg + 4) & 31) * (DH * D * 2) + e * (DH * D * 2 - 4);
    const unsigned off = lane * (D * F * 2 / 16), ows = (e ? WS_END : 2048) + (w & 7) * 128, oo = (w & 7) * 128;
    unsigned keep;
    asm volatile("s_mov_b32 %0, m0\n\ts_mov_b32 m0, %13\n\ts_nop 0\n\tglobal_load_lds_dword %1, %6 nt\n\tglobal_load_lds_dword %3, %9 nt\n\t"
                 "global_load_lds_dword %4, %11 nt\n\tglobal_load_lds_dword %1, %7 nt\n\tglobal_load_lds_dword %2, %8 nt\n\t"
                 "global_load_lds_dword %3, %10 nt\n\tglobal_load_lds_dword %5, %12 nt\n\ts_mov_b32 m0, %0"
                 : "=&s"(keep) : "v"(okv), "v"(owo), "v"(off), "v"(ows), "v"(oo), "s"(kc_), "s"(vc_), "s"(wo_), "s"(wu_), "s"(wd_), "s"(ws_), "s"(out_),
                   "s"((unsigned)(size_t)lds + L_PP) : "memory");
  }
  asm volatile("" :: "s"(x_), "s"(ws_) : "memory");
  const unsigned t0 = (unsigned)__builtin_amdgcn_s_memrealtime();
  const rsrc_t wsr = mk_rsrc(ws_, WS_END);
  unsigned tag;
  v4u msk;
  auto meet = [&]() {
    tag = *(const __attribute__((address_space(4))) unsigned*)(ws_ + WS_TAG) + 1u;
    const unsigned mm = (lane & 1) ? 0xffff0000u : 0x0000ffffu;
    const int q = (lane & 7) >> 1;
    msk = (v4u){q == 0 ? mm : 0u, q == 1 ? mm : 0u, q == 2 ? mm : 0u, q == 3 ? mm : 0u};
    asm volatile("" : "+s"(tag) : "s"(ka));
    asm volatile("" : "+v"(msk));
    BAR();
  };

  if (wave >= 4) {
    NO_LOADS();
    unsigned fin0 = t0 * 16u + (ORG + 63 * TAU);
    if (tid < 284) *(volatile LAS unsigned*)(lds + L_FL + lane * 4) = lane < W_PLAN || lane >= W_P ? 0u : fin0;
    meet();
    fin0 += max((int)(((unsigned)__builtin_amdgcn_s_memrealtime() - t0) * 16u) + OM - ORG, 0);
    if (ww == 0) post(lds, W_PLAN, fin0, lane);
    __builtin_amdgcn_s_setprio(2);
    constexpr int HSTR = SK * DH * 2, BSTR = H * HSTR;
    const rsrc_t kr = mk_rsrc((const char*)kc_ + (size_t)h * HSTR, BSTR + HSTR), vr = mk_rsrc((const char*)vc_ + (size_t)h * HSTR, BSTR + HSTR);
    auto group_off = [&](int g) { return ((g & 4) ? 0 : BSTR) + ((g + h) & 3) * (4 * STEP); };
    unsigned loff = (mem * 4 + ww) * 3072 + (lane >> 3) * 384 + (lane & 7) * 16;
    LAS char* flw = lds + L_FL + (W_PROG + ww) * 4;
    const LAS char* qp = lds + L_QO + (lane & 7) * 16;
    LAS float* sc = (LAS float*)(lds + L_SC) + 256 + ww * 8 + (lane >> 3) + (lane & 3) * 32 - (lane & 4) * 64;
    LAS char* part = lds + L_PART + ((ww * 2 + (lane >> 5)) * DH + (lane & 7) * 8 + ((lane >> 4) & 1) * 4) * 4;
    const LAS unsigned* pp = (const LAS unsigned*)(lds + L_PP) + ww * 8 + (lane >> 3);
    asm volatile("" : "+v"(loff), "+v"(flw), "+v"(qp), "+v"(sc), "+v"(part), "+v"(pp));
    unsigned due = fin0 - 63 * TAU + ww * (TAU / 4), fin = fin0;
    int adj = 0, step = TAU, slow = 0, late = 0, win;
    auto pace = [&](int lead = 0) {
      late = until(due - lead) - lead; due += step; fin += adj;
      win = min((unsigned)(late + ((WBASE - 4 + ww) << WSH)) >> (WSH + 2), WMAX - 1u);
      if (lead && late < 0) { win = 4; late = 0; }
      SB();
    };
    auto report = [&](unsigned prog) {
      asm volatile("s_mov_b64 exec, 1\n\tds_write2_b32 %0, %1, %2 offset1:12\n\ts_mov_b64 exec, -1" :: "v"(flw), "v"(prog), "v"(fin + late) : "memory");
    };
    auto gauge = [&]() {
      if (late >= 300) return late + 2 * TAU;
      asm volatile("s_waitcnt vmcnt(3)" ::: "memory");
      return (int)((unsigned)__builtin_amdgcn_s_memrealtime() * 16u - due) + 2 * step;
    };
    int back0 = 0;
    auto steer = [&](v2u pl, int g) {
      const int left = 61 - LEND - 4 * g, mul = g < 13 ? MUL : g == 13 ? 28 : g == 14 ? 51 : 256;
      const int sw = __builtin_amdgcn_readfirstlane(pl.x), sl = (sw & 1) * VSLOW;
      fin += 16 * (sl - slow); slow = sl;
      const int slope = left > 0 ? sw >> 1 : 0, gap = (int)(pl.y - fin) - slope * left;
      adj = __builtin_amdgcn_readfirstlane(min(max(slope + ((gap * mul) >> 8), -FAST), TAU / 2));
      step = TAU + adj + ((unsigned)(g - 7) < 4u ? slow : 0);
    };
    auto fetch = [&](rsrc_t r, int so, int j) {
      unsigned cnt;
      asm volatile("1:\n\ts_getreg_b32 %0, hwreg(HW_REG_IB_STS, 0, 4)\n\ts_cmp_gt_u32 %0, %1\n\ts_cbranch_scc1 1b" : "=&s"(cnt) : "s"(win) : "scc", "memory");
      const v4u x = ld_nt(r, loff + j * 128, so);
      SB();
      return x;
    };
    v4u U[NP + 4][3];
#pragma unroll
    for (int u = 0; u < NP + 4; ++u) {
      pace((NP + 3 - u) * LEAD);
#pragma unroll
      for (int j = 0; j < 3; ++j) U[u][j] = fetch(kr, group_off(u >> 2) + (u & 3) * STEP, j);
      if (u & 1) report(u >> 1);
    }
    v4u q[3], q2[3];
    for (;;) {
      const v4u f = *(volatile LAS v4u*)(lds + L_FL + W_Q * 4);
#pragma unroll
      for (int j = 0; j < 3; ++j) { q[j] = *(volatile LAS v4u*)(qp + 384 + j * 128); q2[j] = *(volatile LAS v4u*)(qp + j * 128); }
      if (__builtin_amdgcn_readfirstlane(f.x & f.y & f.z)) break;
    }
#pragma unroll 1
    for (int g = 3; g < 9; ++g) {
      const rsrc_t r = g == 8 ? vr : kr;
      const int so = group_off(g);
      float pick = 0.f;
      v2u pl;
#pragma unroll
      for (int k = 0; k < 4; ++k) {
        if (k == 1) pl = *(volatile LAS v2u*)(lds + L_FL + W_SLOPE * 4);
        pace();
        float d = 0.f;
#pragma unroll
        for (int j = 0; j < 3; ++j) { d = dot8(U[NP + k][j], q[j], d); asm volatile("" : "+v"(d)); SB(); U[NP + k][j] = fetch(r, so + k * STEP, j); }
        if (k == 1) steer(pl, g); else if (k != 3) report(2 * g + (k >> 1));
        d = sum8(d);
        pick = (lane & 7) == k ? d : pick;
        if (g < 5) {
          const float e = sum8(dot8(U[k][2], q[2], dot8(U[k][1], q[1], dot8(U[k][0], q[0], 0.f))));
          pick = (lane & 7) == 4 + k ? e : pick;
#pragma unroll
          for (int j = 0; j < 3; ++j) U[k][j] = U[4 + k][j];
        }
        asm volatile("" : "+v"(pick));
        SB();
        if (k == 0 && g == 8) back0 = gauge();
      }
      if (g < 5 || !(lane & 4)) *sc = rbf(rbf(pick) * 0.07216878364870322f);
      sc += 128;
      if (g == 4) {
#pragma unroll
        for (int j = 0; j < 3; ++j) q[j] = q2[j];
      }
      SB();
    }
    f4 o[3];
#pragma unroll
    for (int j = 0; j < 3; ++j) o[j] = (f4){0.f, 0.f, 0.f, 0.f};
    auto vstep = [&](int g, bool more) {
      const int so = group_off(g);
      unsigned f = *(volatile LAS unsigned*)(lds + L_FL + W_P * 4), pv[4];
#pragma unroll
      for (int k = 0; k < 4; ++k) pv[k] = *(volatile LAS unsigned*)(pp + k * 32);
      v2u pl;
#pragma unroll
      for (int k = 0; k < 4; ++k) {
        if (k == 1 && more) pl = *(volatile LAS v2u*)(lds + L_FL + W_SLOPE * 4);
        if (more) pace();
        if (k == 0 && more && (unsigned)__builtin_amdgcn_readfirstlane(f) < (g < 13 ? 1u : 2u)) {
          wait1<0>(lds, W_P, g < 13 ? 1u : 2u);
#pragma unroll
          for (int i = 0; i < 4; ++i) pv[i] = *(volatile LAS unsigned*)(pp + i * 32);
        }
        const v4u a = msk & pv[k];
#pragma unroll
        for (int j = 0; j < 3; ++j) { o[j] = mfma(a, U[NP + k][j], o[j]); SB(); if (more) U[NP + k][j] = fetch(vr, so + k * STEP, j); }
        if (more) {
          if (k == 1) steer(pl, g); else if (k != 3) report(2 * g + (k >> 1));
          if (k == 0 && g == 9) post(lds, W_GROW + ww, gauge() - back0, lane);
        }
        SB();
      }
      pp += 128;
    };
    auto vflush = [&](int ph) {
      if (((lane >> 3) & 1) == (lane >> 5)) {
#pragma unroll
        for (int j = 0; j < 3; ++j) *(LAS f4*)(part + ph * (8 * DH * 4) + j * 256) = o[j];
      }
    };
#pragma unroll 1
    for (int g = 9; g < 16; ++g) {
      vstep(g, true);
      if (g == 12) {
        vflush(0);
#pragma unroll
        for (int j = 0; j < 3; ++j) o[j] = (f4){0.f, 0.f, 0.f, 0.f};
      }
    }
    fin -= adj;
    report(P_OUT);
    vstep(16, false);
    vflush(1);
    report(P_DONE);
    __builtin_amdgcn_s_setprio(0);
  } else {
    meet();
    if (job) {
      float v[10], sum = 0.f;
#pragma unroll
      for (int i = 0; i < 5; ++i) { v[2 * i] = __uint_as_float(xr[i] << 16); v[2 * i + 1] = __uint_as_float(xr[i] & 0xffff0000u); sum += v[2 * i] + v[2 * i + 1]; }
      const float mean = wave_sum(sum) * (1.f / D);
      float q = 0.f;
#pragma unroll
      for (int i = 0; i < 10; ++i) { const float dd = v[i] - mean; q += dd * dd; }
      const float var = rbf(wave_sum(q) * (1.f / D)), mb = rbf(mean);
      const float inv = __builtin_amdgcn_rcpf(rbf(__builtin_amdgcn_sqrtf(rbf(var + 1e-5f))));
#pragma unroll
      for (int i = 0; i < 10; ++i) {
        const unsigned t = bfbits(rbf(v[i] - mb) * inv);
        *(LAS unsigned*)(lds + L_XX + ((lane + 64 * (i >> 1)) * 2 + (i & 1)) * 8 + (wave & 1) * 4) = t | (t << 16);
      }
      post(lds, W_LN + (wave & 1), 1, lane);
      waitn<0, 2>(lds, W_LN, 1);
#pragma unroll 1
      for (int pass = 0;; ++pass) {
        f4 a0 = {0.f, 0.f, 0.f, 0.f}, a1 = a0;
        const LAS char* xp = lds + L_XX + (pass ? mem * 80 + (lane >> 3) : row0 + wave * 80) * 8;
#pragma unroll
        for (int i = 0; i < 10; ++i) {
          const v2u xv = *(const LAS v2u*)(xp + i * 64);
          a0 = mfma(msk & xv.x, R[i], a0); a1 = mfma(msk & xv.y, R[i], a1);
        }
        const bool more = wave == 3 && ffn && pass == 0;
        if (more) {
          const rsrc_t wr = mk_rsrc(wu_, D * F * 2);
          const unsigned uoff = (mem * 80 + (lane >> 3)) * (F * 2) + fb * 128 + (lane & 7) * 16;
#pragma unroll
          for (int i = 0; i < 10; ++i) { R[i] = ld_nt(wr, uoff, i * (8 * F * 2)); SB(); }
        }
        const f4 s = {fold(a0[0], a1[0]), fold(a0[1], a1[1]), fold(a0[2], a1[2]), fold(a0[3], a1[3])};
        if ((lane & 8) == 0) *(LAS f4*)(lds + L_PJ + (wave * 2 + (lane >> 5)) * 256 + (lane & 7) * 32 + ((lane >> 4) & 1) * 16) = s;
        post(lds, W_PJ + wave, 1, lane);
        if (wave < 3) break;
        waitn<1, 4>(lds, W_PJ, 1);
        const LAS float* pr = (const LAS float*)(lds + L_PJ) + lane;
        const float c0 = pr[384], c1 = pr[448];
        put2(wsr, WS_P + ((h * 16 + mem + 8 * pass) * 64 + lane) * 16, tag,
             pass ? c0 : (pr[0] + pr[128]) + (pr[256] + c0), pass ? c1 : (pr[64] + pr[192]) + (pr[320] + c1));
        if (!more) break;
      }
    }
    if (wave < 3) {
      const int ot = wave * 64 + lane;
      {
        wait1<4>(lds, W_PROG, T_Q);
        float r0, r1;
        get2<2, 24>(wsr, WS_P + ((h * 16 + wave * 2) * 64 + lane) * 16, 1024, tag, r0, r1);
        LAS u16* dst = (LAS u16*)(lds + L_QO) + ot;
        dst[0] = (u16)bfbits(r0); dst[DH] = (u16)bfbits(r1);
        post(lds, W_Q + wave, 1, lane);
      }
      if (wave == 2) {
        if (ffn) {
          wait1<8>(lds, W_PROG, T_H);
          float gt[2], up[2];
          get2<2, 24>(wsr, WS_P + ((h * 16 + 6) * 64 + lane) * 16, 1024, tag, gt[0], gt[1]);
          get2<8, 24>(wsr, WS_P + ((h * 16 + 8) * 64 + lane) * 16, 1024, tag, up[0], up[1]);
#pragma unroll
          for (int b = 0; b < 2; ++b) {
            const float g = rbf(gt[b]), t2 = 2.f * 0.7978845608028654f * (g + 0.044715f * g * g * g);
            *(LAS u16*)(lds + L_HB + b * 128 + lane * 2) = (u16)bfbits(rbf(g * __builtin_amdgcn_rcpf(1.f + __expf(-t2))) * rbf(up[b]));
          }
        }
        post(lds, W_HID, 1, lane);
      } else if (wave == 0) {
        gu64* stbox = (gu64*)(ws + WS_ST) + h * 32;
#pragma unroll 1
        for (int ph = 0; ph < 2; ++ph) {
          waitn<4, 4>(lds, W_PROG, P_K1 + ph * 8);
          const f4 s0 = *(const LAS f4*)(lds + L_SC + ph * 2048 + lane * 32), s1 = *(const LAS f4*)(lds + L_SC + ph * 2048 + lane * 32 + 16);
          const float s[8] = {s0.x, s0.y, s0.z, s0.w, s1.x, s1.y, s1.z, s1.w};
          constexpr float L2E = 1.4426950408889634f;
          float m = s[0];
#pragma unroll
          for (int i = 1; i < 8; ++i) m = fmaxf(m, s[i]);
          m = wave_max(m);
          const float ms = m == -INFINITY ? 0.f : m;
          float l = 0.f;
#pragma unroll
          for (int i = 0; i < 8; ++i) l += __builtin_amdgcn_exp2f((s[i] - ms) * L2E);
          l = wave_sum(l);
          if (lane < 2) put(stbox + ph * 16 + mem * 2 + lane, tag, __float_as_uint(lane ? l : m));
          wait1<4>(lds, W_PROG, P_K1 + ph * 8 + T_ST);
          const float st = get(stbox + ph * 16 + (lane & 15), tag);
          const float oth = dpp<XOR1>(st), mj = (lane & 1) ? oth : st, lj = (lane & 1) ? st : oth;
          float M = fmaxf(mj, dpp<ROR2>(mj)); M = fmaxf(M, dpp<ROR4>(M)); M = fmaxf(M, dpp<ROR8>(M));
          float Ls = lj * __builtin_amdgcn_exp2f((mj - M) * L2E);
          Ls += dpp<ROR2>(Ls); Ls += dpp<ROR4>(Ls); Ls += dpp<ROR8>(Ls);
          const float inv = __builtin_amdgcn_rcpf(__uint_as_float(__builtin_amdgcn_readfirstlane(__float_as_uint(Ls))));
          unsigned p[8];
#pragma unroll
          for (int i = 0; i < 8; ++i) { const unsigned t = bfbits(__builtin_amdgcn_exp2f((s[i] - M) * L2E) * inv); p[i] = t | (t << 16); }
          *(LAS v4u*)(lds + L_PP + ph * 2048 + lane * 32) = (v4u){p[0], p[1], p[2], p[3]};
          *(LAS v4u*)(lds + L_PP + ph * 2048 + lane * 32 + 16) = (v4u){p[4], p[5], p[6], p[7]};
          post(lds, W_P, ph + 1, lane);
        }
      } else {
        const unsigned fin0 = peek(lds, W_PLAN);
        unsigned at = fin0 - CP0, plan = fin0;
        float prev = 0.f;
        int slope = 0, vs = 0;
        const unsigned half = (unsigned)__builtin_amdgcn_s_getreg(20 | (2 << 6) | (0 << 11));
#pragma unroll 1
        for (int c = 0; c < NCP; ++c) {
          at = __builtin_amdgcn_readfirstlane(at);
          until(at);
          const v4u e = *(volatile LAS v4u*)(lds + L_FL + W_EST * 4), bg = *(volatile LAS v4u*)(lds + L_FL + W_GROW * 4);
          const unsigned mine = e.x + (unsigned)(((int)(e.y - e.x) + (int)(e.z - e.x) + (int)(e.w - e.x)) >> 2);
          const int grow = min(max((int)(bg.x + bg.y + bg.z + bg.w) >> 5, -128), 127) + 128;
          gu64* cp = (gu64*)(ws + WS_CP) + c * 256;
          if (lane == 0 && (mem & 3) == 1) put(cp + (w >> 5) * 8 + (w & 7), tag, ((mine >> 2) << 10) | (grow << 2) | half | (peek(lds, W_P) ? 2u : 0u));
          unsigned look = at + (c == 3 ? CPW3 : c ? CPW : CPW0);
          u64 x; bool ok;
#pragma unroll 1
          for (int tr = c ? 2 : 0;; ++tr) {
            until(look);
            x = __hip_atomic_load(cp + lane, RLX_AGENT);
            ok = (unsigned)(x >> 32) == tag;
            if (tr == 2 || __all(ok)) break;
            look += CPW;
          }
          if (w == 0 && c == NCP - 1 && __all(ok && (x & 2))) {
            if (lane == 0) __hip_atomic_store((GAS unsigned*)(ws + WS_TAG), tag, RLX_AGENT);
            post(lds, W_MOVED, 1, lane);
          }
          const bool up = ok && (x & 1);
          const float sum = ok ? (float)(((int)((unsigned)x - ((fin0 >> 2) << 10)) >> 10) * 4) : 0.f;
          const float sa = wave_sum(sum), su = wave_sum(up ? sum : 0.f);
          const float na = (float)__builtin_popcountll(__builtin_amdgcn_ballot_w64(ok)), nu = (float)__builtin_popcountll(__builtin_amdgcn_ballot_w64(up));
          const float mean = sa * __builtin_amdgcn_rcpf(fmaxf(na, 1.f));
          const float hi = fmaxf(nu > 0.f ? su * __builtin_amdgcn_rcpf(nu) : mean, na > nu ? (sa - su) * __builtin_amdgcn_rcpf(na - nu) : mean);
          const float est = fmaxf(mean, hi - 0.015f * (float)(int)(plan - at));
          const bool first = c < 2;
          const unsigned before = plan;
          const float worst = wave_max(ok ? sum : -1e9f) - 600.f;
          if (first || __builtin_amdgcn_readfirstlane(est - (float)(int)(plan - fin0) > 100.f))
            plan = fin0 + __builtin_amdgcn_readfirstlane((int)fminf(fmaxf(
                first ? est + 80.f : est + (c == 3 ? 1.f : 0.5f) * fminf(fmaxf(est - prev, 0.f), 600.f) + 100.f, -8000.f), 16000.f));
          const bool hint = c == 1 && __builtin_amdgcn_readfirstlane(est - prev > 580.f);
          prev = est;
          if (!first) plan += __builtin_amdgcn_readfirstlane((int)(0.25f * fminf(fmaxf(worst - (float)(int)(plan - fin0), 0.f), 4000.f)));
          if (first) {
            const v4u e2 = *(volatile LAS v4u*)(lds + L_FL + W_EST * 4);
            const int own = min(min((int)(e2.x - plan), (int)(e2.y - plan)), min((int)(e2.z - plan), (int)(e2.w - plan)));
            const float left = (float)((int)(plan - look) + own - 2400) * (1.f / TAU) - (float)LEND;
            slope = (int)(-(float)own * __builtin_amdgcn_rcpf(fmaxf(left, 8.f)));
          }
          at = (c == 3 ? before : plan) - (c == 0 ? CP1 : c == 1 ? CP2 : c == 2 ? CP3 : CP4);
          if (c == 3 && !vs && __builtin_amdgcn_readfirstlane(wave_sum(ok ? (float)((int)(((unsigned)x >> 2) & 255u) - 128) : 0.f) > 7.5f * na)
              && (int)(before + 1000u - plan) > 0) plan = before + 1000u;
          if (hint) { vs = 1; plan += 16 * VSLOW; prev += (float)(16 * VSLOW); }
          if (lane == 0) *(volatile LAS v2u*)(lds + L_FL + W_SLOPE * 4) = (v2u){(unsigned)(slope * 2 + vs), plan};
        }
      }
      waitn<4, 4>(lds, W_PROG, P_V1 + 4);
      const LAS float* pr = (const LAS float*)(lds + L_PART) + ot;
      LAS u16* odst = (LAS u16*)(lds + L_QO) + (2 + (lane >> 5)) * DH + wave * 32 + (lane & 31);
      float s0 = 0.f;
#pragma unroll
      for (int r = 0; r < 8; ++r) s0 += pr[r * DH];
      unsigned oout = WS_O + ((h * 8 + mem) * DH + ot) * 16;
      unsigned oin = WS_O + ((hc * 8 + (lane >> 5) * 4) * DH + (mem & 1) * 96 + wave * 32 + (lane & 31)) * 16;
      asm volatile("" : "+v"(s0), "+v"(oout), "+v"(oin), "+v"(odst), "+v"(pr));
      wait1<2>(lds, W_PROG + 3, P_OUT);
      waitn<0, 4>(lds, W_PROG, P_DONE);
      const float s1 = ((pr[8 * DH] + pr[9 * DH]) + (pr[10 * DH] + pr[11 * DH])) + ((pr[12 * DH] + pr[13 * DH]) + (pr[14 * DH] + pr[15 * DH]));
      put2(wsr, oout, tag, s1, s0);
      __builtin_amdgcn_s_sleep(9);
      until(peek(lds, W_PLAN) + T_OG);
      float r0, r1;
      get2<4, 0>(wsr, oin, DH * 16, tag, r0, r1);
      auto t = __builtin_amdgcn_permlane32_swap(__float_as_uint(r0), __float_as_uint(r1), false, false);
      *odst = (u16)bfbits(__uint_as_float(t[0]) + __uint_as_float(t[1]));
      BAR();
      NO_LOADS();
      return;
    }
  }
  NO_LOADS();
  const int oline = (w >> 6) * 8 + (w & 7), ob = oline >= 10, ocol = (oline - 10 * ob) * 64 + mem * 8;
  const int oi = ob * D + ocol + (lane & 7);
  float xres = 0.f;
  if (wave == 3) {
    const unsigned lds0 = (unsigned)(size_t)lds;
    const RowMap rm = row_map(lane);
    wait1<8>(lds, W_HID, 1);
    wait1<8>(lds, W_PROG, P_V1 + 2);
    warm_code(3);
    if (oline < 20) xres = __uint_as_float((unsigned)((const GAS u16*)x_)[oi] << 16);
    asm volatile("" : "+v"(xres));
    wait1<2>(lds, W_PROG + 3, P_OUT - 1);
    const unsigned plan = *(volatile LAS unsigned*)(lds + L_FL + W_PLAN * 4), own = *(volatile LAS unsigned*)(lds + L_FL + (W_EST + 3) * 4);
    unsigned due = __builtin_amdgcn_readfirstlane(plan + min(max((int)(own - plan), 0), 1000)) + WO0, tau = ffn ? TAU_WO : TAU_WO * 4 / 3;
    const unsigned dec = ffn ? DEC_WO : DEC_WO * 2;
    until(__builtin_amdgcn_readfirstlane(own) + WO0);
    if (ffn) stage_rows((gptr)wd_ + ((fb * 64 + (mem & 1) * 32) * (D * 2) + (mem >> 1) * 320), lds0 + L_WD, 2, rm, due, tau, dec);
    stage_rows((gptr)wo_ + ((hc * DH + (mem & 1) * 96) * (D * 2) + (mem >> 1) * 320), lds0 + L_WO, 6, rm, due, tau, dec);
  }
  const int tt = wave - 3;
  const LAS char* ap = lds + L_QO + 768 + ((lane >> 2) & 1) * 384 + (lane >> 4) * 16;
  const LAS char* bp = lds + L_WO + ((lane >> 4) * 8 + ((lane >> 2) & 3)) * 320 + tt * 64 + (lane & 3) * 8;
  const LAS char* hp = lds + L_HB + ((lane >> 2) & 1) * 128 + (mem & 1) * 64 + (lane >> 4) * 16;
  unsigned ydst = WS_Y + (((((mem >> 1) * 5 + tt) * 4 + (lane >> 4)) * 64 + hc * 2 + (mem & 1)) * 16 + (lane & 15)) * 16;
  asm volatile("s_waitcnt vmcnt(10)" : "+v"(bp), "+v"(hp), "+v"(ap), "+v"(ydst) :: "memory");
  if (wave == 3) post(lds, W_STAGED, 1, lane);
  wait1<0>(lds, W_STAGED, 1);
  v4u bf[6];
#pragma unroll
  for (int i = 0; i < 4; ++i) bf[i] = tr_frag(bp + (i >> 1) * 10240 + (i & 1) * 32);
  const f4 zero = {0.f, 0.f, 0.f, 0.f};
  const v4u hf = *(const LAS v4u*)hp;
  const f4 g0 = mfma(hf, tr_frag(bp + L_WD), zero), g1 = mfma(hf, tr_frag(bp + L_WD + 32), zero);
  float gx = !ffn ? 0.f : lane < 32 ? g0.x : g1.x;
  asm volatile("s_waitcnt vmcnt(0)" : "+v"(ap), "+v"(ydst), "+v"(gx), "+v"(bf[0]), "+v"(bf[1]), "+v"(bf[2]), "+v"(bf[3]) :: "memory");
  BAR();
  {
    v4u a[3];
#pragma unroll
    for (int ks = 0; ks < 3; ++ks) a[ks] = *(const LAS v4u*)(ap + ks * 64);
    bf[4] = tr_frag(bp + 20480); bf[5] = tr_frag(bp + 20480 + 32);
    SB();
    f4 y0 = zero, y1 = zero;
#pragma unroll
    for (int ks = 0; ks < 3; ++ks) { y0 = mfma(a[ks], bf[2 * ks], y0); y1 = mfma(a[ks], bf[2 * ks + 1], y1); }
    put2(wsr, ydst, tag, lane < 32 ? y0.x : y1.x, gx);
  }
  if (wave == 3 && oline < 20) {
    unsigned ysrc = WS_Y + ((ocol >> 4) * 2 + ob) * (64 * 16 * 16) + (mem & 1) * 128 + (lane >> 3) * 256 + (lane & 7) * 16;
    GAS u16* op = (GAS u16*)out_ + oi;
    asm volatile("" : "+v"(ysrc), "+v"(op));
    const unsigned moved = peek(lds, W_MOVED);
    __builtin_amdgcn_s_sleep(6);
    until(peek(lds, W_PLAN) + T_YG);
    float s, gs;
    get2<8, 0>(wsr, ysrc, 2048, tag, s, gs);
    s += dpp<ROR8>(s); gs += dpp<ROR8>(gs);
    s = rbf(xrow_sum(s)); gs = rbf(xrow_sum(gs));
    const float gate = rbf(gs * __builtin_amdgcn_rcpf(1.f + __expf(-gs)));
    if (lane < 8) *op = (u16)bfbits(xres + rbf(s * gate));
    if (w == 0 && !moved && lane == 0) __hip_atomic_store((GAS unsigned*)(ws + WS_TAG), tag, RLX_AGENT);
  }
  asm volatile(".Lfd_last:");
}

extern "C" void kernel_launch(void* const* d_in, const int* in_sizes, int n_in, void* d_out, int out_size, void* d_ws, size_t ws_size,
                              hipStream_t stream) {
  fused_decode<<<256, NTHREADS, 0, stream>>>((const u16*)d_in[0], (const u16*)d_in[1], (const u16*)d_in[2], (const u16*)d_in[3],
                                             (const u16*)d_in[4], (const u16*)d_in[6], (const u16*)d_in[7], (const u16*)d_in[8],
                                             (u16*)d_out, (char*)d_ws);
}
```

```cpp
#include <hip/hip_runtime.h>
#include <stdint.h>
#define XVTHR 580.f
#define XDTHR 7.5f
#define XT_Q 2
#define XDEC_WO 57
#define XTAU_WO 410
#define XWO0 650
#define XCP4 10400
#define XTAU 650
#define XT_OG 3150
#define XT_YG 4990
#define XLEAD 118
#define XOM 1300
#define XORG 2200

typedef unsigned short u16;
typedef unsigned long long u64;
typedef unsigned v4u __attribute__((ext_vector_type(4)));
typedef unsigned v2u __attribute__((ext_vector_type(2)));
typedef float f4 __attribute__((ext_vector_type(4)));
typedef short s4 __attribute__((ext_vector_type(4)));
typedef __bf16 bf16x2 __attribute__((ext_vector_type(2)));
typedef __bf16 bf16x8 __attribute__((ext_vector_type(8)));
#define GAS __attribute__((address_space(1)))
#define LAS __attribute__((address_space(3)))
#define RLX_AGENT __ATOMIC_RELAXED, __HIP_MEMORY_SCOPE_AGENT
typedef const GAS char* gptr;
typedef GAS u64 gu64;
typedef __amdgpu_buffer_rsrc_t rsrc_t;

constexpr int D = 640, H = 32, SK = 4096, DH = 192, F = 1536, NTHREADS = 512;
constexpr int STEP = 32 * 3072;
constexpr int NP = 8;
constexpr unsigned SPIN_CAP = 1u << 16;

constexpr int P_K1 = 10, P_V1 = 26, P_OUT = 32, P_DONE = 34;
constexpr int T_Q = XT_Q, T_ST = 4, T_H = 22;

constexpr int TAU = XTAU;
constexpr int ORG = XORG, OM = XOM;
constexpr int LEAD = XLEAD;
constexpr int WBASE = 24, WSH = 8, WMAX = 10;
constexpr int NCP = 5;
constexpr int CP0 = 34400, CP1 = 28000, CP2 = 21200, CP3 = 16100, CP4 = XCP4;
constexpr int CPW0 = 4400, CPW = 2400, CPW3 = 2000;
constexpr int LEND = 13, MUL = 23, FAST = 100, VSLOW = 58;
constexpr int WO0 = XWO0, TAU_WO = XTAU_WO, DEC_WO = XDEC_WO;
constexpr int T_OG = XT_OG, T_YG = XT_YG;

constexpr int WS_TAG = 0;
constexpr int WS_CP = 4096;
constexpr int WS_ST = WS_CP + 8 * 256 * 8;
constexpr int WS_P = WS_ST + H * 2 * 16 * 8;
constexpr int WS_O = WS_P + H * 16 * 64 * 16;
constexpr int WS_Y = WS_O + H * 8 * DH * 16;
constexpr int WS_END = WS_Y + 80 * 64 * 16 * 16;
constexpr int L_WO = 0;
constexpr int L_WD = 30720;
constexpr int L_XX = L_WD + 10240;
constexpr int L_PART = L_XX + 5120;
constexpr int L_QO = L_PART + 12288;
constexpr int L_HB = L_QO + 1536;
constexpr int L_SC = L_HB + 256;
constexpr int L_PP = L_SC + 4096;
constexpr int L_FL = L_PP + 4096;
constexpr int L_PJ = L_FL + 160;
constexpr int L_TOTAL = 84 * 1024;
constexpr int W_PROG = 0, W_PJ = 4, W_LN = 8, W_SLOPE = 10, W_PLAN = 11, W_EST = 12, W_P = 16, W_STAGED = 17, W_HID = 18, W_MOVED = 19,
              W_Q = 20, W_GROW = 24, W_DUMP = 32;

__device__ __forceinline__ float rbf(float f) { return (float)(__bf16)f; }
__device__ __forceinline__ unsigned bfbits(float f) { return (unsigned)__builtin_bit_cast(u16, (__bf16)f); }
__device__ __forceinline__ float dot2(unsigned a, unsigned b, float acc) {
  return __builtin_amdgcn_fdot2_f32_bf16(__builtin_bit_cast(bf16x2, a), __builtin_bit_cast(bf16x2, b), acc, false);
}
__device__ __forceinline__ float dot8(v4u a, v4u b, float acc) {
  return dot2(a.w, b.w, dot2(a.z, b.z, dot2(a.y, b.y, dot2(a.x, b.x, acc))));
}
__device__ __forceinline__ f4 mfma(v4u a, v4u b, f4 c) {
  return __builtin_amdgcn_mfma_f32_16x16x32_bf16(__builtin_bit_cast(bf16x8, a), __builtin_bit_cast(bf16x8, b), c, 0, 0, 0);
}
template <int CTRL> __device__ __forceinline__ float dpp(float x) {
  return __builtin_bit_cast(float, __builtin_amdgcn_mov_dpp(__builtin_bit_cast(int, x), CTRL, 0xf, 0xf, true));
}
constexpr int XOR1 = 0xB1, XOR2 = 0x4E, HMIR = 0x141, ROR2 = 0x122, ROR4 = 0x124, ROR8 = 0x128;
__device__ __forceinline__ float sum8(float x) { x += dpp<XOR1>(x); x += dpp<XOR2>(x); return x + dpp<HMIR>(x); }
__device__ __forceinline__ float xrow_sum(float x) {
  auto s = __builtin_amdgcn_permlane16_swap(__float_as_uint(x), __float_as_uint(x), false, false);
  x = __uint_as_float(s[0]) + __uint_as_float(s[1]);
  auto t = __builtin_amdgcn_permlane32_swap(__float_as_uint(x), __float_as_uint(x), false, false);
  return __uint_as_float(t[0]) + __uint_as_float(t[1]);
}
__device__ __forceinline__ float wave_sum(float x) { x = sum8(x); return xrow_sum(x + dpp<ROR8>(x)); }
__device__ __forceinline__ float wave_max(float x) {
  x = fmaxf(x, dpp<XOR1>(x)); x = fmaxf(x, dpp<XOR2>(x)); x = fmaxf(x, dpp<HMIR>(x)); x = fmaxf(x, dpp<ROR8>(x));
  auto s = __builtin_amdgcn_permlane16_swap(__float_as_uint(x), __float_as_uint(x), false, false);
  x = fmaxf(__uint_as_float(s[0]), __uint_as_float(s[1]));
  auto t = __builtin_amdgcn_permlane32_swap(__float_as_uint(x), __float_as_uint(x), false, false);
  return fmaxf(__uint_as_float(t[0]), __uint_as_float(t[1]));
}
__device__ __forceinline__ float fold(float a, float b) {
  auto t = __builtin_amdgcn_permlane32_swap(__float_as_uint(a), __float_as_uint(b), false, false);
  return __uint_as_float(t[0]) + dpp<ROR8>(__uint_as_float(t[1]));
}
__device__ __forceinline__ rsrc_t mk_rsrc(const void* p, int bytes) { return __builtin_amdgcn_make_buffer_rsrc((void*)p, 0, bytes, 0x00020000); }
__device__ __forceinline__ v4u ld_nt(rsrc_t r, unsigned voff, unsigned soff) { return __builtin_amdgcn_raw_buffer_load_b128(r, voff, soff, 2); }
#define SB() __builtin_amdgcn_sched_barrier(0)
#define BAR() asm volatile("s_waitcnt lgkmcnt(0)\n\ts_barrier" ::: "memory")
#define NO_LOADS() __builtin_amdgcn_s_waitcnt(0x0F70)

__device__ __forceinline__ void put(gu64* g, unsigned tag, unsigned v) { __hip_atomic_store(g, ((u64)tag << 32) | v, RLX_AGENT); }
__device__ __forceinline__ void put2(rsrc_t r, unsigned off, unsigned tag, float a, float b) {
  __builtin_amdgcn_raw_buffer_store_b128((v4u){__float_as_uint(a), tag, __float_as_uint(b), tag}, r, off, 0, 16);
}
__device__ __forceinline__ float get(gu64* g, unsigned tag) {
#pragma unroll 1
  for (unsigned spins = 0; spins < SPIN_CAP; ++spins) {
    const u64 x = __hip_atomic_load(g, RLX_AGENT);
    if (__all((unsigned)(x >> 32) == tag)) return __uint_as_float((unsigned)x);
    __builtin_amdgcn_s_sleep(16);
  }
  return __builtin_nanf("");
}
template <int N, int SLEEP> __device__ __forceinline__ void get2(rsrc_t r, unsigned off, int stride, unsigned tag, float& a, float& b) {
#pragma unroll 1
  for (unsigned spins = 0; spins < SPIN_CAP; ++spins) {
    asm volatile("" ::: "memory");
    v4u x[N];
#pragma unroll
    for (int k = 0; k < N; ++k) x[k] = __builtin_amdgcn_raw_buffer_load_b128(r, off, k * stride, 16);
    unsigned lo = tag, hi = tag;
#pragma unroll
    for (int k = 0; k < N; ++k) {
      lo = min(min(lo, x[k].y), x[k].w); hi = max(max(hi, x[k].y), x[k].w);
      a = k ? a + __uint_as_float(x[k].x) : __uint_as_float(x[k].x);
      b = k ? b + __uint_as_float(x[k].z) : __uint_as_float(x[k].z);
      asm volatile("" : "+v"(a), "+v"(b), "+v"(lo), "+v"(hi));
    }
    if (__builtin_amdgcn_ballot_w64(lo != hi) == 0) return;
    if (SLEEP) __builtin_amdgcn_s_sleep(SLEEP);
  }
  a = b = __builtin_nanf("");
}
__device__ __forceinline__ void post(LAS char* lds, int word, unsigned v, int lane) {
  asm volatile("" ::: "memory");
  if (lane == 0) *(volatile LAS unsigned*)(lds + L_FL + word * 4) = v;
  asm volatile("" ::: "memory");
}
__device__ __forceinline__ unsigned peek(LAS char* lds, int word) {
  return __builtin_amdgcn_readfirstlane(*(volatile LAS unsigned*)(lds + L_FL + word * 4));
}
template <int SLEEP> __device__ __forceinline__ void wait1(LAS char* lds, int word, unsigned want) {
  while (peek(lds, word) < want) { if (SLEEP) __builtin_amdgcn_s_sleep(SLEEP); }
  asm volatile("" ::: "memory");
}
template <int SLEEP, int N> __device__ __forceinline__ void waitn(LAS char* lds, int word, unsigned want) {
  for (;;) {
    const v4u v = *(volatile LAS v4u*)(lds + L_FL + word * 4);
    unsigned m = min(v.x, v.y);
    if (N == 4) m = min(m, min(v.z, v.w));
    if (__builtin_amdgcn_readfirstlane(m) >= want) break;
    if (SLEEP) __builtin_amdgcn_s_sleep(SLEEP);
  }
  asm volatile("" ::: "memory");
}
__device__ __forceinline__ int until(unsigned due) {
  int late;
  asm volatile("s_branch 2f\n1:\n\ts_sleep 1\n2:\n\ts_memrealtime vcc\n\ts_waitcnt lgkmcnt(0)\n\ts_lshl_b32 %0, vcc_lo, 4\n\t"
               "s_sub_i32 %0, %0, %1\n\ts_cmp_lt_i32 %0, 0\n\ts_cbranch_scc1 1b" : "=&s"(late) : "s"(due) : "vcc", "scc", "memory");
  return late;
}
struct RowMap { unsigned o[5]; };
__device__ __forceinline__ RowMap row_map(int lane) {
  RowMap m;
#pragma unroll
  for (int i = 0; i < 5; ++i) { const unsigned ci = i * 64 + lane, row = (ci * 52429u) >> 20; m.o[i] = row * (D * 2 - 320) + ci * 16; }
  asm volatile("" : "+v"(m.o[0]), "+v"(m.o[1]), "+v"(m.o[2]), "+v"(m.o[3]), "+v"(m.o[4]));
  return m;
}
__device__ __forceinline__ void stage_rows(gptr src, unsigned lds_dst, int ng, const RowMap& m, unsigned& due, unsigned& tau, unsigned dec) {
#pragma unroll 1
  for (int g = 0; g < ng; ++g) {
    unsigned keep;
    until(due); due += tau; tau -= dec;
    asm volatile("s_mov_b32 %0, m0\n\ts_mov_b32 m0, %7\n\ts_nop 0\n\tglobal_load_lds_dwordx4 %1, %6\n\t"
                 "s_add_u32 m0, m0, 0x400\n\ts_nop 0\n\tglobal_load_lds_dwordx4 %2, %6\n\t"
                 "s_add_u32 m0, m0, 0x400\n\ts_nop 0\n\tglobal_load_lds_dwordx4 %3, %6\n\t"
                 "s_add_u32 m0, m0, 0x400\n\ts_nop 0\n\tglobal_load_lds_dwordx4 %4, %6\n\t"
                 "s_add_u32 m0, m0, 0x400\n\ts_nop 0\n\tglobal_load_lds_dwordx4 %5, %6\n\ts_mov_b32 m0, %0"
                 : "=&s"(keep) : "v"(m.o[0]), "v"(m.o[1]), "v"(m.o[2]), "v"(m.o[3]), "v"(m.o[4]), "s"(src), "s"(lds_dst) : "scc", "memory");
    src += 16 * D * 2; lds_dst += 5120;
  }
}
__device__ __forceinline__ v4u tr_frag(const LAS char* p) {
  const v2u b0 = __builtin_bit_cast(v2u, __builtin_amdgcn_ds_read_tr16_b64_v4i16((LAS s4*)p));
  const v2u b1 = __builtin_bit_cast(v2u, __builtin_amdgcn_ds_read_tr16_b64_v4i16((LAS s4*)(p + 1280)));
  return (v4u){b0.x, b0.y, b1.x, b1.y};
}

extern "C" __global__ void __launch_bounds__(512, 2)
fused_decode(const u16* x_, const u16* kc_, const u16* wq_, const u16* wg_, char* ws_, const u16* wu_, const u16* vc_, const u16* wo_,
             const u16*, u16*) {
  auto late_args = [&](u64& wd, u64& out) {
    v4u t;
    asm volatile("s_load_dwordx4 %0, %1, 0x40\n\ts_waitcnt lgkmcnt(0)" : "=s"(t) : "s"(__builtin_amdgcn_kernarg_segment_ptr()));
    wd = t.x | ((u64)t.y << 32); out = t.z | ((u64)t.w << 32);
  };
  SB();
  __shared__ __attribute__((aligned(16))) char lds_raw[L_TOTAL];
  LAS char* lds = (LAS char*)lds_raw;
  GAS char* ws = (GAS char*)ws_;
  const int tid = threadIdx.x, lane = tid & 63, wave = __builtin_amdgcn_readfirstlane(tid >> 6);
  const int w = blockIdx.x, h = (w & 7) * 4 + (w >> 6), mem = (w >> 3) & 7;
  const int ww = wave - 4;
  const int hc = (h + 4) & 31;
  u64 code0;
  {
    u64 pc; unsigned back;
    asm volatile("s_getpc_b64 %0\n.Lfd_here:\n\ts_mov_b32 %1, .Lfd_here-fused_decode" : "=s"(pc), "=s"(back));
    code0 = pc - back;
  }
  auto warm_code = [&](int who) {
    u64 ex; unsigned keep;
    const unsigned c0 = (unsigned)(tid - who * 64) * 4096u + (w >> 3) * 128u;
    asm volatile("v_cmp_gt_u32 vcc, .Lfd_last-fused_decode, %2\n\ts_and_saveexec_b64 %0, vcc\n\ts_cbranch_execz 1f\n\t"
                 "s_mov_b32 %1, m0\n\ts_mov_b32 m0, %4\n\ts_nop 0\n\tglobal_load_lds_dword %2, %3\n\ts_mov_b32 m0, %1\n"
                 "1:\n\ts_mov_b64 exec, %0"
                 : "=&s"(ex), "=&s"(keep) : "v"(c0), "s"(code0), "s"((unsigned)(size_t)lds + L_FL + W_DUMP * 4) : "vcc", "scc", "memory");
  };
  warm_code(0);
  const bool ffn = w >= 64, isq = mem < 6;
  const int fb = (w & 7) * 3 + (w >> 6) - 1;
  const int row0 = (mem & 1) * 320 + (lane >> 3), qm = (mem - 6) >> 31;
  unsigned rs8 = isq ? 8 * H * DH * 2 : 8 * F * 2;
  int jobw = wave + ((w >> 4) == 3 ? 4 : 0);
  unsigned woff = (row0 + wave * 80) * (rs8 >> 3) + (((h * 3 + (mem >> 1)) & qm) | (fb & ~qm)) * 128 + (lane & 7) * 16;
  unsigned xoff = (wave & 1) * (D * 2) + lane * 4;
  asm volatile("" : "+s"(rs8), "+s"(jobw));
  asm volatile("" : "+v"(woff), "+v"(xoff));
  SB();
  const bool job = jobw < 4;
  unsigned xr[5];
  v4u R[10];
  if (job) {
#pragma unroll
    for (int i = 0; i < 5; ++i) xr[i] = *(const GAS unsigned*)((gptr)x_ + xoff + 256 * i);
    SB();
    const rsrc_t wr = mk_rsrc(isq ? wq_ : wg_, 80 * rs8);
    unsigned so = 0;
#pragma unroll
    for (int i = 0; i < 10; ++i) { R[i] = ld_nt(wr, woff, so); so += rs8; SB(); }
  }
  if ((w >> 3) == 7 && wave == 0) {
    u64 wd_, out_;
    late_args(wd_, out_);
    const unsigned g = (lane >> 2) & 3, hg = (w & 7) * 4 + g, e = lane & 1;
    const unsigned okv = (((lane >> 1) & 1) * H + hg) * (SK * DH * 2) + e * (SK * DH * 2 - 4), owo = ((hg + 4) & 31) * (DH * D * 2) + e * (DH * D * 2 - 4);
    const unsigned off = (lane & 15) * (D * F * 2 / 16), ows = (e ? WS_END : 2048) + (w & 7) * 128, oo = (w & 7) * 128;
    unsigned keep;
    asm volatile("s_mov_b32 %0, m0\n\ts_mov_b32 m0, %13\n\ts_nop 0\n\tglobal_load_lds_dword %1, %6 nt\n\tglobal_load_lds_dword %3, %9 nt\n\t"
                 "global_load_lds_dword %4, %11 nt\n\tglobal_load_lds_dword %1, %7 nt\n\tglobal_load_lds_dword %2, %8 nt\n\t"
                 "global_load_lds_dword %3, %10 nt\n\tglobal_load_lds_dword %5, %12 nt\n\ts_mov_b32 m0, %0"
                 : "=&s"(keep) : "v"(okv), "v"(owo), "v"(off), "v"(ows), "v"(oo), "s"(kc_), "s"(vc_), "s"(wo_), "s"(wu_), "s"(wd_), "s"(ws_), "s"(out_),
                   "s"((unsigned)(size_t)lds + L_PP) : "memory");
  }
  asm volatile("" :: "s"(x_), "s"(ws_) : "memory");
  const unsigned t0 = (unsigned)__builtin_amdgcn_s_memrealtime();
  const rsrc_t wsr = mk_rsrc(ws_, WS_END);
  unsigned tag;
  v4u msk;
  auto meet = [&]() {
    tag = *(const __attribute__((address_space(4))) unsigned*)(ws_ + WS_TAG) + 1u;
    const unsigned mm = (lane & 1) ? 0xffff0000u : 0x0000ffffu;
    const int q = (lane & 7) >> 1;
    msk = (v4u){q == 0 ? mm : 0u, q == 1 ? mm : 0u, q == 2 ? mm : 0u, q == 3 ? mm : 0u};
    asm volatile("" : "+s"(tag));
    asm volatile("" : "+v"(msk));
    BAR();
  };

  if (wave >= 4) {
    NO_LOADS();
    unsigned fin0 = t0 * 16u + (ORG + 63 * TAU);
    if (tid < 284) *(volatile LAS unsigned*)(lds + L_FL + lane * 4) = lane < W_PLAN || lane >= W_P ? 0u : fin0;
    meet();
    fin0 += max((int)(((unsigned)__builtin_amdgcn_s_memrealtime() - t0) * 16u) + OM - ORG, 0);
    if (ww == 0) post(lds, W_PLAN, fin0, lane);
    __builtin_amdgcn_s_setprio(2);
    constexpr int HSTR = SK * DH * 2, BSTR = H * HSTR;
    const rsrc_t kr = mk_rsrc((const char*)kc_ + (size_t)h * HSTR, BSTR + HSTR), vr = mk_rsrc((const char*)vc_ + (size_t)h * HSTR, BSTR + HSTR);
    auto group_off = [&](int g) { return ((g & 4) ? 0 : BSTR) + ((g + h) & 3) * (4 * STEP); };
    unsigned loff = (mem * 4 + ww) * 3072 + (lane >> 3) * 384 + (lane & 7) * 16;
    LAS char* flw = lds + L_FL + (W_PROG + ww) * 4;
    const LAS char* qp = lds + L_QO + (lane & 7) * 16;
    LAS float* sc = (LAS float*)(lds + L_SC) + 256 + ww * 8 + (lane >> 3) + (lane & 3) * 32 - (lane & 4) * 64;
    LAS char* part = lds + L_PART + ((ww * 2 + (lane >> 5)) * DH + (lane & 7) * 8 + ((lane >> 4) & 1) * 4) * 4;
    const LAS unsigned* pp = (const LAS unsigned*)(lds + L_PP) + ww * 8 + (lane >> 3);
    asm volatile("" : "+v"(loff), "+v"(flw), "+v"(qp), "+v"(sc), "+v"(part), "+v"(pp));
    unsigned due = fin0 - 63 * TAU + ww * (TAU / 4), fin = fin0;
    int adj = 0, step = TAU, slow = 0, late = 0, win;
    auto pace = [&](int lead = 0) {
      late = until(due - lead) - lead; due += step; fin += adj;
      win = min((unsigned)(late + ((WBASE - 4 + ww) << WSH)) >> (WSH + 2), WMAX - 1u);
      if (lead && late < 0) { win = 4; late = 0; }
      SB();
    };
    auto report = [&](unsigned prog) {
      asm volatile("s_mov_b64 exec, 1\n\tds_write2_b32 %0, %1, %2 offset1:12\n\ts_mov_b64 exec, -1" :: "v"(flw), "v"(prog), "v"(fin + late) : "memory");
    };
    auto gauge = [&]() {
      if (late >= 300) return late + 2 * TAU;
      asm volatile("s_waitcnt vmcnt(3)" ::: "memory");
      return (int)((unsigned)__builtin_amdgcn_s_memrealtime() * 16u - due) + 2 * step;
    };
    int back0 = 0;
    auto steer = [&](v2u pl, int g) {
      const int left = 61 - LEND - 4 * g, mul = g < 13 ? MUL : g == 13 ? 28 : g == 14 ? 51 : 256;
      const int sw = __builtin_amdgcn_readfirstlane(pl.x), sl = (sw & 1) * VSLOW;
      fin += 16 * (sl - slow); slow = sl;
      const int slope = left > 0 ? sw >> 1 : 0, gap = (int)(pl.y - fin) - slope * left;
      adj = __builtin_amdgcn_readfirstlane(min(max(slope + ((gap * mul) >> 8), -FAST), TAU / 2));
      step = TAU + adj + ((unsigned)(g - 7) < 4u ? slow : 0);
    };
    auto fetch = [&](rsrc_t r, int so, int j) {
      unsigned cnt;
      asm volatile("1:\n\ts_getreg_b32 %0, hwreg(HW_REG_IB_STS, 0, 4)\n\ts_cmp_gt_u32 %0, %1\n\ts_cbranch_scc1 1b" : "=&s"(cnt) : "s"(win) : "scc", "memory");
      const v4u x = ld_nt(r, loff + j * 128, so);
      SB();
      return x;
    };
    v4u U[NP + 4][3];
#pragma unroll
    for (int u = 0; u < NP + 4; ++u) {
      pace((NP + 3 - u) * LEAD);
#pragma unroll
      for (int j = 0; j < 3; ++j) U[u][j] = fetch(kr, group_off(u >> 2) + (u & 3) * STEP, j);
      if (u & 1) report(u >> 1);
    }
    v4u q[3], q2[3];
    for (;;) {
      const v4u f = *(volatile LAS v4u*)(lds + L_FL + W_Q * 4);
#pragma unroll
      for (int j = 0; j < 3; ++j) { q[j] = *(volatile LAS v4u*)(qp + 384 + j * 128); q2[j] = *(volatile LAS v4u*)(qp + j * 128); }
      if (__builtin_amdgcn_readfirstlane(f.x & f.y & f.z)) break;
    }
#pragma unroll 1
    for (int g = 3; g < 9; ++g) {
      const rsrc_t r = g == 8 ? vr : kr;
      const int so = group_off(g);
      float pick = 0.f;
      v2u pl;
#pragma unroll
      for (int k = 0; k < 4; ++k) {
        if (k == 1) pl = *(volatile LAS v2u*)(lds + L_FL + W_SLOPE * 4);
        pace();
        float d = 0.f;
#pragma unroll
        for (int j = 0; j < 3; ++j) { d = dot8(U[NP + k][j], q[j], d); asm volatile("" : "+v"(d)); SB(); U[NP + k][j] = fetch(r, so + k * STEP, j); }
        if (k == 1) steer(pl, g); else if (k != 3) report(2 * g + (k >> 1));
        d = sum8(d);
        pick = (lane & 7) == k ? d : pick;
        if (g < 5) {
          const float e = sum8(dot8(U[k][2], q[2], dot8(U[k][1], q[1], dot8(U[k][0], q[0], 0.f))));
          pick = (lane & 7) == 4 + k ? e : pick;
#pragma unroll
          for (int j = 0; j < 3; ++j) U[k][j] = U[4 + k][j];
        }
        asm volatile("" : "+v"(pick));
        SB();
        if (k == 0 && g == 8) back0 = gauge();
      }
      if (g < 5 || !(lane & 4)) *sc = rbf(rbf(pick) * 0.07216878364870322f);
      sc += 128;
      if (g == 4) {
#pragma unroll
        for (int j = 0; j < 3; ++j) q[j] = q2[j];
      }
      SB();
    }
    f4 o[3];
#pragma unroll
    for (int j = 0; j < 3; ++j) o[j] = (f4){0.f, 0.f, 0.f, 0.f};
    auto vstep = [&](int g, bool more) {
      const int so = group_off(g);
      unsigned f = *(volatile LAS unsigned*)(lds + L_FL + W_P * 4), pv[4];
#pragma unroll
      for (int k = 0; k < 4; ++k) pv[k] = *(volatile LAS unsigned*)(pp + k * 32);
      v2u pl;
#pragma unroll
      for (int k = 0; k < 4; ++k) {
        if (k == 1 && more) pl = *(volatile LAS v2u*)(lds + L_FL + W_SLOPE * 4);
        if (more) pace();
        if (k == 0 && more && (unsigned)__builtin_amdgcn_readfirstlane(f) < (g < 13 ? 1u : 2u)) {
          wait1<0>(lds, W_P, g < 13 ? 1u : 2u);
#pragma unroll
          for (int i = 0; i < 4; ++i) pv[i] = *(volatile LAS unsigned*)(pp + i * 32);
        }
        const v4u a = msk & pv[k];
#pragma unroll
        for (int j = 0; j < 3; ++j) { o[j] = mfma(a, U[NP + k][j], o[j]); SB(); if (more) U[NP + k][j] = fetch(vr, so + k * STEP, j); }
        if (more) {
          if (k == 1) steer(pl, g); else if (k != 3) report(2 * g + (k >> 1));
          if (k == 0 && g == 9) post(lds, W_GROW + ww, gauge() - back0, lane);
        }
        SB();
      }
      pp += 128;
    };
    auto vflush = [&](int ph) {
      if (((lane >> 3) & 1) == (lane >> 5)) {
#pragma unroll
        for (int j = 0; j < 3; ++j) *(LAS f4*)(part + ph * (8 * DH * 4) + j * 256) = o[j];
      }
    };
#pragma unroll 1
    for (int g = 9; g < 16; ++g) {
      vstep(g, true);
      if (g == 12) {
        vflush(0);
#pragma unroll
        for (int j = 0; j < 3; ++j) o[j] = (f4){0.f, 0.f, 0.f, 0.f};
      }
    }
    fin -= adj;
    report(P_OUT);
    vstep(16, false);
    vflush(1);
    report(P_DONE);
    __builtin_amdgcn_s_setprio(0);
  } else {
    meet();
    if (job) {
      float v[10], sum = 0.f;
#pragma unroll
      for (int i = 0; i < 5; ++i) { v[2 * i] = __uint_as_float(xr[i] << 16); v[2 * i + 1] = __uint_as_float(xr[i] & 0xffff0000u); sum += v[2 * i] + v[2 * i + 1]; }
      const float mean = wave_sum(sum) * (1.f / D);
      float q = 0.f;
#pragma unroll
      for (int i = 0; i < 10; ++i) { const float dd = v[i] - mean; q += dd * dd; }
      const float var = rbf(wave_sum(q) * (1.f / D)), mb = rbf(mean);
      const float inv = __builtin_amdgcn_rcpf(rbf(__builtin_amdgcn_sqrtf(rbf(var + 1e-5f))));
#pragma unroll
      for (int i = 0; i < 10; ++i) {
        const unsigned t = bfbits(rbf(v[i] - mb) * inv);
        *(LAS unsigned*)(lds + L_XX + ((lane + 64 * (i >> 1)) * 2 + (i & 1)) * 8 + (wave & 1) * 4) = t | (t << 16);
      }
      post(lds, W_LN + (wave & 1), 1, lane);
      waitn<0, 2>(lds, W_LN, 1);
#pragma unroll 1
      for (int pass = 0;; ++pass) {
        f4 a0 = {0.f, 0.f, 0.f, 0.f}, a1 = a0;
        const LAS char* xp = lds + L_XX + (pass ? mem * 80 + (lane >> 3) : row0 + wave * 80) * 8;
#pragma unroll
        for (int i = 0; i < 10; ++i) {
          const v2u xv = *(const LAS v2u*)(xp + i * 64);
          a0 = mfma(msk & xv.x, R[i], a0); a1 = mfma(msk & xv.y, R[i], a1);
        }
        const bool more = wave == 3 && ffn && pass == 0;
        if (more) {
          const rsrc_t wr = mk_rsrc(wu_, D * F * 2);
          const unsigned uoff = (mem * 80 + (lane >> 3)) * (F * 2) + fb * 128 + (lane & 7) * 16;
#pragma unroll
          for (int i = 0; i < 10; ++i) { R[i] = ld_nt(wr, uoff, i * (8 * F * 2)); SB(); }
        }
        const f4 s = {fold(a0[0], a1[0]), fold(a0[1], a1[1]), fold(a0[2], a1[2]), fold(a0[3], a1[3])};
        if ((lane & 8) == 0) *(LAS f4*)(lds + L_PJ + (wave * 2 + (lane >> 5)) * 256 + (lane & 7) * 32 + ((lane >> 4) & 1) * 16) = s;
        post(lds, W_PJ + wave, 1, lane);
        if (wave < 3) break;
        waitn<1, 4>(lds, W_PJ, 1);
        const LAS float* pr = (const LAS float*)(lds + L_PJ) + lane;
        const float c0 = pr[384], c1 = pr[448];
        put2(wsr, WS_P + ((h * 16 + mem + 8 * pass) * 64 + lane) * 16, tag,
             pass ? c0 : (pr[0] + pr[128]) + (pr[256] + c0), pass ? c1 : (pr[64] + pr[192]) + (pr[320] + c1));
        if (!more) break;
      }
    }
    if (wave < 3) {
      const int ot = wave * 64 + lane;
      {
        wait1<4>(lds, W_PROG, T_Q);
        float r0, r1;
        get2<2, 24>(wsr, WS_P + ((h * 16 + wave * 2) * 64 + lane) * 16, 1024, tag, r0, r1);
        LAS u16* dst = (LAS u16*)(lds + L_QO) + ot;
        dst[0] = (u16)bfbits(r0); dst[DH] = (u16)bfbits(r1);
        post(lds, W_Q + wave, 1, lane);
      }
      if (wave == 2) {
        if (ffn) {
          wait1<8>(lds, W_PROG, T_H);
          float gt[2], up[2];
          get2<2, 24>(wsr, WS_P + ((h * 16 + 6) * 64 + lane) * 16, 1024, tag, gt[0], gt[1]);
          get2<8, 24>(wsr, WS_P + ((h * 16 + 8) * 64 + lane) * 16, 1024, tag, up[0], up[1]);
#pragma unroll
          for (int b = 0; b < 2; ++b) {
            const float g = rbf(gt[b]), t2 = 2.f * 0.7978845608028654f * (g + 0.044715f * g * g * g);
            *(LAS u16*)(lds + L_HB + b * 128 + lane * 2) = (u16)bfbits(rbf(g * __builtin_amdgcn_rcpf(1.f + __expf(-t2))) * rbf(up[b]));
          }
        }
        post(lds, W_HID, 1, lane);
      } else if (wave == 0) {
        gu64* stbox = (gu64*)(ws + WS_ST) + h * 32;
#pragma unroll 1
        for (int ph = 0; ph < 2; ++ph) {
          waitn<4, 4>(lds, W_PROG, P_K1 + ph * 8);
          const f4 s0 = *(const LAS f4*)(lds + L_SC + ph * 2048 + lane * 32), s1 = *(const LAS f4*)(lds + L_SC + ph * 2048 + lane * 32 + 16);
          const float s[8] = {s0.x, s0.y, s0.z, s0.w, s1.x, s1.y, s1.z, s1.w};
          constexpr float L2E = 1.4426950408889634f;
          float m = s[0];
#pragma unroll
          for (int i = 1; i < 8; ++i) m = fmaxf(m, s[i]);
          m = wave_max(m);
          const float ms = m == -INFINITY ? 0.f : m;
          float l = 0.f;
#pragma unroll
          for (int i = 0; i < 8; ++i) l += __builtin_amdgcn_exp2f((s[i] - ms) * L2E);
          l = wave_sum(l);
          if (lane < 2) put(stbox + ph * 16 + mem * 2 + lane, tag, __float_as_uint(lane ? l : m));
          wait1<4>(lds, W_PROG, P_K1 + ph * 8 + T_ST);
          const float st = get(stbox + ph * 16 + (lane & 15), tag);
          const float oth = dpp<XOR1>(st), mj = (lane & 1) ? oth : st, lj = (lane & 1) ? st : oth;
          float M = fmaxf(mj, dpp<ROR2>(mj)); M = fmaxf(M, dpp<ROR4>(M)); M = fmaxf(M, dpp<ROR8>(M));
          float Ls = lj * __builtin_amdgcn_exp2f((mj - M) * L2E);
          Ls += dpp<ROR2>(Ls); Ls += dpp<ROR4>(Ls); Ls += dpp<ROR8>(Ls);
          const float inv = __builtin_amdgcn_rcpf(__uint_as_float(__builtin_amdgcn_readfirstlane(__float_as_uint(Ls))));
          unsigned p[8];
#pragma unroll
          for (int i = 0; i < 8; ++i) { const unsigned t = bfbits(__builtin_amdgcn_exp2f((s[i] - M) * L2E) * inv); p[i] = t | (t << 16); }
          *(LAS v4u*)(lds + L_PP + ph * 2048 + lane * 32) = (v4u){p[0], p[1], p[2], p[3]};
          *(LAS v4u*)(lds + L_PP + ph * 2048 + lane * 32 + 16) = (v4u){p[4], p[5], p[6], p[7]};
          post(lds, W_P, ph + 1, lane);
        }
      } else {
        const unsigned fin0 = peek(lds, W_PLAN);
        unsigned at = fin0 - CP0, plan = fin0;
        float prev = 0.f;
        int slope = 0, vs = 0;
        const unsigned half = (unsigned)__builtin_amdgcn_s_getreg(20 | (2 << 6) | (0 << 11));
#pragma unroll 1
        for (int c = 0; c < NCP; ++c) {
          at = __builtin_amdgcn_readfirstlane(at);
          until(at);
          const v4u e = *(volatile LAS v4u*)(lds + L_FL + W_EST * 4), bg = *(volatile LAS v4u*)(lds + L_FL + W_GROW * 4);
          const unsigned mine = e.x + (unsigned)(((int)(e.y - e.x) + (int)(e.z - e.x) + (int)(e.w - e.x)) >> 2);
          const int grow = min(max((int)(bg.x + bg.y + bg.z + bg.w) >> 5, -128), 127) + 128;
          gu64* cp = (gu64*)(ws + WS_CP) + c * 256;
          if (lane == 0 && (mem & 3) == 1) put(cp + (w >> 5) * 8 + (w & 7), tag, ((mine >> 2) << 10) | (grow << 2) | half | (peek(lds, W_P) ? 2u : 0u));
          unsigned look = at + (c == 3 ? CPW3 : c ? CPW : CPW0);
          u64 x; bool ok;
#pragma unroll 1
          for (int tr = c ? 2 : 0;; ++tr) {
            until(look);
            x = __hip_atomic_load(cp + lane, RLX_AGENT);
            ok = (unsigned)(x >> 32) == tag;
            if (tr == 2 || __all(ok)) break;
            look += CPW;
          }
          if (w == 0 && c == NCP - 1 && __all(ok && (x & 2))) {
            if (lane == 0) __hip_atomic_store((GAS unsigned*)(ws + WS_TAG), tag, RLX_AGENT);
            post(lds, W_MOVED, 1, lane);
          }
          const bool up = ok && (x & 1);
          const float sum = ok ? (float)(((int)((unsigned)x - ((fin0 >> 2) << 10)) >> 10) * 4) : 0.f;
          const float sa = wave_sum(sum), su = wave_sum(up ? sum : 0.f);
          const float na = (float)__builtin_popcountll(__builtin_amdgcn_ballot_w64(ok)), nu = (float)__builtin_popcountll(__builtin_amdgcn_ballot_w64(up));
          const float mean = sa * __builtin_amdgcn_rcpf(fmaxf(na, 1.f));
          const float hi = fmaxf(nu > 0.f ? su * __builtin_amdgcn_rcpf(nu) : mean, na > nu ? (sa - su) * __builtin_amdgcn_rcpf(na - nu) : mean);
          const float est = fmaxf(mean, hi - 0.015f * (float)(int)(plan - at));
          const bool first = c < 2;
          const unsigned before = plan;
          const float worst = wave_max(ok ? sum : -1e9f) - 600.f;
          if (first || __builtin_amdgcn_readfirstlane(est - (float)(int)(plan - fin0) > 100.f))
            plan = fin0 + __builtin_amdgcn_readfirstlane((int)fminf(fmaxf(
                first ? est + 80.f : est + (c == 3 ? 1.f : 0.5f) * fminf(fmaxf(est - prev, 0.f), 600.f) + 100.f, -8000.f), 16000.f));
          const bool hint = c == 1 && __builtin_amdgcn_readfirstlane(est - prev > XVTHR);
          prev = est;
          if (!first) plan += __builtin_amdgcn_readfirstlane((int)(0.25f * fminf(fmaxf(worst - (float)(int)(plan - fin0), 0.f), 4000.f)));
          if (first) {
            const v4u e2 = *(volatile LAS v4u*)(lds + L_FL + W_EST * 4);
            const int own = min(min((int)(e2.x - plan), (int)(e2.y - plan)), min((int)(e2.z - plan), (int)(e2.w - plan)));
            const float left = (float)((int)(plan - look) + own - 2400) * (1.f / TAU) - (float)LEND;
            slope = (int)(-(float)own * __builtin_amdgcn_rcpf(fmaxf(left, 8.f)));
          }
          at = (c == 3 ? before : plan) - (c == 0 ? CP1 : c == 1 ? CP2 : c == 2 ? CP3 : CP4);
          if (c == 3 && !vs && __builtin_amdgcn_readfirstlane(wave_sum(ok ? (float)((int)(((unsigned)x >> 2) & 255u) - 128) : 0.f) > XDTHR * na)
              && (int)(before + 1000u - plan) > 0) plan = before + 1000u;
          if (hint) { vs = 1; plan += 16 * VSLOW; prev += (float)(16 * VSLOW); }
          if (lane == 0) *(volatile LAS v2u*)(lds + L_FL + W_SLOPE * 4) = (v2u){(unsigned)(slope * 2 + vs), plan};
        }
      }
      waitn<4, 4>(lds, W_PROG, P_V1 + 4);
      const LAS float* pr = (const LAS float*)(lds + L_PART) + ot;
      LAS u16* odst = (LAS u16*)(lds + L_QO) + (2 + (lane >> 5)) * DH + wave * 32 + (lane & 31);
      float s0 = 0.f;
#pragma unroll
      for (int r = 0; r < 8; ++r) s0 += pr[r * DH];
      unsigned oout = WS_O + ((h * 8 + mem) * DH + ot) * 16;
      unsigned oin = WS_O + ((hc * 8 + (lane >> 5) * 4) * DH + (mem & 1) * 96 + wave * 32 + (lane & 31)) * 16;
      asm volatile("" : "+v"(s0), "+v"(oout), "+v"(oin), "+v"(odst), "+v"(pr));
      wait1<2>(lds, W_PROG + 3, P_OUT);
      waitn<0, 4>(lds, W_PROG, P_DONE);
      const float s1 = ((pr[8 * DH] + pr[9 * DH]) + (pr[10 * DH] + pr[11 * DH])) + ((pr[12 * DH] + pr[13 * DH]) + (pr[14 * DH] + pr[15 * DH]));
      put2(wsr, oout, tag, s1, s0);
      __builtin_amdgcn_s_sleep(9);
      until(peek(lds, W_PLAN) + T_OG);
      float r0, r1;
      get2<4, 0>(wsr, oin, DH * 16, tag, r0, r1);
      auto t = __builtin_amdgcn_permlane32_swap(__float_as_uint(r0), __float_as_uint(r1), false, false);
      *odst = (u16)bfbits(__uint_as_float(t[0]) + __uint_as_float(t[1]));
      BAR();
      NO_LOADS();
      return;
    }
  }
  NO_LOADS();
  const int oline = (w >> 6) * 8 + (w & 7), ob = oline >= 10, ocol = (oline - 10 * ob) * 64 + mem * 8;
  const int oi = ob * D + ocol + (lane & 7);
  float xres = 0.f;
  u64 wd_ = 0, out_ = 0;
  if (wave == 3) {
    late_args(wd_, out_);
    const unsigned lds0 = (unsigned)(size_t)lds;
    const RowMap rm = row_map(lane);
    wait1<8>(lds, W_HID, 1);
    wait1<8>(lds, W_PROG, P_V1 + 2);
    warm_code(3);
    if (oline < 20) xres = __uint_as_float((unsigned)((const GAS u16*)x_)[oi] << 16);
    asm volatile("" : "+v"(xres));
    wait1<2>(lds, W_PROG + 3, P_OUT - 1);
    const unsigned plan = *(volatile LAS unsigned*)(lds + L_FL + W_PLAN * 4), own = *(volatile LAS unsigned*)(lds + L_FL + (W_EST + 3) * 4);
    unsigned due = __builtin_amdgcn_readfirstlane(plan + min(max((int)(own - plan), 0), 1000)) + WO0, tau = ffn ? TAU_WO : TAU_WO * 4 / 3;
    const unsigned dec = ffn ? DEC_WO : DEC_WO * 2;
    until(__builtin_amdgcn_readfirstlane(own) + WO0);
    if (ffn) stage_rows((gptr)wd_ + ((fb * 64 + (mem & 1) * 32) * (D * 2) + (mem >> 1) * 320), lds0 + L_WD, 2, rm, due, tau, dec);
    stage_rows((gptr)wo_ + ((hc * DH + (mem & 1) * 96) * (D * 2) + (mem >> 1) * 320), lds0 + L_WO, 6, rm, due, tau, dec);
  }
  const int tt = wave - 3;
  const LAS char* ap = lds + L_QO + 768 + ((lane >> 2) & 1) * 384 + (lane >> 4) * 16;
  const LAS char* bp = lds + L_WO + ((lane >> 4) * 8 + ((lane >> 2) & 3)) * 320 + tt * 64 + (lane & 3) * 8;
  const LAS char* hp = lds + L_HB + ((lane >> 2) & 1) * 128 + (mem & 1) * 64 + (lane >> 4) * 16;
  unsigned ydst = WS_Y + (((((mem >> 1) * 5 + tt) * 4 + (lane >> 4)) * 64 + hc * 2 + (mem & 1)) * 16 + (lane & 15)) * 16;
  asm volatile("s_waitcnt vmcnt(10)" : "+v"(bp), "+v"(hp), "+v"(ap), "+v"(ydst) :: "memory");
  if (wave == 3) post(lds, W_STAGED, 1, lane);
  wait1<0>(lds, W_STAGED, 1);
  v4u bf[6];
#pragma unroll
  for (int i = 0; i < 4; ++i) bf[i] = tr_frag(bp + (i >> 1) * 10240 + (i & 1) * 32);
  const f4 zero = {0.f, 0.f, 0.f, 0.f};
  const v4u hf = *(const LAS v4u*)hp;
  const f4 g0 = mfma(hf, tr_frag(bp + L_WD), zero), g1 = mfma(hf, tr_frag(bp + L_WD + 32), zero);
  float gx = !ffn ? 0.f : lane < 32 ? g0.x : g1.x;
  asm volatile("s_waitcnt vmcnt(0)" : "+v"(ap), "+v"(ydst), "+v"(gx), "+v"(bf[0]), "+v"(bf[1]), "+v"(bf[2]), "+v"(bf[3]) :: "memory");
  BAR();
  {
    v4u a[3];
#pragma unroll
    for (int ks = 0; ks < 3; ++ks) a[ks] = *(const LAS v4u*)(ap + ks * 64);
    bf[4] = tr_frag(bp + 20480); bf[5] = tr_frag(bp + 20480 + 32);
    SB();
    f4 y0 = zero, y1 = zero;
#pragma unroll
    for (int ks = 0; ks < 3; ++ks) { y0 = mfma(a[ks], bf[2 * ks], y0); y1 = mfma(a[ks], bf[2 * ks + 1], y1); }
    put2(wsr, ydst, tag, lane < 32 ? y0.x : y1.x, gx);
  }
  if (wave == 3 && oline < 20) {
    unsigned ysrc = WS_Y + ((ocol >> 4) * 2 + ob) * (64 * 16 * 16) + (mem & 1) * 128 + (lane >> 3) * 256 + (lane & 7) * 16;
    GAS u16* op = (GAS u16*)out_ + oi;
    asm volatile("" : "+v"(ysrc), "+v"(op));
    const unsigned moved = peek(lds, W_MOVED);
    __builtin_amdgcn_s_sleep(6);
    until(peek(lds, W_PLAN) + T_YG);
    float s, gs;
    get2<8, 0>(wsr, ysrc, 2048, tag, s, gs);
    s += dpp<ROR8>(s); gs += dpp<ROR8>(gs);
    s = rbf(xrow_sum(s)); gs = rbf(xrow_sum(gs));
    const float gate = rbf(gs * __builtin_amdgcn_rcpf(1.f + __expf(-gs)));
    if (lane < 8) *op = (u16)bfbits(xres + rbf(s * gate));
    if (w == 0 && !moved && lane == 0) __hip_atomic_store((GAS unsigned*)(ws + WS_TAG), tag, RLX_AGENT);
  }
  asm volatile(".Lfd_last:");
}

extern "C" void kernel_launch(void* const* d_in, const int* in_sizes, int n_in, void* d_out, int out_size, void* d_ws, size_t ws_size,
                              hipStream_t stream) {
  fused_decode<<<256, NTHREADS, 0, stream>>>((const u16*)d_in[0], (const u16*)d_in[1], (const u16*)d_in[3], (const u16*)d_in[6], (char*)d_ws,
                                             (const u16*)d_in[7], (const u16*)d_in[2], (const u16*)d_in[4], (const u16*)d_in[8], (u16*)d_out);
}
```

```cpp
#include <hip/hip_runtime.h>
#include <stdint.h>

typedef unsigned short u16;
typedef unsigned long long u64;
typedef unsigned v4u __attribute__((ext_vector_type(4)));
typedef unsigned v2u __attribute__((ext_vector_type(2)));
typedef float f4 __attribute__((ext_vector_type(4)));
typedef short s4 __attribute__((ext_vector_type(4)));
typedef __bf16 bf16x2 __attribute__((ext_vector_type(2)));
typedef __bf16 bf16x8 __attribute__((ext_vector_type(8)));
#define GAS __attribute__((address_space(1)))
#define LAS __attribute__((address_space(3)))
#define RLX_AGENT __ATOMIC_RELAXED, __HIP_MEMORY_SCOPE_AGENT
typedef const GAS char* gptr;
typedef GAS u64 gu64;
typedef __amdgpu_buffer_rsrc_t rsrc_t;

constexpr int D = 640, H = 32, SK = 4096, DH = 192, F = 1536, NTHREADS = 512;
constexpr int STEP = 32 * 3072;
constexpr int NP = 8;
constexpr unsigned SPIN_CAP = 1u << 16;

constexpr int P_K1 = 10, P_V1 = 26, P_OUT = 32, P_DONE = 34;
constexpr int T_Q = 2, T_ST = 4, T_H = 22;

constexpr int TAU = 650;
constexpr int ORG = 2200, OM = 1300;
constexpr int LEAD = 118;
constexpr int WBASE = 24, WSH = 8, WMAX = 10;
constexpr int NCP = 5;
constexpr int CP0 = 34400, CP1 = 28000, CP2 = 21200, CP3 = 16100, CP4 = 10400;
constexpr int CPW0 = 4400, CPW = 2400, CPW3 = 2000;
constexpr int LEND = 13, MUL = 23, FAST = 100, VSLOW = 58;
constexpr int WO0 = 650, TAU_WO = 410, DEC_WO = 57;
constexpr int T_OG = 3150, T_YG = 4990;

constexpr int WS_TAG = 0;
constexpr int WS_CP = 4096;
constexpr int WS_ST = WS_CP + 8 * 256 * 8;
constexpr int WS_P = WS_ST + H * 2 * 16 * 8;
constexpr int WS_O = WS_P + H * 16 * 64 * 16;
constexpr int WS_Y = WS_O + H * 8 * DH * 16;
constexpr int WS_END = WS_Y + 80 * 64 * 16 * 16;
constexpr int L_WO = 0;
constexpr int L_WD = 30720;
constexpr int L_XX = L_WD + 10240;
constexpr int L_PART = L_XX + 5120;
constexpr int L_QO = L_PART + 12288;
constexpr int L_HB = L_QO + 1536;
constexpr int L_SC = L_HB + 256;
constexpr int L_PP = L_SC + 4096;
constexpr int L_FL = L_PP + 4096;
constexpr int L_PJ = L_FL + 160;
constexpr int L_TOTAL = 84 * 1024;
constexpr int W_PROG = 0, W_PJ = 4, W_LN = 8, W_SLOPE = 10, W_PLAN = 11, W_EST = 12, W_P = 16, W_STAGED = 17, W_HID = 18, W_MOVED = 19,
              W_Q = 20, W_GROW = 24, W_TAG = 28, W_DUMP = 32;

__device__ __forceinline__ float rbf(float f) { return (float)(__bf16)f; }
__device__ __forceinline__ unsigned bfbits(float f) { return (unsigned)__builtin_bit_cast(u16, (__bf16)f); }
__device__ __forceinline__ float dot2(unsigned a, unsigned b, float acc) {
  return __builtin_amdgcn_fdot2_f32_bf16(__builtin_bit_cast(bf16x2, a), __builtin_bit_cast(bf16x2, b), acc, false);
}
__device__ __forceinline__ float dot8(v4u a, v4u b, float acc) {
  return dot2(a.w, b.w, dot2(a.z, b.z, dot2(a.y, b.y, dot2(a.x, b.x, acc))));
}
__device__ __forceinline__ f4 mfma(v4u a, v4u b, f4 c) {
  return __builtin_amdgcn_mfma_f32_16x16x32_bf16(__builtin_bit_cast(bf16x8, a), __builtin_bit_cast(bf16x8, b), c, 0, 0, 0);
}
template <int CTRL> __device__ __forceinline__ float dpp(float x) {
  return __builtin_bit_cast(float, __builtin_amdgcn_mov_dpp(__builtin_bit_cast(int, x), CTRL, 0xf, 0xf, true));
}
constexpr int XOR1 = 0xB1, XOR2 = 0x4E, HMIR = 0x141, ROR2 = 0x122, ROR4 = 0x124, ROR8 = 0x128;
__device__ __forceinline__ float sum8(float x) { x += dpp<XOR1>(x); x += dpp<XOR2>(x); return x + dpp<HMIR>(x); }
__device__ __forceinline__ float xrow_sum(float x) {
  auto s = __builtin_amdgcn_permlane16_swap(__float_as_uint(x), __float_as_uint(x), false, false);
  x = __uint_as_float(s[0]) + __uint_as_float(s[1]);
  auto t = __builtin_amdgcn_permlane32_swap(__float_as_uint(x), __float_as_uint(x), false, false);
  return __uint_as_float(t[0]) + __uint_as_float(t[1]);
}
__device__ __forceinline__ float wave_sum(float x) { x = sum8(x); return xrow_sum(x + dpp<ROR8>(x)); }
__device__ __forceinline__ float wave_max(float x) {
  x = fmaxf(x, dpp<XOR1>(x)); x = fmaxf(x, dpp<XOR2>(x)); x = fmaxf(x, dpp<HMIR>(x)); x = fmaxf(x, dpp<ROR8>(x));
  auto s = __builtin_amdgcn_permlane16_swap(__float_as_uint(x), __float_as_uint(x), false, false);
  x = fmaxf(__uint_as_float(s[0]), __uint_as_float(s[1]));
  auto t = __builtin_amdgcn_permlane32_swap(__float_as_uint(x), __float_as_uint(x), false, false);
  return fmaxf(__uint_as_float(t[0]), __uint_as_float(t[1]));
}
__device__ __forceinline__ float fold(float a, float b) {
  auto t = __builtin_amdgcn_permlane32_swap(__float_as_uint(a), __float_as_uint(b), false, false);
  return __uint_as_float(t[0]) + dpp<ROR8>(__uint_as_float(t[1]));
}
__device__ __forceinline__ rsrc_t mk_rsrc(const void* p, int bytes) { return __builtin_amdgcn_make_buffer_rsrc((void*)p, 0, bytes, 0x00020000); }
__device__ __forceinline__ v4u ld_nt(rsrc_t r, unsigned voff, unsigned soff) { return __builtin_amdgcn_raw_buffer_load_b128(r, voff, soff, 2); }
#define SB() __builtin_amdgcn_sched_barrier(0)
#define BAR() asm volatile("s_waitcnt lgkmcnt(0)\n\ts_barrier" ::: "memory")
#define NO_LOADS() __builtin_amdgcn_s_waitcnt(0x0F70)

__device__ __forceinline__ void put(gu64* g, unsigned tag, unsigned v) { __hip_atomic_store(g, ((u64)tag << 32) | v, RLX_AGENT); }
__device__ __forceinline__ void put2(rsrc_t r, unsigned off, unsigned tag, float a, float b) {
  __builtin_amdgcn_raw_buffer_store_b128((v4u){__float_as_uint(a), tag, __float_as_uint(b), tag}, r, off, 0, 16);
}
__device__ __forceinline__ float get(gu64* g, unsigned tag) {
#pragma unroll 1
  for (unsigned spins = 0; spins < SPIN_CAP; ++spins) {
    const u64 x = __hip_atomic_load(g, RLX_AGENT);
    if (__all((unsigned)(x >> 32) == tag)) return __uint_as_float((unsigned)x);
    __builtin_amdgcn_s_sleep(16);
  }
  return __builtin_nanf("");
}
template <int N, int SLEEP> __device__ __forceinline__ void get2(rsrc_t r, unsigned off, int stride, unsigned tag, float& a, float& b) {
#pragma unroll 1
  for (unsigned spins = 0; spins < SPIN_CAP; ++spins) {
    asm volatile("" ::: "memory");
    v4u x[N];
#pragma unroll
    for (int k = 0; k < N; ++k) x[k] = __builtin_amdgcn_raw_buffer_load_b128(r, off, k * stride, 16);
    unsigned lo = tag, hi = tag;
#pragma unroll
    for (int k = 0; k < N; ++k) {
      lo = min(min(lo, x[k].y), x[k].w); hi = max(max(hi, x[k].y), x[k].w);
      a = k ? a + __uint_as_float(x[k].x) : __uint_as_float(x[k].x);
      b = k ? b + __uint_as_float(x[k].z) : __uint_as_float(x[k].z);
      asm volatile("" : "+v"(a), "+v"(b), "+v"(lo), "+v"(hi));
    }
    if (__builtin_amdgcn_ballot_w64(lo != hi) == 0) return;
    if (SLEEP) __builtin_amdgcn_s_sleep(SLEEP);
  }
  a = b = __builtin_nanf("");
}
__device__ __forceinline__ void post(LAS char* lds, int word, unsigned v, int lane) {
  asm volatile("" ::: "memory");
  if (lane == 0) *(volatile LAS unsigned*)(lds + L_FL + word * 4) = v;
  asm volatile("" ::: "memory");
}
__device__ __forceinline__ unsigned peek(LAS char* lds, int word) {
  return __builtin_amdgcn_readfirstlane(*(volatile LAS unsigned*)(lds + L_FL + word * 4));
}
template <int SLEEP> __device__ __forceinline__ void wait1(LAS char* lds, int word, unsigned want) {
  while (peek(lds, word) < want) { if (SLEEP) __builtin_amdgcn_s_sleep(SLEEP); }
  asm volatile("" ::: "memory");
}
template <int SLEEP, int N> __device__ __forceinline__ void waitn(LAS char* lds, int word, unsigned want) {
  for (;;) {
    const v4u v = *(volatile LAS v4u*)(lds + L_FL + word * 4);
    unsigned m = min(v.x, v.y);
    if (N == 4) m = min(m, min(v.z, v.w));
    if (__builtin_amdgcn_readfirstlane(m) >= want) break;
    if (SLEEP) __builtin_amdgcn_s_sleep(SLEEP);
  }
  asm volatile("" ::: "memory");
}
__device__ __forceinline__ int until(unsigned due) {
  int late;
  asm volatile("s_branch 2f\n1:\n\ts_sleep 1\n2:\n\ts_memrealtime vcc\n\ts_waitcnt lgkmcnt(0)\n\ts_lshl_b32 %0, vcc_lo, 4\n\t"
               "s_sub_i32 %0, %0, %1\n\ts_cmp_lt_i32 %0, 0\n\ts_cbranch_scc1 1b" : "=&s"(late) : "s"(due) : "vcc", "scc", "memory");
  return late;
}
struct RowMap { unsigned o[5]; };
__device__ __forceinline__ RowMap row_map(int lane) {
  RowMap m;
#pragma unroll
  for (int i = 0; i < 5; ++i) { const unsigned ci = i * 64 + lane, row = (ci * 52429u) >> 20; m.o[i] = row * (D * 2 - 320) + ci * 16; }
  asm volatile("" : "+v"(m.o[0]), "+v"(m.o[1]), "+v"(m.o[2]), "+v"(m.o[3]), "+v"(m.o[4]));
  return m;
}
__device__ __forceinline__ void stage_rows(gptr src, unsigned lds_dst, int ng, const RowMap& m, unsigned& due, unsigned& tau, unsigned dec) {
#pragma unroll 1
  for (int g = 0; g < ng; ++g) {
    unsigned keep;
    until(due); due += tau; tau -= dec;
    asm volatile("s_mov_b32 %0, m0\n\ts_mov_b32 m0, %7\n\ts_nop 0\n\tglobal_load_lds_dwordx4 %1, %6\n\t"
                 "s_add_u32 m0, m0, 0x400\n\ts_nop 0\n\tglobal_load_lds_dwordx4 %2, %6\n\t"
                 "s_add_u32 m0, m0, 0x400\n\ts_nop 0\n\tglobal_load_lds_dwordx4 %3, %6\n\t"
                 "s_add_u32 m0, m0, 0x400\n\ts_nop 0\n\tglobal_load_lds_dwordx4 %4, %6\n\t"
                 "s_add_u32 m0, m0, 0x400\n\ts_nop 0\n\tglobal_load_lds_dwordx4 %5, %6\n\ts_mov_b32 m0, %0"
                 : "=&s"(keep) : "v"(m.o[0]), "v"(m.o[1]), "v"(m.o[2]), "v"(m.o[3]), "v"(m.o[4]), "s"(src), "s"(lds_dst) : "scc", "memory");
    src += 16 * D * 2; lds_dst += 5120;
  }
}
__device__ __forceinline__ v4u tr_frag(const LAS char* p) {
  const v2u b0 = __builtin_bit_cast(v2u, __builtin_amdgcn_ds_read_tr16_b64_v4i16((LAS s4*)p));
  const v2u b1 = __builtin_bit_cast(v2u, __builtin_amdgcn_ds_read_tr16_b64_v4i16((LAS s4*)(p + 1280)));
  return (v4u){b0.x, b0.y, b1.x, b1.y};
}

extern "C" __global__ void __launch_bounds__(512, 2)
fused_decode(const u16* x_, const u16* kc_, const u16* wq_, const u16* wg_, char* ws_, const u16* wu_, const u16* vc_, const u16* wo_,
             const u16*  , u16*  ) {
  auto late_args = [&](u64& wd, u64& out) {
    v4u t;
    asm volatile("s_load_dwordx4 %0, %1, 0x40\n\ts_waitcnt lgkmcnt(0)" : "=s"(t) : "s"(__builtin_amdgcn_kernarg_segment_ptr()));
    wd = t.x | ((u64)t.y << 32); out = t.z | ((u64)t.w << 32);
  };
  SB();
  __shared__ __attribute__((aligned(16))) char lds_raw[L_TOTAL];
  LAS char* lds = (LAS char*)lds_raw;
  GAS char* ws = (GAS char*)ws_;
  const int tid = threadIdx.x, lane = tid & 63, wave = __builtin_amdgcn_readfirstlane(tid >> 6);
  const int w = blockIdx.x, h = (w & 7) * 4 + (w >> 6), mem = (w >> 3) & 7;
  const int ww = wave - 4;
  const int hc = (h + 4) & 31;
  u64 code0;
  {
    u64 pc; unsigned back;
    asm volatile("s_getpc_b64 %0\n.Lfd_here:\n\ts_mov_b32 %1, .Lfd_here-fused_decode" : "=s"(pc), "=s"(back));
    code0 = pc - back;
  }
  auto warm_code = [&](int who) {
    u64 ex; unsigned keep;
    const unsigned c0 = (unsigned)(tid - who * 64) * 4096u + (w >> 3) * 128u;
    asm volatile("v_cmp_gt_u32 vcc, .Lfd_last-fused_decode, %2\n\ts_and_saveexec_b64 %0, vcc\n\ts_cbranch_execz 1f\n\t"
                 "s_mov_b32 %1, m0\n\ts_mov_b32 m0, %4\n\ts_nop 0\n\tglobal_load_lds_dword %2, %3\n\ts_mov_b32 m0, %1\n"
                 "1:\n\ts_mov_b64 exec, %0"
                 : "=&s"(ex), "=&s"(keep) : "v"(c0), "s"(code0), "s"((unsigned)(size_t)lds + L_FL + W_DUMP * 4) : "vcc", "scc", "memory");
  };
  warm_code(0);
  const bool ffn = w >= 64, isq = mem < 6;
  const int fb = (w & 7) * 3 + (w >> 6) - 1;
  const int row0 = (mem & 1) * 320 + (lane >> 3), qm = (mem - 6) >> 31;
  unsigned rs8 = isq ? 8 * H * DH * 2 : 8 * F * 2;
  int jobw = wave + ((w >> 4) == 3 ? 4 : 0);
  unsigned woff = (row0 + wave * 80) * (rs8 >> 3) + (((h * 3 + (mem >> 1)) & qm) | (fb & ~qm)) * 128 + (lane & 7) * 16;
  unsigned xoff = (wave & 1) * (D * 2) + lane * 4;
  asm volatile("" : "+s"(rs8), "+s"(jobw));
  asm volatile("" : "+v"(woff), "+v"(xoff));
  SB();
  const bool job = jobw < 4;
  unsigned xr[5];
  v4u R[10];
  if (job) {
#pragma unroll
    for (int i = 0; i < 5; ++i) xr[i] = *(const GAS unsigned*)((gptr)x_ + xoff + 256 * i);
    SB();
    const rsrc_t wr = mk_rsrc(isq ? wq_ : wg_, 80 * rs8);
    unsigned so = 0;
#pragma unroll
    for (int i = 0; i < 10; ++i) { R[i] = ld_nt(wr, woff, so); so += rs8; SB(); }
  }
  if ((w >> 3) == 7 && wave == 0) {
    u64 wd_, out_;
    late_args(wd_, out_);
    const unsigned g = (lane >> 2) & 3, hg = (w & 7) * 4 + g, e = lane & 1;
    const unsigned okv = (((lane >> 1) & 1) * H + hg) * (SK * DH * 2) + e * (SK * DH * 2 - 4), owo = ((hg + 4) & 31) * (DH * D * 2) + e * (DH * D * 2 - 4);
    const unsigned off = (lane & 15) * (D * F * 2 / 16), ows = (e ? WS_END : 2048) + (w & 7) * 128;
    const unsigned oo = (w & 7) * 128 + 112;
    unsigned keep;
    asm volatile("s_mov_b32 %0, m0\n\ts_mov_b32 m0, %13\n\ts_nop 0\n\tglobal_load_lds_dword %1, %6 nt\n\tglobal_load_lds_dword %3, %9 nt\n\t"
                 "global_load_lds_dword %4, %11 nt\n\tglobal_load_lds_dword %1, %7 nt\n\tglobal_load_lds_dword %2, %8 nt\n\t"
                 "global_load_lds_dword %3, %10 nt\n\tglobal_load_lds_dword %5, %12 nt\n\ts_mov_b32 m0, %0"
                 : "=&s"(keep) : "v"(okv), "v"(owo), "v"(off), "v"(ows), "v"(oo), "s"(kc_), "s"(vc_), "s"(wo_), "s"(wu_), "s"(wd_), "s"(ws_), "s"(out_),
                   "s"((unsigned)(size_t)lds + L_PP) : "memory");
  }
  asm volatile("" :: "s"(x_), "s"(ws_) : "memory");
  const unsigned t0 = (unsigned)__builtin_amdgcn_s_memrealtime();
  const rsrc_t wsr = mk_rsrc(ws_, WS_END);
  unsigned tag;
  v4u msk;
  auto get_tag = [&]() { wait1<1>(lds, W_TAG + 1, 1); tag = peek(lds, W_TAG) + 1u; };
  auto meet = [&]() {
    unsigned probe = *(const __attribute__((address_space(4))) unsigned*)x_;
    const unsigned mm = (lane & 1) ? 0xffff0000u : 0x0000ffffu;
    const int q = (lane & 7) >> 1;
    msk = (v4u){q == 0 ? mm : 0u, q == 1 ? mm : 0u, q == 2 ? mm : 0u, q == 3 ? mm : 0u};
    asm volatile("" :: "s"(probe));
    asm volatile("" : "+v"(msk));
    BAR();
  };

  if (wave >= 4) {
    NO_LOADS();
    unsigned fin0 = t0 * 16u + (ORG + 63 * TAU);
    if (tid < 286) *(volatile LAS unsigned*)(lds + L_FL + lane * 4) = lane < W_PLAN || lane >= W_P ? 0u : fin0;
    meet();
    fin0 += max((int)(((unsigned)__builtin_amdgcn_s_memrealtime() - t0) * 16u) + OM - ORG, 0);
    if (ww == 0) post(lds, W_PLAN, fin0, lane);
    __builtin_amdgcn_s_setprio(2);
    constexpr int HSTR = SK * DH * 2, BSTR = H * HSTR;
    const rsrc_t kr = mk_rsrc((const char*)kc_ + (size_t)h * HSTR, BSTR + HSTR), vr = mk_rsrc((const char*)vc_ + (size_t)h * HSTR, BSTR + HSTR);
    auto group_off = [&](int g) { return ((g & 4) ? 0 : BSTR) + ((g + h) & 3) * (4 * STEP); };
    unsigned loff = (mem * 4 + ww) * 3072 + (lane >> 3) * 384 + (lane & 7) * 16;
    LAS char* flw = lds + L_FL + (W_PROG + ww) * 4;
    const LAS char* qp = lds + L_QO + (lane & 7) * 16;
    LAS float* sc = (LAS float*)(lds + L_SC) + 256 + ww * 8 + (lane >> 3) + (lane & 3) * 32 - (lane & 4) * 64;
    LAS char* part = lds + L_PART + ((ww * 2 + (lane >> 5)) * DH + (lane & 7) * 8 + ((lane >> 4) & 1) * 4) * 4;
    const LAS unsigned* pp = (const LAS unsigned*)(lds + L_PP) + ww * 8 + (lane >> 3);
    asm volatile("" : "+v"(loff), "+v"(flw), "+v"(qp), "+v"(sc), "+v"(part), "+v"(pp));
    unsigned due = fin0 - 63 * TAU + ww * (TAU / 4), fin = fin0;
    int adj = 0, step = TAU, slow = 0, late = 0, win;
    auto pace = [&](int lead = 0) {
      late = until(due - lead) - lead; due += step; fin += adj;
      win = min((unsigned)(late + ((WBASE - 4 + ww) << WSH)) >> (WSH + 2), WMAX - 1u);
      if (lead && late < 0) { win = 4; late = 0; }
      SB();
    };
    auto report = [&](unsigned prog) {
      asm volatile("s_mov_b64 exec, 1\n\tds_write2_b32 %0, %1, %2 offset1:12\n\ts_mov_b64 exec, -1" :: "v"(flw), "v"(prog), "v"(fin + late) : "memory");
    };
    auto gauge = [&]() {
      if (late >= 300) return late + 2 * TAU;
      asm volatile("s_waitcnt vmcnt(3)" ::: "memory");
      return (int)((unsigned)__builtin_amdgcn_s_memrealtime() * 16u - due) + 2 * step;
    };
    int back0 = 0;
    auto steer = [&](v2u pl, int g) {
      const int left = 61 - LEND - 4 * g, mul = g < 13 ? MUL : g == 13 ? 28 : g == 14 ? 51 : 256;
      const int sw = __builtin_amdgcn_readfirstlane(pl.x), sl = (sw & 1) * VSLOW;
      fin += 16 * (sl - slow); slow = sl;
      const int slope = left > 0 ? sw >> 1 : 0, gap = (int)(pl.y - fin) - slope * left;
      adj = __builtin_amdgcn_readfirstlane(min(max(slope + ((gap * mul) >> 8), -FAST), TAU / 2));
      step = TAU + adj + ((unsigned)(g - 7) < 4u ? slow : 0);
    };
    auto fetch = [&](rsrc_t r, int so, int j) {
      unsigned cnt;
      asm volatile("1:\n\ts_getreg_b32 %0, hwreg(HW_REG_IB_STS, 0, 4)\n\ts_cmp_gt_u32 %0, %1\n\ts_cbranch_scc1 1b" : "=&s"(cnt) : "s"(win) : "scc", "memory");
      const v4u x = ld_nt(r, loff + j * 128, so);
      SB();
      return x;
    };
    unsigned left_by_last = 0;
    if (ww == 0) left_by_last = __hip_atomic_load((GAS unsigned*)(ws + WS_TAG), RLX_AGENT);
    v4u U[NP + 4][3];
#pragma unroll
    for (int u = 0; u < NP + 4; ++u) {
      pace((NP + 3 - u) * LEAD);
#pragma unroll
      for (int j = 0; j < 3; ++j) U[u][j] = fetch(kr, group_off(u >> 2) + (u & 3) * STEP, j);
      if (u == 1 && tid == 256) *(volatile LAS v2u*)(lds + L_FL + W_TAG * 4) = (v2u){left_by_last, 1u};
      if (u & 1) report(u >> 1);
    }
    v4u q[3], q2[3];
    for (;;) {
      const v4u f = *(volatile LAS v4u*)(lds + L_FL + W_Q * 4);
#pragma unroll
      for (int j = 0; j < 3; ++j) { q[j] = *(volatile LAS v4u*)(qp + 384 + j * 128); q2[j] = *(volatile LAS v4u*)(qp + j * 128); }
      if (__builtin_amdgcn_readfirstlane(f.x & f.y & f.z)) break;
    }
#pragma unroll 1
    for (int g = 3; g < 9; ++g) {
      const rsrc_t r = g == 8 ? vr : kr;
      const int so = group_off(g);
      float pick = 0.f;
      v2u pl;
#pragma unroll
      for (int k = 0; k < 4; ++k) {
        if (k == 1) pl = *(volatile LAS v2u*)(lds + L_FL + W_SLOPE * 4);
        pace();
        float d = 0.f;
#pragma unroll
        for (int j = 0; j < 3; ++j) { d = dot8(U[NP + k][j], q[j], d); asm volatile("" : "+v"(d)); SB(); U[NP + k][j] = fetch(r, so + k * STEP, j); }
        if (k == 1) steer(pl, g); else if (k != 3) report(2 * g + (k >> 1));
        d = sum8(d);
        pick = (lane & 7) == k ? d : pick;
        if (g < 5) {
          const float e = sum8(dot8(U[k][2], q[2], dot8(U[k][1], q[1], dot8(U[k][0], q[0], 0.f))));
          pick = (lane & 7) == 4 + k ? e : pick;
#pragma unroll
          for (int j = 0; j < 3; ++j) U[k][j] = U[4 + k][j];
        }
        asm volatile("" : "+v"(pick));
        SB();
        if (k == 0 && g == 8) back0 = gauge();
      }
      if (g < 5 || !(lane & 4)) *sc = rbf(rbf(pick) * 0.07216878364870322f);
      sc += 128;
      if (g == 4) {
#pragma unroll
        for (int j = 0; j < 3; ++j) q[j] = q2[j];
      }
      SB();
    }
    f4 o[3];
#pragma unroll
    for (int j = 0; j < 3; ++j) o[j] = (f4){0.f, 0.f, 0.f, 0.f};
    auto vstep = [&](int g, bool more) {
      const int so = group_off(g);
      unsigned f = *(volatile LAS unsigned*)(lds + L_FL + W_P * 4), pv[4];
#pragma unroll
      for (int k = 0; k < 4; ++k) pv[k] = *(volatile LAS unsigned*)(pp + k * 32);
      v2u pl;
#pragma unroll
      for (int k = 0; k < 4; ++k) {
        if (k == 1 && more) pl = *(volatile LAS v2u*)(lds + L_FL + W_SLOPE * 4);
        if (more) pace();
        if (k == 0 && more && (unsigned)__builtin_amdgcn_readfirstlane(f) < (g < 13 ? 1u : 2u)) {
          wait1<0>(lds, W_P, g < 13 ? 1u : 2u);
#pragma unroll
          for (int i = 0; i < 4; ++i) pv[i] = *(volatile LAS unsigned*)(pp + i * 32);
        }
        const v4u a = msk & pv[k];
#pragma unroll
        for (int j = 0; j < 3; ++j) { o[j] = mfma(a, U[NP + k][j], o[j]); SB(); if (more) U[NP + k][j] = fetch(vr, so + k * STEP, j); }
        if (more) {
          if (k == 1) steer(pl, g); else if (k != 3) report(2 * g + (k >> 1));
          if (k == 0 && g == 9) post(lds, W_GROW + ww, min(max(gauge() - back0, -1000), 1000) + 0x10000, lane);
        }
        SB();
      }
      pp += 128;
    };
    auto vflush = [&](int ph) {
      if (((lane >> 3) & 1) == (lane >> 5)) {
#pragma unroll
        for (int j = 0; j < 3; ++j) *(LAS f4*)(part + ph * (8 * DH * 4) + j * 256) = o[j];
      }
    };
#pragma unroll 1
    for (int g = 9; g < 16; ++g) {
      vstep(g, true);
      if (g == 12) {
        vflush(0);
#pragma unroll
        for (int j = 0; j < 3; ++j) o[j] = (f4){0.f, 0.f, 0.f, 0.f};
      }
    }
    fin -= adj;
    report(P_OUT);
    vstep(16, false);
    vflush(1);
    report(P_DONE);
    __builtin_amdgcn_s_setprio(0);
  } else {
    meet();
    if (job) {
      float v[10], sum = 0.f;
#pragma unroll
      for (int i = 0; i < 5; ++i) { v[2 * i] = __uint_as_float(xr[i] << 16); v[2 * i + 1] = __uint_as_float(xr[i] & 0xffff0000u); sum += v[2 * i] + v[2 * i + 1]; }
      const float mean = wave_sum(sum) * (1.f / D);
      float q = 0.f;
#pragma unroll
      for (int i = 0; i < 10; ++i) { const float dd = v[i] - mean; q += dd * dd; }
      const float var = rbf(wave_sum(q) * (1.f / D)), mb = rbf(mean);
      const float inv = __builtin_amdgcn_rcpf(rbf(__builtin_amdgcn_sqrtf(rbf(var + 1e-5f))));
#pragma unroll
      for (int i = 0; i < 10; ++i) {
        const unsigned t = bfbits(rbf(v[i] - mb) * inv);
        *(LAS unsigned*)(lds + L_XX + ((lane + 64 * (i >> 1)) * 2 + (i & 1)) * 8 + (wave & 1) * 4) = t | (t << 16);
      }
      post(lds, W_LN + (wave & 1), 1, lane);
      waitn<0, 2>(lds, W_LN, 1);
#pragma unroll 1
      for (int pass = 0;; ++pass) {
        f4 a0 = {0.f, 0.f, 0.f, 0.f}, a1 = a0;
        const LAS char* xp = lds + L_XX + (pass ? mem * 80 + (lane >> 3) : row0 + wave * 80) * 8;
#pragma unroll
        for (int i = 0; i < 10; ++i) {
          const v2u xv = *(const LAS v2u*)(xp + i * 64);
          a0 = mfma(msk & xv.x, R[i], a0); a1 = mfma(msk & xv.y, R[i], a1);
        }
        const bool more = wave == 3 && ffn && pass == 0;
        if (more) {
          const rsrc_t wr = mk_rsrc(wu_, D * F * 2);
          const unsigned uoff = (mem * 80 + (lane >> 3)) * (F * 2) + fb * 128 + (lane & 7) * 16;
#pragma unroll
          for (int i = 0; i < 10; ++i) { R[i] = ld_nt(wr, uoff, i * (8 * F * 2)); SB(); }
        }
        const f4 s = {fold(a0[0], a1[0]), fold(a0[1], a1[1]), fold(a0[2], a1[2]), fold(a0[3], a1[3])};
        if ((lane & 8) == 0) *(LAS f4*)(lds + L_PJ + (wave * 2 + (lane >> 5)) * 256 + (lane & 7) * 32 + ((lane >> 4) & 1) * 16) = s;
        post(lds, W_PJ + wave, 1, lane);
        if (wave < 3) break;
        waitn<1, 4>(lds, W_PJ, 1);
        get_tag();
        const LAS float* pr = (const LAS float*)(lds + L_PJ) + lane;
        const float c0 = pr[384], c1 = pr[448];
        put2(wsr, WS_P + ((h * 16 + mem + 8 * pass) * 64 + lane) * 16, tag,
             pass ? c0 : (pr[0] + pr[128]) + (pr[256] + c0), pass ? c1 : (pr[64] + pr[192]) + (pr[320] + c1));
        if (!more) break;
      }
    }
    if (wave < 3) {
      const int ot = wave * 64 + lane;
      {
        wait1<4>(lds, W_PROG, T_Q);
        get_tag();
        float r0, r1;
        get2<2, 24>(wsr, WS_P + ((h * 16 + wave * 2) * 64 + lane) * 16, 1024, tag, r0, r1);
        LAS u16* dst = (LAS u16*)(lds + L_QO) + ot;
        dst[0] = (u16)bfbits(r0); dst[DH] = (u16)bfbits(r1);
        post(lds, W_Q + wave, 1, lane);
      }
      if (wave == 2) {
        if (ffn) {
          wait1<8>(lds, W_PROG, T_H);
          float gt[2], up[2];
          get2<2, 24>(wsr, WS_P + ((h * 16 + 6) * 64 + lane) * 16, 1024, tag, gt[0], gt[1]);
          get2<8, 24>(wsr, WS_P + ((h * 16 + 8) * 64 + lane) * 16, 1024, tag, up[0], up[1]);
#pragma unroll
          for (int b = 0; b < 2; ++b) {
            const float g = rbf(gt[b]), t2 = 2.f * 0.7978845608028654f * (g + 0.044715f * g * g * g);
            *(LAS u16*)(lds + L_HB + b * 128 + lane * 2) = (u16)bfbits(rbf(g * __builtin_amdgcn_rcpf(1.f + __expf(-t2))) * rbf(up[b]));
          }
        }
        post(lds, W_HID, 1, lane);
      } else if (wave == 0) {
        gu64* stbox = (gu64*)(ws + WS_ST) + h * 32;
#pragma unroll 1
        for (int ph = 0; ph < 2; ++ph) {
          waitn<4, 4>(lds, W_PROG, P_K1 + ph * 8);
          const f4 s0 = *(const LAS f4*)(lds + L_SC + ph * 2048 + lane * 32), s1 = *(const LAS f4*)(lds + L_SC + ph * 2048 + lane * 32 + 16);
          const float s[8] = {s0.x, s0.y, s0.z, s0.w, s1.x, s1.y, s1.z, s1.w};
          constexpr float L2E = 1.4426950408889634f;
          float m = s[0];
#pragma unroll
          for (int i = 1; i < 8; ++i) m = fmaxf(m, s[i]);
          m = wave_max(m);
          const float ms = m == -INFINITY ? 0.f : m;
          float l = 0.f;
#pragma unroll
          for (int i = 0; i < 8; ++i) l += __builtin_amdgcn_exp2f((s[i] - ms) * L2E);
          l = wave_sum(l);
          if (lane < 2) put(stbox + ph * 16 + mem * 2 + lane, tag, __float_as_uint(lane ? l : m));
          wait1<4>(lds, W_PROG, P_K1 + ph * 8 + T_ST);
          const float st = get(stbox + ph * 16 + (lane & 15), tag);
          const float oth = dpp<XOR1>(st), mj = (lane & 1) ? oth : st, lj = (lane & 1) ? st : oth;
          float M = fmaxf(mj, dpp<ROR2>(mj)); M = fmaxf(M, dpp<ROR4>(M)); M = fmaxf(M, dpp<ROR8>(M));
          float Ls = lj * __builtin_amdgcn_exp2f((mj - M) * L2E);
          Ls += dpp<ROR2>(Ls); Ls += dpp<ROR4>(Ls); Ls += dpp<ROR8>(Ls);
          const float inv = __builtin_amdgcn_rcpf(__uint_as_float(__builtin_amdgcn_readfirstlane(__float_as_uint(Ls))));
          unsigned p[8];
#pragma unroll
          for (int i = 0; i < 8; ++i) { const unsigned t = bfbits(__builtin_amdgcn_exp2f((s[i] - M) * L2E) * inv); p[i] = t | (t << 16); }
          *(LAS v4u*)(lds + L_PP + ph * 2048 + lane * 32) = (v4u){p[0], p[1], p[2], p[3]};
          *(LAS v4u*)(lds + L_PP + ph * 2048 + lane * 32 + 16) = (v4u){p[4], p[5], p[6], p[7]};
          post(lds, W_P, ph + 1, lane);
        }
      } else {
        const unsigned fin0 = peek(lds, W_PLAN);
        unsigned at = fin0 - CP0, plan = fin0;
        float prev = 0.f;
        int slope = 0, vs = 0;
        const unsigned half = (unsigned)__builtin_amdgcn_s_getreg(20 | (2 << 6) | (0 << 11));
#pragma unroll 1
        for (int c = 0; c < NCP; ++c) {
          at = __builtin_amdgcn_readfirstlane(at);
          until(at);
          const v4u e = *(volatile LAS v4u*)(lds + L_FL + W_EST * 4), bg = *(volatile LAS v4u*)(lds + L_FL + W_GROW * 4);
          const unsigned mine = e.x + (unsigned)(((int)(e.y - e.x) + (int)(e.z - e.x) + (int)(e.w - e.x)) >> 2);
          const int gs = (int)(bg.x + bg.y + bg.z + bg.w), gn = (gs + 0x8000) >> 16;
          const int grow = gn ? (int)((float)(gs - (gn << 16)) * 0.125f * __builtin_amdgcn_rcpf((float)gn)) + 128 : 0;
          gu64* cp = (gu64*)(ws + WS_CP) + c * 256;
          if (lane == 0 && (mem & 3) == 1) put(cp + (w >> 5) * 8 + (w & 7), tag, ((mine >> 2) << 10) | (grow << 2) | half | (peek(lds, W_P) ? 2u : 0u));
          unsigned look = at + (c == 3 ? CPW3 : c ? CPW : CPW0);
          u64 x; bool ok;
#pragma unroll 1
          for (int tr = c ? 2 : 0;; ++tr) {
            until(look);
            x = __hip_atomic_load(cp + lane, RLX_AGENT);
            ok = (unsigned)(x >> 32) == tag;
            if (tr == 2 || __all(ok)) break;
            look += CPW;
          }
          if (w == 0 && c == NCP - 1 && __all(ok && (x & 2))) {
            if (lane == 0) __hip_atomic_store((GAS unsigned*)(ws + WS_TAG), tag, RLX_AGENT);
            post(lds, W_MOVED, 1, lane);
          }
          const bool up = ok && (x & 1);
          const float sum = ok ? (float)(((int)((unsigned)x - ((fin0 >> 2) << 10)) >> 10) * 4) : 0.f;
          const float sa = wave_sum(sum), su = wave_sum(up ? sum : 0.f);
          const float na = (float)__builtin_popcountll(__builtin_amdgcn_ballot_w64(ok)), nu = (float)__builtin_popcountll(__builtin_amdgcn_ballot_w64(up));
          const float mean = sa * __builtin_amdgcn_rcpf(fmaxf(na, 1.f));
          const float hi = fmaxf(nu > 0.f ? su * __builtin_amdgcn_rcpf(nu) : mean, na > nu ? (sa - su) * __builtin_amdgcn_rcpf(na - nu) : mean);
          const float est = fmaxf(mean, hi - 0.015f * (float)(int)(plan - at));
          const bool first = c < 2;
          const unsigned before = plan;
          const float worst = wave_max(ok ? sum : -1e9f) - 600.f;
          if (first || __builtin_amdgcn_readfirstlane(est - (float)(int)(plan - fin0) > 100.f))
            plan = fin0 + __builtin_amdgcn_readfirstlane((int)fminf(fmaxf(
                first ? est + 80.f : est + (c == 3 ? 1.f : 0.5f) * fminf(fmaxf(est - prev, 0.f), 600.f) + 100.f, -8000.f), 16000.f));
          const bool hint = c == 1 && __builtin_amdgcn_readfirstlane(est - prev > 580.f);
          prev = est;
          if (!first) plan += __builtin_amdgcn_readfirstlane((int)(0.25f * fminf(fmaxf(worst - (float)(int)(plan - fin0), 0.f), 4000.f)));
          if (first) {
            const v4u e2 = *(volatile LAS v4u*)(lds + L_FL + W_EST * 4);
            const int own = min(min((int)(e2.x - plan), (int)(e2.y - plan)), min((int)(e2.z - plan), (int)(e2.w - plan)));
            const float left = (float)((int)(plan - look) + own - 2400) * (1.f / TAU) - (float)LEND;
            slope = (int)(-(float)own * __builtin_amdgcn_rcpf(fmaxf(left, 8.f)));
          }
          at = (c == 3 ? before : plan) - (c == 0 ? CP1 : c == 1 ? CP2 : c == 2 ? CP3 : CP4);
          const int gr = ok ? (int)(((unsigned)x >> 2) & 255u) : 0;
          if (c == 3 && !vs && __builtin_amdgcn_readfirstlane(wave_sum(gr ? (float)gr - 135.5f : 0.f) > 0.f) && (int)(before + 1000u - plan) > 0)
            plan = before + 1000u;
          if (hint) { vs = 1; plan += 16 * VSLOW; prev += (float)(16 * VSLOW); }
          if (lane == 0) *(volatile LAS v2u*)(lds + L_FL + W_SLOPE * 4) = (v2u){(unsigned)(slope * 2 + vs), plan};
        }
      }
      waitn<4, 4>(lds, W_PROG, P_V1 + 4);
      const LAS float* pr = (const LAS float*)(lds + L_PART) + ot;
      LAS u16* odst = (LAS u16*)(lds + L_QO) + (2 + (lane >> 5)) * DH + wave * 32 + (lane & 31);
      float s0 = 0.f;
#pragma unroll
      for (int r = 0; r < 8; ++r) s0 += pr[r * DH];
      unsigned oout = WS_O + ((h * 8 + mem) * DH + ot) * 16;
      unsigned oin = WS_O + ((hc * 8 + (lane >> 5) * 4) * DH + (mem & 1) * 96 + wave * 32 + (lane & 31)) * 16;
      asm volatile("" : "+v"(s0), "+v"(oout), "+v"(oin), "+v"(odst), "+v"(pr));
      wait1<2>(lds, W_PROG + 3, P_OUT);
      waitn<0, 4>(lds, W_PROG, P_DONE);
      const float s1 = ((pr[8 * DH] + pr[9 * DH]) + (pr[10 * DH] + pr[11 * DH])) + ((pr[12 * DH] + pr[13 * DH]) + (pr[14 * DH] + pr[15 * DH]));
      put2(wsr, oout, tag, s1, s0);
      __builtin_amdgcn_s_sleep(9);
      until(peek(lds, W_PLAN) + T_OG);
      float r0, r1;
      get2<4, 0>(wsr, oin, DH * 16, tag, r0, r1);
      auto t = __builtin_amdgcn_permlane32_swap(__float_as_uint(r0), __float_as_uint(r1), false, false);
      *odst = (u16)bfbits(__uint_as_float(t[0]) + __uint_as_float(t[1]));
      BAR();
      NO_LOADS();
      return;
    }
  }
  NO_LOADS();
  get_tag();
  const int oline = (w >> 6) * 8 + (w & 7), ob = oline >= 10, ocol = (oline - 10 * ob) * 64 + mem * 8;
  const int oi = ob * D + ocol + (lane & 7);
  float xres = 0.f;
  u64 wd_ = 0, out_ = 0;
  if (wave == 3) {
    late_args(wd_, out_);
    const unsigned lds0 = (unsigned)(size_t)lds;
    const RowMap rm = row_map(lane);
    wait1<8>(lds, W_HID, 1);
    wait1<8>(lds, W_PROG, P_V1 + 2);
    warm_code(3);
    if (oline < 20) xres = __uint_as_float((unsigned)((const GAS u16*)x_)[oi] << 16);
    asm volatile("" : "+v"(xres));
    wait1<2>(lds, W_PROG + 3, P_OUT - 1);
    const unsigned plan = *(volatile LAS unsigned*)(lds + L_FL + W_PLAN * 4), own = *(volatile LAS unsigned*)(lds + L_FL + (W_EST + 3) * 4);
    unsigned due = __builtin_amdgcn_readfirstlane(plan + min(max((int)(own - plan), 0), 1000)) + WO0, tau = ffn ? TAU_WO : TAU_WO * 4 / 3;
    const unsigned dec = ffn ? DEC_WO : DEC_WO * 2;
    until(__builtin_amdgcn_readfirstlane(own) + WO0);
    if (ffn) stage_rows((gptr)wd_ + ((fb * 64 + (mem & 1) * 32) * (D * 2) + (mem >> 1) * 320), lds0 + L_WD, 2, rm, due, tau, dec);
    stage_rows((gptr)wo_ + ((hc * DH + (mem & 1) * 96) * (D * 2) + (mem >> 1) * 320), lds0 + L_WO, 6, rm, due, tau, dec);
  }
  const int tt = wave - 3;
  const LAS char* ap = lds + L_QO + 768 + ((lane >> 2) & 1) * 384 + (lane >> 4) * 16;
  const LAS char* bp = lds + L_WO + ((lane >> 4) * 8 + ((lane >> 2) & 3)) * 320 + tt * 64 + (lane & 3) * 8;
  const LAS char* hp = lds + L_HB + ((lane >> 2) & 1) * 128 + (mem & 1) * 64 + (lane >> 4) * 16;
  unsigned ydst = WS_Y + (((((mem >> 1) * 5 + tt) * 4 + (lane >> 4)) * 64 + hc * 2 + (mem & 1)) * 16 + (lane & 15)) * 16;
  asm volatile("s_waitcnt vmcnt(10)" : "+v"(bp), "+v"(hp), "+v"(ap), "+v"(ydst) :: "memory");
  if (wave == 3) post(lds, W_STAGED, 1, lane);
  wait1<0>(lds, W_STAGED, 1);
  v4u bf[6];
#pragma unroll
  for (int i = 0; i < 4; ++i) bf[i] = tr_frag(bp + (i >> 1) * 10240 + (i & 1) * 32);
  const f4 zero = {0.f, 0.f, 0.f, 0.f};
  const v4u hf = *(const LAS v4u*)hp;
  const f4 g0 = mfma(hf, tr_frag(bp + L_WD), zero), g1 = mfma(hf, tr_frag(bp + L_WD + 32), zero);
  float gx = !ffn ? 0.f : lane < 32 ? g0.x : g1.x;
  asm volatile("s_waitcnt vmcnt(0)" : "+v"(ap), "+v"(ydst), "+v"(gx), "+v"(bf[0]), "+v"(bf[1]), "+v"(bf[2]), "+v"(bf[3]) :: "memory");
  BAR();
  {
    v4u a[3];
#pragma unroll
    for (int ks = 0; ks < 3; ++ks) a[ks] = *(const LAS v4u*)(ap + ks * 64);
    bf[4] = tr_frag(bp + 20480); bf[5] = tr_frag(bp + 20480 + 32);
    SB();
    f4 y0 = zero, y1 = zero;
#pragma unroll
    for (int ks = 0; ks < 3; ++ks) { y0 = mfma(a[ks], bf[2 * ks], y0); y1 = mfma(a[ks], bf[2 * ks + 1], y1); }
    put2(wsr, ydst, tag, lane < 32 ? y0.x : y1.x, gx);
  }
  if (wave == 3 && oline < 20) {
    unsigned ysrc = WS_Y + ((ocol >> 4) * 2 + ob) * (64 * 16 * 16) + (mem & 1) * 128 + (lane >> 3) * 256 + (lane & 7) * 16;
    GAS u16* op = (GAS u16*)out_ + oi;
    asm volatile("" : "+v"(ysrc), "+v"(op));
    const unsigned moved = peek(lds, W_MOVED);
    __builtin_amdgcn_s_sleep(6);
    until(peek(lds, W_PLAN) + T_YG);
    float s, gs;
    get2<8, 0>(wsr, ysrc, 2048, tag, s, gs);
    s += dpp<ROR8>(s); gs += dpp<ROR8>(gs);
    s = rbf(xrow_sum(s)); gs = rbf(xrow_sum(gs));
    const float gate = rbf(gs * __builtin_amdgcn_rcpf(1.f + __expf(-gs)));
    if (lane < 8) *op = (u16)bfbits(xres + rbf(s * gate));
    if (w == 0 && !moved && lane == 0) __hip_atomic_store((GAS unsigned*)(ws + WS_TAG), tag, RLX_AGENT);
  }
  asm volatile(".Lfd_last:");
}

extern "C" void kernel_launch(void* const* d_in, const int* in_sizes, int n_in, void* d_out, int out_size, void* d_ws, size_t ws_size,
                              hipStream_t stream) {
  fused_decode<<<256, NTHREADS, 0, stream>>>((const u16*)d_in[0], (const u16*)d_in[1], (const u16*)d_in[3], (const u16*)d_in[6], (char*)d_ws,
                                             (const u16*)d_in[7], (const u16*)d_in[2], (const u16*)d_in[4], (const u16*)d_in[8], (u16*)d_out);
}
```
